```python
import torch

def _softmax(x, axis=-1):
    return torch.softmax(x, dim=axis)
def _gelu(x):
    return torch.nn.functional.gelu(x, approximate="tanh")
def _silu(x):
    return torch.nn.functional.silu(x)
def _sigmoid(x):
    return torch.sigmoid(x)
def _layernorm(x, axis=-1, eps=1e-5):
    m = x.mean(dim=axis, keepdim=True)
    v = x.var(dim=axis, keepdim=True, unbiased=False)
    return (x - m) / torch.sqrt(v + eps)
def _scatter_add(tgt, idx, src, axis=0):
    return tgt.clone().index_add_(axis, idx, src)
def _segment_sum(src, seg, n):
    out = torch.zeros((n,) + src.shape[1:], dtype=src.dtype)
    return out.index_add_(0, seg, src)
def _pad(x, pw):
    flat = tuple(v for pair in reversed(pw) for v in pair)
    return torch.nn.functional.pad(x, flat)

kernel_name = 'gated_mix__softmax_full_mha__linear_kernel_hedgehog__prompt'

def setup_inputs():

    _w = (lambda a: (lambda t: t.to(torch.bfloat16) if t.is_floating_point() else t)(torch.as_tensor(a)))
    def _A_setup():
        B, S, D = 1, 2048, 640
        return {
            "x": _w((lambda *s: torch.randn(*s, dtype=torch.bfloat16) * 0.05)(B, S, D)),
            "wqkv": _w((lambda *s: torch.randn(*s, dtype=torch.bfloat16) * 0.8)(D, 3 * D)),
            "wo": _w((lambda *s: torch.randn(*s, dtype=torch.bfloat16) * 0.05)(D, D)),
        }
    _A_in = _A_setup()

    def _B_setup():
        B, S, D, H = 1, 2048, 640, 32
        Dh = D // H
        return {
            "x": _w((lambda *s: torch.randn(*s, dtype=torch.bfloat16) * 0.05)(B, S, D)),
            "wqkv": _w((lambda *s: torch.randn(*s, dtype=torch.bfloat16) * 1.6)(D, 3 * D)),
            "w_phi": _w((lambda *s: torch.randn(*s, dtype=torch.bfloat16) * 1.6)(H, Dh, Dh) * (Dh ** -0.5)),
            "wo": _w((lambda *s: torch.randn(*s, dtype=torch.bfloat16) * 0.05)(D, D)),
        }
    _B_in = _B_setup()

    return {
        "A_x": _A_in["x"],
        "A_wqkv": _A_in["wqkv"],
        "A_wo": _A_in["wo"],
        "B_x": _B_in["x"],
        "B_wqkv": _B_in["wqkv"],
        "B_w_phi": _B_in["w_phi"],
        "B_wo": _B_in["wo"],
        "gate_w": _w((lambda *s: torch.randn(*s, dtype=torch.bfloat16) * 0.05)(640) * 16.0),
    }

def reference(A_x, A_wqkv, A_wo, B_x, B_wqkv, B_w_phi, B_wo, gate_w):
    def _A_ref(x, wqkv, wo):
        B, S, D = x.shape
        H = 32; Dh = D // H
        qkv = (lambda x, *d: x.permute(*d))((x @ wqkv).reshape(B, S, 3, H, Dh), 2, 0, 3, 1, 4)
        q, k, v = qkv[0], qkv[1], qkv[2]
        scale = Dh ** -0.5
        scores = torch.einsum("bhqd,bhkd->bhqk", q, k) * scale
        p = _softmax(scores, axis=-1)
        o = torch.einsum("bhqk,bhkd->bhqd", p, v)
        return (lambda x, *d: x.permute(*d))(o, 0, 2, 1, 3).reshape(B, S, D) @ wo

    def _B_ref(x, wqkv, w_phi, wo):
        B, S, D = x.shape
        H = 32; Dh = D // H
        qkv = (lambda x, *d: x.permute(*d))((x @ wqkv).reshape(B, S, 3, H, Dh), 2, 0, 3, 1, 4)
        q, k, v = qkv[0], qkv[1], qkv[2]
        pq = torch.einsum("bhsd,hdf->bhsf", q, w_phi)
        pk = torch.einsum("bhsd,hdf->bhsf", k, w_phi)
        phi_q = torch.concatenate([_softmax(pq, axis=-1), _softmax(-pq, axis=-1)], axis=-1)
        phi_k = torch.concatenate([_softmax(pk, axis=-1), _softmax(-pk, axis=-1)], axis=-1)
        kv = torch.einsum("bhsf,bhsd->bhfd", phi_k, v)
        z  = torch.einsum("bhsf->bhf", phi_k)
        num = torch.einsum("bhqf,bhfd->bhqd", phi_q, kv)
        den = torch.einsum("bhqf,bhf->bhq", phi_q, z)[..., None] + 1e-6
        o = num / den
        return (lambda x, *d: x.permute(*d))(o, 0, 2, 1, 3).reshape(B, S, D) @ wo

    x = A_x
    g = _sigmoid((x * gate_w).sum(-1, keepdims=True))
    a = _A_ref(x, A_wqkv, A_wo)
    b = _B_ref(x, B_wqkv, B_w_phi, B_wo)
    return g * a + (1 - g) * b
```

```cpp
#include <hip/hip_runtime.h>
#include <hip/hip_bf16.h>
#include <stdint.h>

typedef __attribute__((ext_vector_type(8))) __bf16 bf16x8;
typedef __attribute__((ext_vector_type(16))) __bf16 bf16x16;
typedef __attribute__((ext_vector_type(4))) short s16x4;
typedef __attribute__((ext_vector_type(8))) short s16x8;
typedef __attribute__((ext_vector_type(4))) float f32x4;
typedef __attribute__((ext_vector_type(8))) float f32x8;
typedef __attribute__((ext_vector_type(16))) float f32x16;
typedef __attribute__((ext_vector_type(4))) unsigned u32x4;
typedef __attribute__((ext_vector_type(2))) unsigned u32x2;
typedef __attribute__((address_space(3))) const char* lds_cptr;
typedef __attribute__((address_space(3))) char* lds_ptr;

constexpr int SEQ = 2048, DM = 640, NH = 32;
constexpr float LOG2E = 1.4426950408889634f;
constexpr float QSCALE = 0.22360679774997896f * 1.4426950408889634f;

constexpr int KVT = 2816;
constexpr int QFT = 1280;
constexpr size_t WS_KV = 0;
constexpr size_t WS_QA = WS_KV + (size_t)NH * 64 * KVT;
constexpr size_t WS_QB = WS_QA + (size_t)NH * 64 * QFT;
constexpr size_t WS_KB = WS_QB + (size_t)NH * 64 * QFT;
constexpr size_t WS_VB = WS_KB + (size_t)NH * 64 * QFT;
constexpr size_t WS_WOP = WS_VB + (size_t)NH * SEQ * 48;
constexpr size_t WS_OC = WS_WOP + (size_t)160 * 640 * 16;
constexpr size_t WS_KMAX = WS_OC + (size_t)160 * SEQ * 16;
constexpr size_t WS_G = WS_KMAX + (size_t)NH * 64 * 4;
constexpr size_t WS_WPH = WS_G + (size_t)SEQ * 4;
constexpr size_t WS_KVP = WS_WPH + (size_t)NH * 1536;
constexpr size_t WS_FLAG = WS_KVP + (size_t)NH * 8 * 3840;
constexpr size_t WS_END = WS_FLAG + (size_t)NH * 128;

typedef __attribute__((ext_vector_type(2))) __bf16 bf16x2;
typedef __attribute__((ext_vector_type(2))) float f32x2;
__device__ __forceinline__ unsigned cvtpk(float lo, float hi) {
    return __builtin_bit_cast(unsigned, __builtin_convertvector(f32x2{lo, hi}, bf16x2)); }
__device__ __forceinline__ float bflo(unsigned w) { return __uint_as_float(w << 16); }
__device__ __forceinline__ float bfhi(unsigned w) { return __uint_as_float(w & 0xffff0000u); }
__device__ __forceinline__ void glds16(const void* sbase, unsigned voff, unsigned lds_base) {
    unsigned sv;
    asm volatile("s_mov_b32 %0, m0\n\ts_mov_b32 m0, %3\n\ts_nop 0\n\tglobal_load_lds_dwordx4 %1, %2\n\ts_mov_b32 m0, %0"
                 : "=&s"(sv) : "v"(voff), "s"(sbase), "s"(lds_base) : "memory"); }
__device__ __forceinline__ s16x4 tr_read(lds_cptr p) {
    return __builtin_amdgcn_ds_read_tr16_b64_v4i16((__attribute__((address_space(3))) s16x4*)p); }
__device__ __forceinline__ bf16x8 cat8(s16x4 a, s16x4 b) {
    s16x8 r = {a[0], a[1], a[2], a[3], b[0], b[1], b[2], b[3]}; return __builtin_bit_cast(bf16x8, r); }
#define LDS_B128(p) (*(const __attribute__((address_space(3))) bf16x8*)(p))
#define LDS_B64(p) (*(const __attribute__((address_space(3))) s16x4*)(p))
#define MFMA16(a, b, c) __builtin_amdgcn_mfma_f32_16x16x32_bf16(a, b, c, 0, 0, 0)
#define MFMA32(a, b, c) __builtin_amdgcn_mfma_f32_32x32x16_bf16(a, b, c, 0, 0, 0)
#define MFMA32K8(a, b, c) __builtin_amdgcn_mfma_f32_32x32x8bf16_1k(a, b, c, 0, 0, 0)
#define WAIT_VM(n) asm volatile("s_waitcnt vmcnt(" #n ")" ::: "memory")
#define RAW_BAR() do { asm volatile("s_waitcnt lgkmcnt(0)\n\ts_barrier" ::: "memory"); } while (0)
__device__ __forceinline__ float xhalf_max(float v) {
    auto r = __builtin_amdgcn_permlane32_swap(__float_as_uint(v), __float_as_uint(v), false, false);
    return fmaxf(__uint_as_float(r[0]), __uint_as_float(r[1])); }
__device__ __forceinline__ float xhalf_min(float v) {
    auto r = __builtin_amdgcn_permlane32_swap(__float_as_uint(v), __float_as_uint(v), false, false);
    return fminf(__uint_as_float(r[0]), __uint_as_float(r[1])); }
__device__ __forceinline__ float xhalf_sum(float v) {
    auto r = __builtin_amdgcn_permlane32_swap(__float_as_uint(v), __float_as_uint(v), false, false);
    return __uint_as_float(r[0]) + __uint_as_float(r[1]); }

__device__ __forceinline__ void oc_map(int c, int e, int& br, int& d) {
    const int e4 = e >> 2, e3 = e & 3;
    if (c == 0) { br = 0; d = 8 * e4 + e3; }
    else if (c == 1) { br = e4; d = 16 + e3; }
    else if (c == 2) { br = 1; d = 8 * e4 + e3; }
    else if (c == 3) { br = 0; d = 4 + 8 * e4 + e3; }
    else { br = 1; d = 4 + 8 * e4 + e3; }
}

constexpr int K1_STAGE = 49152;
__global__ __launch_bounds__(768) void k1_qkv(const uint16_t* __restrict__ x, const uint16_t* __restrict__ wA, const uint16_t* __restrict__ wB,
                                              const uint16_t* __restrict__ woA, const uint16_t* __restrict__ woB,
                                              const uint16_t* __restrict__ wphi, const uint16_t* __restrict__ gatew, char* __restrict__ ws) {
    __shared__ __attribute__((aligned(16))) char lds[3 * K1_STAGE];
    const int bid = blockIdx.x, xcd = bid & 7, bi = bid >> 3;
    const int rt = (xcd & 1) * 8 + (bi & 7), cg = (xcd >> 1) * 4 + (bi >> 3);
    const int tid = threadIdx.x, lane = tid & 63;
    const int wid = __builtin_amdgcn_readfirstlane(tid >> 6);
    const unsigned lds0 = (unsigned)(uintptr_t)lds;
    const int rot = (((bi >> 3) * 5) / 2 + (bi & 7) * 4) % 10;

    const char* sbase; unsigned sstep; unsigned voff[4]; unsigned ldst;
    if (wid < 4) {
        sbase = (const char*)x + (size_t)rt * 128 * 1280; sstep = 128; ldst = wid * 4096;
#pragma unroll
        for (int j = 0; j < 4; ++j) { const int row = 8 * (wid * 4 + j) + (lane >> 3); voff[j] = row * 1280 + (((lane & 7) ^ (row & 7)) << 4); }
    } else {
        sbase = (const char*)(cg < 8 ? wA : wB) + (cg & 7) * 480; sstep = 64 * 3840; ldst = 16384 + (wid - 4) * 4096;
#pragma unroll
        for (int j = 0; j < 4; ++j) {
            const int p = (wid - 4) * 4 + j, b = p >> 3; int q = (p & 7) * 64 + lane, row, ch;
            if (q < 248) { q = q < 239 ? q : 239; row = q / 30; ch = q - row * 30; }
            else { q -= 248; q = q < 239 ? q : 239; row = q / 30; ch = q - row * 30; row += 8; }
            voff[j] = (16 * b + row) * 3840 + ch * 16;
        }
    }
    auto stage = [&](int s) {
        int kc = s + rot; kc = kc >= 10 ? kc - 10 : kc;
        const char* sb = sbase + (size_t)kc * sstep;
        const unsigned dst = lds0 + (s % 3) * K1_STAGE + ldst;
#pragma unroll
        for (int j = 0; j < 4; ++j) glds16(sb, voff[j], dst + j * 1024);
    };
    stage(0); stage(1);

    unsigned gx[5], gw[5];
    const int gtok = rt * 128 + cg * 8 + wid;
#pragma unroll
    for (int j = 0; j < 5; ++j) { gx[j] = 0; gw[j] = 0; }
    if (wid < 8) {
#pragma unroll
        for (int j = 0; j < 5; ++j) {
            const void* px = (const char*)x + (size_t)gtok * 1280 + (lane + 64 * j) * 4;
            const void* pw = (const char*)gatew + (lane + 64 * j) * 4;
            asm volatile("global_load_dword %0, %1, off" : "+v"(gx[j]) : "v"(px));
            asm volatile("global_load_dword %0, %1, off" : "+v"(gw[j]) : "v"(pw));
        }
    }
    unsigned wo[8];
    const int item = bid * 400 + tid, wch = item / 640, wn = item - wch * 640;
#pragma unroll
    for (int e = 0; e < 8; ++e) wo[e] = 0;
    if (tid < 400) {
        const int h = wch / 5, c = wch - h * 5;
#pragma unroll
        for (int e = 0; e < 8; ++e) {
            int br, d; oc_map(c, e, br, d);
            const void* p = (br ? woB : woA) + (size_t)(h * 20 + d) * 640 + wn;
            asm volatile("global_load_ushort %0, %1, off" : "+v"(wo[e]) : "v"(p));
        }
    }

    const int tg = wid & 3, sg = wid >> 2;
    const int g = lane >> 4, l15 = lane & 15, q4 = l15 >> 2, p4 = lane & 3;
    const int xrow = 32 * tg + l15;
    const unsigned xa0 = xrow * 128 + ((g ^ (xrow & 7)) << 4), xa1 = xa0 ^ 64;
    const unsigned wa = 16384 + 8192 * (g >> 1) + 480 * (8 * (g & 1) + q4) + 128 * (g & 1) + 160 * sg + 40 * p4;
    f32x4 acc[2][5];
#pragma unroll
    for (int m = 0; m < 2; ++m)
#pragma unroll
        for (int i = 0; i < 5; ++i) acc[m][i] = f32x4{0.f, 0.f, 0.f, 0.f};

    for (int s = 0; s < 10; ++s) {
        if (s == 0) { WAIT_VM(22); }
        else if (s < 9) { WAIT_VM(4); } else { WAIT_VM(0); }
        if (s == 0) {
            if (wid >= 8) { WAIT_VM(4); } else if (wid == 7) { WAIT_VM(14); }
        }
        RAW_BAR();
        if (s + 2 < 10) stage(s + 2);
        const lds_cptr buf = (lds_cptr)lds + (s % 3) * K1_STAGE;
#pragma unroll
        for (int ks = 0; ks < 2; ++ks) {
            bf16x8 xf[2], wf[5];
#pragma unroll
            for (int m = 0; m < 2; ++m) xf[m] = LDS_B128(buf + (ks ? xa1 : xa0) + m * 2048);
#pragma unroll
            for (int i = 0; i < 5; ++i) wf[i] = cat8(tr_read(buf + wa + ks * 16384 + i * 8), tr_read(buf + wa + ks * 16384 + i * 8 + 1920));
#pragma unroll
            for (int i = 0; i < 5; ++i)
#pragma unroll
                for (int m = 0; m < 2; ++m) acc[m][i] = MFMA16(wf[i], xf[m], acc[m][i]);
        }
    }

    const int gg = 3 * (cg & 7) + sg, type = gg >> 3, head = 4 * (gg & 7) + g, br = cg >> 3;
    float kn = 0.f;
#pragma unroll
    for (int m = 0; m < 2; ++m) {
        const int tok = rt * 128 + 32 * tg + 16 * m + l15, tile = tok >> 5, r32 = tok & 31;
        const float sc = (type == 0 && br == 0) ? QSCALE : 1.0f;
        unsigned w[10];
#pragma unroll
        for (int i = 0; i < 5; ++i) { w[2 * i] = cvtpk(acc[m][i][0] * sc, acc[m][i][1] * sc); w[2 * i + 1] = cvtpk(acc[m][i][2] * sc, acc[m][i][3] * sc); }
        if (type < 2) {
            char* dst;
            if (br == 0) dst = type == 0 ? ws + WS_QA + ((size_t)head * 64 + tile) * QFT : ws + WS_KV + ((size_t)head * 64 + tile) * KVT;
            else dst = ws + (type == 0 ? WS_QB : WS_KB) + ((size_t)head * 64 + tile) * QFT;
            *(u32x4*)(dst + r32 * 16) = u32x4{w[0], w[1], w[2], w[3]};
            *(u32x4*)(dst + 512 + r32 * 16) = u32x4{w[4], w[5], w[6], w[7]};
            *(u32x2*)(dst + 1024 + r32 * 8) = u32x2{w[8], w[9]};
            if (type == 1 && br == 0) {
                float n2 = 0.f;
#pragma unroll
                for (int i = 0; i < 5; ++i)
#pragma unroll
                    for (int r = 0; r < 4; ++r) n2 = fmaf(acc[m][i][r], acc[m][i][r], n2);
                kn = fmaxf(kn, n2);
                if (!(n2 == n2)) kn = __builtin_inff();
            }
        } else {
            char* dst = br == 0 ? ws + WS_KV + ((size_t)head * 64 + tile) * KVT + 1280 + r32 * 48 : ws + WS_VB + ((size_t)head * SEQ + tok) * 48;
            *(u32x4*)(dst) = u32x4{w[0], w[1], w[2], w[3]};
            *(u32x4*)(dst + 16) = u32x4{w[4], w[5], w[6], w[7]};
            *(u32x4*)(dst + 32) = u32x4{w[8], w[9], 0x00003f80u, 0u};
        }
    }
    if (type == 1 && br == 0) {
#pragma unroll
        for (int o = 1; o < 16; o <<= 1) kn = fmaxf(kn, __shfl_xor(kn, o));
        if (l15 == 0) ((float*)(ws + WS_KMAX))[head * 64 + rt * 4 + tg] = kn;
    }
    asm volatile("" : "+v"(gx[0]), "+v"(gx[1]), "+v"(gx[2]), "+v"(gx[3]), "+v"(gx[4]), "+v"(gw[0]), "+v"(gw[1]), "+v"(gw[2]), "+v"(gw[3]), "+v"(gw[4]));
    asm volatile("" : "+v"(wo[0]), "+v"(wo[1]), "+v"(wo[2]), "+v"(wo[3]), "+v"(wo[4]), "+v"(wo[5]), "+v"(wo[6]), "+v"(wo[7]));
    if (wid < 8) {
        float d = 0.f;
#pragma unroll
        for (int j = 0; j < 5; ++j) { d = fmaf(bflo(gx[j]), bflo(gw[j]), d); d = fmaf(bfhi(gx[j]), bfhi(gw[j]), d); }
#pragma unroll
        for (int o = 1; o < 64; o <<= 1) d += __shfl_xor(d, o);
        if (lane == 0) ((float*)(ws + WS_G))[gtok] = 1.0f / (1.0f + __expf(-d));
    }
    if (tid < 400)
        *(u32x4*)(ws + WS_WOP + (size_t)item * 16) = u32x4{wo[0] | (wo[1] << 16), wo[2] | (wo[3] << 16), wo[4] | (wo[5] << 16), wo[6] | (wo[7] << 16)};
    if (wid == 11 && bid < 32) {
        const int m = lane & 31, hh = lane >> 5, hp = (m >> 2) & 1, r = (m & 3) + 4 * (m >> 3), f = 10 * hp + r;
        const uint16_t* wp = wphi + bid * 400;
        unsigned v[12];
#pragma unroll
        for (int j = 0; j < 8; ++j) v[j] = r < 10 ? wp[(8 * hh + j) * 20 + f] : 0;
#pragma unroll
        for (int j = 0; j < 4; ++j) v[8 + j] = (r < 10 && hh == 0) ? wp[(16 + j) * 20 + f] : 0;
        char* dst = ws + WS_WPH + bid * 1536;
        *(u32x4*)(dst + lane * 16) = u32x4{v[0] | (v[1] << 16), v[2] | (v[3] << 16), v[4] | (v[5] << 16), v[6] | (v[7] << 16)};
        *(u32x2*)(dst + 1024 + lane * 8) = u32x2{v[8] | (v[9] << 16), v[10] | (v[11] << 16)};
        if (lane < 32) ((unsigned*)(ws + WS_FLAG))[bid * 32 + lane] = 0u;
    }
}

constexpr int SLOT = 8 * KVT;
constexpr int L_VB = 5 * SLOT;
constexpr int L_PHI = L_VB + 12288;
constexpr int L_MISC = 7 * SLOT;
constexpr int L_FLAG = L_MISC + 3072;
constexpr int K2_LDS = 163840;

__device__ __forceinline__ void attn_chunk(lds_cptr base, unsigned ka, unsigned kb, unsigned va, const bf16x8& q0, const s16x4& q1,
                                           const f32x16& negb, f32x16& o) {
#pragma unroll 2
    for (int t = 0; t < 8; ++t) {
        const lds_cptr tb = base + t * KVT;
        const bf16x8 k0 = LDS_B128(tb + ka);
        const s16x4 k1 = LDS_B64(tb + kb);
        const bf16x8 v0 = cat8(tr_read(tb + va), tr_read(tb + va + 192));
        const bf16x8 v1 = cat8(tr_read(tb + va + 768), tr_read(tb + va + 960));
        f32x16 s = MFMA32(k0, q0, negb);
        s = MFMA32K8(k1, q1, s);
        f32x8 p0, p1;
#pragma unroll
        for (int r = 0; r < 8; ++r) { p0[r] = __builtin_amdgcn_exp2f(s[r]); p1[r] = __builtin_amdgcn_exp2f(s[8 + r]); }
        o = __builtin_amdgcn_smfmac_f32_32x32x32_bf16(v0, __builtin_bit_cast(bf16x16, p0), o, 0xDDDD, 0, 0);
        o = __builtin_amdgcn_smfmac_f32_32x32x32_bf16(v1, __builtin_bit_cast(bf16x16, p1), o, 0xDDDD, 0, 0);
    }
}

__global__ __launch_bounds__(1024) void k2_attn(char* __restrict__ ws) {
    __shared__ __attribute__((aligned(16))) char lds[K2_LDS];
    const int bid = blockIdx.x, xcd = bid & 7, bi = bid >> 3;
    const int h = xcd * 4 + (bi >> 3), qt = bi & 7;
    const int tid = threadIdx.x, lane = tid & 63, r32 = lane & 31, hh = lane >> 5;
    const int wid = __builtin_amdgcn_readfirstlane(tid >> 6);
    const int qs = wid & 7, kh = wid >> 3;
    const unsigned lds0 = (unsigned)(uintptr_t)lds;
    const int tile = qt * 8 + qs;
    const char* kvbase = ws + WS_KV + (size_t)h * 64 * KVT;

    u32x4 qa0, pf0, wp0; u32x2 qa1, pf1, wp1; float kmx;
    {
        const char* pq = ws + WS_QA + ((size_t)h * 64 + tile) * QFT;
        const char* pp = ws + (kh ? WS_QB : WS_KB) + ((size_t)h * 64 + tile) * QFT;
        const char* pw = ws + WS_WPH + h * 1536;
        const unsigned o16 = lane * 16, o8 = 1024 + r32 * 8, o8w = 1024 + lane * 8;
        asm volatile("global_load_dwordx4 %0, %1, %2" : "=v"(pf0) : "v"(o16), "s"(pp));
        asm volatile("global_load_dwordx2 %0, %1, %2" : "=v"(pf1) : "v"(o8), "s"(pp));
        asm volatile("global_load_dwordx4 %0, %1, %2" : "=v"(wp0) : "v"(o16), "s"(pw));
        asm volatile("global_load_dwordx2 %0, %1, %2" : "=v"(wp1) : "v"(o8w), "s"(pw));
        asm volatile("global_load_dwordx4 %0, %1, %2" : "=v"(qa0) : "v"(o16), "s"(pq));
        asm volatile("global_load_dwordx2 %0, %1, %2" : "=v"(qa1) : "v"(o8), "s"(pq));
        const char* pk = ws + WS_KMAX + h * 256; const unsigned o4 = lane * 4;
        asm volatile("global_load_dword %0, %1, %2" : "=v"(kmx) : "v"(o4), "s"(pk));
    }
    const unsigned l16 = lane * 16;
    auto load_chunk = [&](int c, int slot) {
        const char* sb = kvbase + (size_t)c * SLOT;
        glds16(sb + wid * 1024, l16, lds0 + slot * SLOT + wid * 1024);
        if (wid < 6) glds16(sb + (16 + wid) * 1024, l16, lds0 + slot * SLOT + (16 + wid) * 1024);
    };
    const lds_cptr L = (lds_cptr)lds;
    const unsigned ka = lane * 16, kb = 1024 + r32 * 8;
    const int p4 = lane & 3, q4 = (lane & 15) >> 2, blk = (lane >> 4) & 1;
    const unsigned va = 1280 + (8 * hh + q4) * 48 + (blk ? 32 + (p4 ? 8 : 0) : 8 * p4);

    if (wid < 12) glds16(ws + WS_VB + ((size_t)h * SEQ + qt * 256) * 48 + wid * 1024, l16, lds0 + L_VB + wid * 1024);
    load_chunk(0, 0); load_chunk(4, 1);
    load_chunk(1, 2); load_chunk(5, 3); load_chunk(2, 4);
    WAIT_VM(5);
    asm volatile("" : "+v"(pf0), "+v"(pf1), "+v"(wp0), "+v"(wp1), "+v"(qa0), "+v"(qa1), "+v"(kmx));
    const bf16x8 q0 = __builtin_bit_cast(bf16x8, qa0);
    if (hh) qa1 = u32x2{0u, 0u};
    const s16x4 q1 = __builtin_bit_cast(s16x4, qa1);
    float nb;
    {
        float n2 = 0.f;
#pragma unroll
        for (int j = 0; j < 4; ++j) { n2 = fmaf(bflo(qa0[j]), bflo(qa0[j]), n2); n2 = fmaf(bfhi(qa0[j]), bfhi(qa0[j]), n2); }
#pragma unroll
        for (int j = 0; j < 2; ++j) { n2 = fmaf(bflo(qa1[j]), bflo(qa1[j]), n2); n2 = fmaf(bfhi(qa1[j]), bfhi(qa1[j]), n2); }
        n2 = xhalf_sum(n2);
#pragma unroll
        for (int of = 1; of < 64; of <<= 1) kmx = fmaxf(kmx, __shfl_xor(kmx, of));
        nb = -1.01f * __builtin_amdgcn_sqrtf(n2) * __builtin_amdgcn_sqrtf(kmx);
    }
    unsigned phq[10];
    {
        f32x16 pq = MFMA32(__builtin_bit_cast(bf16x8, wp0), __builtin_bit_cast(bf16x8, pf0), f32x16{});
        pq = MFMA32K8(__builtin_bit_cast(s16x4, wp1), __builtin_bit_cast(s16x4, pf1), pq);
        float mx = pq[0], mn = pq[0];
#pragma unroll
        for (int u = 1; u < 10; ++u) { mx = fmaxf(mx, pq[u]); mn = fminf(mn, pq[u]); }
        mx = xhalf_max(mx); mn = xhalf_min(mn);
        float ep[10], en[10], sp = 0.f, sn = 0.f;
        const float cmx = -mx * LOG2E, cmn = mn * LOG2E;
#pragma unroll
        for (int u = 0; u < 10; ++u) {
            ep[u] = __builtin_amdgcn_exp2f(fmaf(pq[u], LOG2E, cmx)); sp += ep[u];
            en[u] = __builtin_amdgcn_exp2f(fmaf(pq[u], -LOG2E, cmn)); sn += en[u];
        }
        sp = __builtin_amdgcn_rcpf(xhalf_sum(sp)); sn = __builtin_amdgcn_rcpf(xhalf_sum(sn));
#pragma unroll
        for (int u = 0; u < 5; ++u) { phq[u] = cvtpk(ep[2 * u] * sp, ep[2 * u + 1] * sp); phq[5 + u] = cvtpk(en[2 * u] * sn, en[2 * u + 1] * sn); }
        if (kh == 0) {
            lds_ptr d = (lds_ptr)lds + L_PHI + (qs * 32 + r32) * 80 + hh * 40;
#pragma unroll
            for (int u = 0; u < 5; ++u) *(__attribute__((address_space(3))) u32x2*)(d + u * 8) = u32x2{phq[2 * u], phq[2 * u + 1]};
        }
    }
    if (wid < 6) { WAIT_VM(6); } else { WAIT_VM(3); }
    RAW_BAR();
    if (wid == 3) {
        f32x16 c0 = {}, c1 = {};
        const unsigned vb = L_VB - 1280 + va;
        const unsigned pb = L_PHI + (8 * hh + q4) * 80 + blk * 32 + p4 * 8;
#pragma unroll 2
        for (int ks = 0; ks < 16; ++ks) {
            const bf16x8 a = cat8(tr_read(L + vb + ks * 768), tr_read(L + vb + ks * 768 + 192));
            const bf16x8 b0 = cat8(tr_read(L + pb + ks * 1280), tr_read(L + pb + ks * 1280 + 320));
            const bf16x8 b1 = cat8(tr_read(L + pb + 64 + ks * 1280), tr_read(L + pb + 64 + ks * 1280 + 320));
            c0 = MFMA32(a, b0, c0); c1 = MFMA32(a, b1, c1);
        }
        char* dst = ws + WS_KVP + ((size_t)h * 8 + qt) * 3840;
        const auto rs = __builtin_amdgcn_make_buffer_rsrc(dst, 0, 3840, 0x00020000);
#pragma unroll
        for (int a = 0; a < 3; ++a) {
            const unsigned off = r32 * 96 + (8 * a + 4 * hh) * 4;
            __builtin_amdgcn_raw_buffer_store_b128(__builtin_bit_cast(u32x4, f32x4{c0[4 * a], c0[4 * a + 1], c0[4 * a + 2], c0[4 * a + 3]}), rs, off, 0, 16);
            __builtin_amdgcn_raw_buffer_store_b128(__builtin_bit_cast(u32x4, f32x4{c1[4 * a], c1[4 * a + 1], c1[4 * a + 2], c1[4 * a + 3]}), rs, off + 3072, 0, 16);
        }
        WAIT_VM(0);
        if (lane == 0) __hip_atomic_store((unsigned*)(ws + WS_FLAG) + h * 32 + qt, 1u, __ATOMIC_RELAXED, __HIP_MEMORY_SCOPE_AGENT);
    }

    f32x16 o;
    float lsum = 1.f;
    for (int attempt = 0;; ++attempt) {
        f32x16 negb;
#pragma unroll
        for (int r = 0; r < 16; ++r) { o[r] = 0.f; negb[r] = nb; }
        attn_chunk(L + (kh ? 1 : 0) * SLOT, ka, kb, va, q0, q1, negb, o);
        WAIT_VM(0);
        RAW_BAR();
        load_chunk(6, 5); load_chunk(3, 6); load_chunk(7, 0);
        attn_chunk(L + (kh ? 3 : 2) * SLOT, ka, kb, va, q0, q1, negb, o);
        WAIT_VM(0);
        RAW_BAR();
        attn_chunk(L + (kh ? 5 : 4) * SLOT, ka, kb, va, q0, q1, negb, o);
        float kvp[8];
        {
            const unsigned* fl = (const unsigned*)(ws + WS_FLAG) + h * 32;
            for (int spin = 0; spin < (1 << 22); ++spin) {
                const unsigned f = __hip_atomic_load(fl + (lane & 7), __ATOMIC_RELAXED, __HIP_MEMORY_SCOPE_AGENT);
                if (__all(f == 1u)) break;
                __builtin_amdgcn_s_sleep(2);
            }
            const char* pk = ws + WS_KVP + (size_t)h * 8 * 3840; const unsigned o4 = (tid < 960 ? tid : 959) * 4;
#pragma unroll
            for (int j = 0; j < 8; ++j) asm volatile("global_load_dword %0, %1, %2 sc1" : "=v"(kvp[j]) : "v"(o4), "s"(pk + j * 3840));
        }
        attn_chunk(L + (kh ? 0 : 6) * SLOT, ka, kb, va, q0, q1, negb, o);
        WAIT_VM(0);
        asm volatile("" : "+v"(kvp[0]), "+v"(kvp[1]), "+v"(kvp[2]), "+v"(kvp[3]), "+v"(kvp[4]), "+v"(kvp[5]), "+v"(kvp[6]), "+v"(kvp[7]));
        {
            const float kvs = ((kvp[0] + kvp[1]) + (kvp[2] + kvp[3])) + ((kvp[4] + kvp[5]) + (kvp[6] + kvp[7]));
            if (tid < 960) {
                const int ph = tid / 24, d = tid - ph * 24, ph_h = ph / 20, u = ph - ph_h * 20;
                *(__attribute__((address_space(3))) uint16_t*)((lds_ptr)lds + L_MISC + d * 96 + ph_h * 48 + u * 2) = (uint16_t)(cvtpk(kvs, 0.f) & 0xffffu);
            }
        }
        RAW_BAR();
        if (kh == 1) {
            lds_ptr d = (lds_ptr)lds + qs * 3072 + lane * 16;
#pragma unroll
            for (int a = 0; a < 3; ++a) *(__attribute__((address_space(3))) f32x4*)(d + a * 1024) = f32x4{o[4 * a], o[4 * a + 1], o[4 * a + 2], o[4 * a + 3]};
        }
        if (tid == 0) *(__attribute__((address_space(3))) unsigned*)((lds_ptr)lds + L_FLAG) = 0u;
        RAW_BAR();
        if (kh == 0) {
            lds_cptr d = L + qs * 3072 + lane * 16;
#pragma unroll
            for (int a = 0; a < 3; ++a) {
                const f32x4 t = *(const __attribute__((address_space(3))) f32x4*)(d + a * 1024);
#pragma unroll
                for (int r = 0; r < 4; ++r) o[4 * a + r] += t[r];
            }
            auto rr = __builtin_amdgcn_permlane32_swap(__float_as_uint(o[8]), __float_as_uint(o[8]), false, false);
            lsum = __uint_as_float(rr[1]);
            if (__any(!(lsum >= 5.4210109e-20f)))
                *(__attribute__((address_space(3))) unsigned*)((lds_ptr)lds + L_FLAG) = 1u;
        }
        RAW_BAR();
        const unsigned redo = *(const __attribute__((address_space(3))) unsigned*)(L + L_FLAG);
        if (__builtin_amdgcn_readfirstlane(redo) == 0u || attempt == 1) break;
        float m = -__builtin_inff();
        for (int t = 0; t < 64; ++t) {
            const char* tb = kvbase + (size_t)t * KVT;
            const bf16x8 k0 = *(const bf16x8*)(tb + ka);
            const s16x4 k1 = *(const s16x4*)(tb + kb);
            f32x16 s = MFMA32(k0, q0, f32x16{});
            s = MFMA32K8(k1, q1, s);
#pragma unroll
            for (int r = 0; r < 16; ++r) m = fmaxf(m, s[r]);
        }
        nb = -xhalf_max(m);
        RAW_BAR();
        load_chunk(0, 0); load_chunk(4, 1); load_chunk(1, 2); load_chunk(5, 3); load_chunk(2, 4);
        WAIT_VM(0);
        RAW_BAR();
    }
    const float gate = ((const float*)(ws + WS_G))[tile * 32 + r32];
    char* oc = ws + WS_OC + ((size_t)h * 5 * SEQ + tile * 32 + r32) * 16;
    if (kh == 0) {
        const float sc = gate * __builtin_amdgcn_rcpf(lsum);
        *(u32x4*)(oc + (size_t)(hh ? 3 : 0) * SEQ * 16) = u32x4{cvtpk(o[0] * sc, o[1] * sc), cvtpk(o[2] * sc, o[3] * sc), cvtpk(o[4] * sc, o[5] * sc), cvtpk(o[6] * sc, o[7] * sc)};
        if (!hh) *(u32x2*)(oc + (size_t)SEQ * 16) = u32x2{cvtpk(o[8] * sc, o[9] * sc), cvtpk(o[10] * sc, o[11] * sc)};
    } else {
        const lds_cptr a = L + L_MISC + r32 * 96 + hh * 48;
        f32x16 ob = MFMA32(LDS_B128(a), __builtin_bit_cast(bf16x8, u32x4{phq[0], phq[1], phq[2], phq[3]}), f32x16{});
        ob = MFMA32(LDS_B128(a + 16), __builtin_bit_cast(bf16x8, u32x4{phq[4], phq[5], phq[6], phq[7]}), ob);
        ob = MFMA32K8(LDS_B64(a + 32), __builtin_bit_cast(s16x4, u32x2{phq[8], phq[9]}), ob);
        auto rr = __builtin_amdgcn_permlane32_swap(__float_as_uint(ob[8]), __float_as_uint(ob[8]), false, false);
        const float sc = (1.0f - gate) / (__uint_as_float(rr[1]) + 1e-6f);
        *(u32x4*)(oc + (size_t)(hh ? 4 : 2) * SEQ * 16) = u32x4{cvtpk(ob[0] * sc, ob[1] * sc), cvtpk(ob[2] * sc, ob[3] * sc), cvtpk(ob[4] * sc, ob[5] * sc), cvtpk(ob[6] * sc, ob[7] * sc)};
        if (!hh) *(u32x2*)(oc + (size_t)SEQ * 16 + 8) = u32x2{cvtpk(ob[8] * sc, ob[9] * sc), cvtpk(ob[10] * sc, ob[11] * sc)};
    }
}

constexpr int K3_STAGE = 36864;
__global__ __launch_bounds__(512) void k3_out(const char* __restrict__ ws, uint16_t* __restrict__ out) {
    __shared__ __attribute__((aligned(16))) char lds[4 * K3_STAGE];
    const int bid = blockIdx.x, xcd = bid & 7, bi = bid >> 3;
    const int tt = (xcd & 3) * 8 + (bi & 7), ct = (xcd >> 2) * 4 + (bi >> 3);
    const int tid = threadIdx.x, lane = tid & 63;
    const int wid = __builtin_amdgcn_readfirstlane(tid >> 6);
    const unsigned lds0 = (unsigned)(uintptr_t)lds;
    const char* sbase; unsigned sstep, voff[5], ldst;
    if (wid < 4) {
        sbase = ws + WS_OC + (size_t)tt * 1024; sstep = 16 * SEQ * 16; ldst = wid * 4096;
#pragma unroll
        for (int j = 0; j < 5; ++j) voff[j] = (wid * 4 + j) * (SEQ * 16) + lane * 16;
    } else {
        sbase = ws + WS_WOP + (size_t)ct * 1280; sstep = 16 * 640 * 16; ldst = 16384 + (wid - 4) * 5120;
#pragma unroll
        for (int j = 0; j < 5; ++j) { const int q = ((wid - 4) * 5 + j) * 64 + lane, c = q / 80, n = q - c * 80; voff[j] = c * (640 * 16) + n * 16; }
    }
    auto stage = [&](int s) {
        const char* sb = sbase + (size_t)s * sstep;
        const unsigned dst = lds0 + (s & 3) * K3_STAGE + ldst;
#pragma unroll
        for (int j = 0; j < 4; ++j) glds16(sb, voff[j], dst + j * 1024);
        if (wid >= 4) glds16(sb, voff[4], dst + 4096);
    };
    stage(0); stage(1); stage(2);
    const int g = lane >> 4, l15 = lane & 15;
    const unsigned oa = g * 1024 + (16 * (wid & 3) + l15) * 16;
    const unsigned wa = 16384 + g * 1280 + (20 * (l15 >> 2) + (l15 & 3)) * 16;
    f32x4 acc[5];
#pragma unroll
    for (int i = 0; i < 5; ++i) acc[i] = f32x4{0.f, 0.f, 0.f, 0.f};
    for (int s = 0; s < 10; ++s) {
        if (wid < 4) { if (s < 8) { WAIT_VM(8); } else if (s == 8) { WAIT_VM(4); } else { WAIT_VM(0); } }
        else { if (s < 8) { WAIT_VM(10); } else if (s == 8) { WAIT_VM(5); } else { WAIT_VM(0); } }
        RAW_BAR();
        if (s + 3 < 10) stage(s + 3);
        if (wid < 4) {
            const lds_cptr buf = (lds_cptr)lds + (s & 3) * K3_STAGE;
#pragma unroll
            for (int ks = 0; ks < 4; ++ks) {
                const bf16x8 of = LDS_B128(buf + oa + ks * 4096);
#pragma unroll
                for (int i = 0; i < 5; ++i) acc[i] = MFMA16(LDS_B128(buf + wa + ks * 5120 + i * 64), of, acc[i]);
            }
        }
    }
    if (wid < 4) {
        char* dst = (char*)out + (size_t)(tt * 64 + 16 * wid + l15) * 1280 + ct * 160 + g * 40;
#pragma unroll
        for (int i = 0; i < 5; ++i) *(u32x2*)(dst + i * 8) = u32x2{cvtpk(acc[i][0], acc[i][1]), cvtpk(acc[i][2], acc[i][3])};
    }
}

extern "C" void kernel_launch(void* const* d_in, const int* in_sizes, int n_in, void* d_out, int out_size, void* d_ws, size_t ws_size,
                              hipStream_t stream) {
    const uint16_t* x = (const uint16_t*)d_in[0];
    const uint16_t* wA = (const uint16_t*)d_in[1];
    const uint16_t* woA = (const uint16_t*)d_in[2];
    const uint16_t* wB = (const uint16_t*)d_in[4];
    const uint16_t* wphi = (const uint16_t*)d_in[5];
    const uint16_t* woB = (const uint16_t*)d_in[6];
    const uint16_t* gatew = (const uint16_t*)d_in[7];
    char* ws = (char*)d_ws;
    k1_qkv<<<256, 768, 0, stream>>>(x, wA, wB, woA, woB, wphi, gatew, ws);
    k2_attn<<<256, 1024, 0, stream>>>(ws);
    k3_out<<<256, 512, 0, stream>>>(ws, (uint16_t*)d_out);
}
```

```cpp
#include <hip/hip_runtime.h>
#include <hip/hip_bf16.h>
#include <stdint.h>

typedef __attribute__((ext_vector_type(8))) __bf16 bf16x8;
typedef __attribute__((ext_vector_type(16))) __bf16 bf16x16;
typedef __attribute__((ext_vector_type(4))) short s16x4;
typedef __attribute__((ext_vector_type(8))) short s16x8;
typedef __attribute__((ext_vector_type(4))) float f32x4;
typedef __attribute__((ext_vector_type(8))) float f32x8;
typedef __attribute__((ext_vector_type(16))) float f32x16;
typedef __attribute__((ext_vector_type(4))) unsigned u32x4;
typedef __attribute__((ext_vector_type(2))) unsigned u32x2;
typedef __attribute__((address_space(3))) const char* lds_cptr;
typedef __attribute__((address_space(3))) char* lds_ptr;

constexpr int SEQ = 2048, DM = 640, NH = 32;
constexpr float LOG2E = 1.4426950408889634f;
constexpr float QSCALE = 0.22360679774997896f * 1.4426950408889634f;

constexpr int KVT = 2816;
constexpr int QFT = 1280;
constexpr size_t WS_KV = 0;
constexpr size_t WS_QA = WS_KV + (size_t)NH * 64 * KVT;
constexpr size_t WS_QKB = WS_QA + (size_t)NH * 64 * QFT;
constexpr size_t WS_VB = WS_QKB + (size_t)NH * 64 * 2 * QFT;
constexpr size_t WS_WOP = WS_VB + (size_t)NH * SEQ * 48;
constexpr size_t WS_OC = WS_WOP + (size_t)160 * 640 * 16;
constexpr size_t WS_KMAX = WS_OC + (size_t)160 * SEQ * 16;
constexpr size_t WS_G = WS_KMAX + (size_t)NH * 64 * 4;
constexpr size_t WS_WPH = WS_G + (size_t)SEQ * 4;
constexpr size_t WS_KVP = WS_WPH + (size_t)NH * 1536;
constexpr size_t WS_FLAG = WS_KVP + (size_t)NH * 8 * 3840;
constexpr size_t WS_END = WS_FLAG + (size_t)NH * 128 + 128;

typedef __attribute__((ext_vector_type(2))) __bf16 bf16x2;
typedef __attribute__((ext_vector_type(2))) float f32x2;
__device__ __forceinline__ unsigned cvtpk(float lo, float hi) {
    return __builtin_bit_cast(unsigned, __builtin_convertvector(f32x2{lo, hi}, bf16x2)); }
__device__ __forceinline__ float bflo(unsigned w) { return __uint_as_float(w << 16); }
__device__ __forceinline__ float bfhi(unsigned w) { return __uint_as_float(w & 0xffff0000u); }
__device__ __forceinline__ void glds16(const void* sbase, unsigned voff, unsigned lds_base) {
    unsigned sv;
    asm volatile("s_mov_b32 %0, m0\n\ts_mov_b32 m0, %3\n\ts_nop 0\n\tglobal_load_lds_dwordx4 %1, %2\n\ts_mov_b32 m0, %0"
                 : "=&s"(sv) : "v"(voff), "s"(sbase), "s"(lds_base) : "memory"); }
__device__ __forceinline__ void glds16_sc1(const void* sbase, unsigned voff, unsigned lds_base) {
    unsigned sv;
    asm volatile("s_mov_b32 %0, m0\n\ts_mov_b32 m0, %3\n\ts_nop 0\n\tglobal_load_lds_dwordx4 %1, %2 sc1\n\ts_mov_b32 m0, %0"
                 : "=&s"(sv) : "v"(voff), "s"(sbase), "s"(lds_base) : "memory"); }
__device__ __forceinline__ void glds4_sc1(const void* sbase, unsigned voff, unsigned lds_base) {
    unsigned sv;
    asm volatile("s_mov_b32 %0, m0\n\ts_mov_b32 m0, %3\n\ts_nop 0\n\tglobal_load_lds_dword %1, %2 sc1\n\ts_mov_b32 m0, %0"
                 : "=&s"(sv) : "v"(voff), "s"(sbase), "s"(lds_base) : "memory"); }
template <int N, int GOFF, int LOFF, bool SC1 = false>
__device__ __forceinline__ void glds_run(const void* sbase, unsigned voff, unsigned lbase) {
    unsigned sv; uint64_t t;
#define GL_HEAD "s_mov_b32 %0, m0\n\ts_add_u32 m0, %4, %6\n\ts_add_u32 %L1, %L3, %5\n\ts_addc_u32 %H1, %H3, 0\n\t"
    static_assert(N >= 1 && N <= 4, "");
    const unsigned lo = (unsigned)(uintptr_t)sbase, hi = (unsigned)((uintptr_t)sbase >> 32);
    unsigned tl, th;
    asm volatile("s_add_u32 %0, %2, %4\n\ts_addc_u32 %1, %3, 0" : "=&s"(tl), "=s"(th) : "s"(lo), "s"(hi), "i"(GOFF) : "scc");
    t = ((uint64_t)th << 32) | tl;
    if constexpr (SC1) {
        asm volatile("s_mov_b32 %0, m0\n\ts_add_u32 m0, %3, %4\n\ts_nop 0\n\tglobal_load_lds_dwordx4 %1, %2 sc1\n\ts_mov_b32 m0, %0"
                     : "=&s"(sv) : "v"(voff), "s"(t), "s"(lbase), "i"(LOFF) : "memory", "scc");
    } else if constexpr (N == 1)
        asm volatile("s_mov_b32 %0, m0\n\ts_add_u32 m0, %3, %4\n\ts_nop 0\n\tglobal_load_lds_dwordx4 %1, %2\n\ts_mov_b32 m0, %0"
                     : "=&s"(sv) : "v"(voff), "s"(t), "s"(lbase), "i"(LOFF) : "memory", "scc");
    else if constexpr (N == 2)
        asm volatile("s_mov_b32 %0, m0\n\ts_add_u32 m0, %3, %4\n\ts_nop 0\n\tglobal_load_lds_dwordx4 %1, %2\n\tglobal_load_lds_dwordx4 %1, %2 offset:1024\n\ts_mov_b32 m0, %0"
                     : "=&s"(sv) : "v"(voff), "s"(t), "s"(lbase), "i"(LOFF) : "memory", "scc");
    else if constexpr (N == 3)
        asm volatile("s_mov_b32 %0, m0\n\ts_add_u32 m0, %3, %4\n\ts_nop 0\n\tglobal_load_lds_dwordx4 %1, %2\n\tglobal_load_lds_dwordx4 %1, %2 offset:1024\n\tglobal_load_lds_dwordx4 %1, %2 offset:2048\n\ts_mov_b32 m0, %0"
                     : "=&s"(sv) : "v"(voff), "s"(t), "s"(lbase), "i"(LOFF) : "memory", "scc");
    else
        asm volatile("s_mov_b32 %0, m0\n\ts_add_u32 m0, %3, %4\n\ts_nop 0\n\tglobal_load_lds_dwordx4 %1, %2\n\tglobal_load_lds_dwordx4 %1, %2 offset:1024\n\tglobal_load_lds_dwordx4 %1, %2 offset:2048\n\tglobal_load_lds_dwordx4 %1, %2 offset:3072\n\ts_mov_b32 m0, %0"
                     : "=&s"(sv) : "v"(voff), "s"(t), "s"(lbase), "i"(LOFF) : "memory", "scc");
#undef GL_HEAD
}
__device__ __forceinline__ s16x4 tr_read(lds_cptr p) {
    return __builtin_amdgcn_ds_read_tr16_b64_v4i16((__attribute__((address_space(3))) s16x4*)p); }
__device__ __forceinline__ bf16x8 cat8(s16x4 a, s16x4 b) {
    s16x8 r = {a[0], a[1], a[2], a[3], b[0], b[1], b[2], b[3]}; return __builtin_bit_cast(bf16x8, r); }
#define LDS_B128(p) (*(const __attribute__((address_space(3))) bf16x8*)(p))
#define LDS_B64(p) (*(const __attribute__((address_space(3))) s16x4*)(p))
#define MFMA16(a, b, c) __builtin_amdgcn_mfma_f32_16x16x32_bf16(a, b, c, 0, 0, 0)
#define MFMA32(a, b, c) __builtin_amdgcn_mfma_f32_32x32x16_bf16(a, b, c, 0, 0, 0)
#define MFMA32K8(a, b, c) __builtin_amdgcn_mfma_f32_32x32x8bf16_1k(a, b, c, 0, 0, 0)
#define WAIT_VM(n) asm volatile("s_waitcnt vmcnt(" #n ")" ::: "memory")
#define RAW_BAR() do { asm volatile("s_waitcnt lgkmcnt(0)\n\ts_barrier" ::: "memory"); } while (0)
__device__ __forceinline__ float xhalf_max(float v) {
    auto r = __builtin_amdgcn_permlane32_swap(__float_as_uint(v), __float_as_uint(v), false, false);
    return fmaxf(__uint_as_float(r[0]), __uint_as_float(r[1])); }
__device__ __forceinline__ float xhalf_min(float v) {
    auto r = __builtin_amdgcn_permlane32_swap(__float_as_uint(v), __float_as_uint(v), false, false);
    return fminf(__uint_as_float(r[0]), __uint_as_float(r[1])); }
__device__ __forceinline__ float xhalf_sum(float v) {
    auto r = __builtin_amdgcn_permlane32_swap(__float_as_uint(v), __float_as_uint(v), false, false);
    return __uint_as_float(r[0]) + __uint_as_float(r[1]); }

#define K1_ROT 0
constexpr int K1_STAGE = 49152;
__global__ __launch_bounds__(768) void k1_qkv(const uint16_t* __restrict__ x, const uint16_t* __restrict__ wA, const uint16_t* __restrict__ wB,
                                              const uint16_t* __restrict__ woA, const uint16_t* __restrict__ woB,
                                              const uint16_t* __restrict__ wphi, const uint16_t* __restrict__ gatew, char* __restrict__ ws) {
    __shared__ __attribute__((aligned(16))) char lds[3 * K1_STAGE];
    const int bid = blockIdx.x, xcd = bid & 7, bi = bid >> 3;
    const int rt = (xcd & 1) * 8 + (bi & 7), cg = (xcd >> 1) * 4 + (bi >> 3), br = cg >> 3;
    const int rot = K1_ROT ? (((bi >> 3) * 5) / 2 + (bi & 7) * 4) % 10 : 0;
    const int tid = threadIdx.x, lane = tid & 63;
    const int wid = __builtin_amdgcn_readfirstlane(tid >> 6);
    const unsigned lds0 = (unsigned)(uintptr_t)lds;
    const bool isx = wid < 4;
    const char* sbase = isx ? (const char*)x + (size_t)rt * 128 * 1280 : (const char*)(br ? wB : wA) + (cg & 7) * 480;
    const unsigned sstep = isx ? 128u : 64u * 3840u;
    const unsigned ldst = lds0 + wid * 4096;
    unsigned voff[4];
#pragma unroll
    for (int j = 0; j < 4; ++j) {
        const int xrow = 8 * (wid * 4 + j) + (lane >> 3);
        const unsigned vx = xrow * 1280 + (((lane ^ xrow) & 7) << 4);
        const int p = (wid - 4) * 4 + j;
        int q = (p & 7) * 64 + lane; const int up = q >= 248; q -= up ? 248 : 0; q = q < 239 ? q : 239;
        const int row = __umul24(q, 2185) >> 16, ch = q - row * 30;
        const unsigned vw = __umul24(16 * (p >> 3) + 8 * up + row, 3840) + ch * 16;
        voff[j] = isx ? vx : vw;
        glds16(sbase + (size_t)rot * sstep, voff[j], ldst + j * 1024);
    }
    auto stage1 = [&](int s, int j) { const int kc = s + rot >= 10 ? s + rot - 10 : s + rot;
        glds16(sbase + (size_t)kc * sstep, voff[j], ldst + (s % 3) * K1_STAGE + j * 1024); };
    asm volatile("s_barrier" ::: "memory");
#pragma unroll
    for (int j = 0; j < 4; ++j) stage1(1, j);
#pragma unroll
    for (int j = 0; j < 4; ++j) stage1(2, j);

    const int tg = wid & 3, sg = wid >> 2;
    const int g = lane >> 4, l15 = lane & 15, q4 = l15 >> 2, p4 = lane & 3;
    const int xrow = 32 * tg + l15;
    const unsigned xa0 = xrow * 128 + ((g ^ (xrow & 7)) << 4), xa1 = xa0 ^ 64;
    const unsigned wa = 16384 + 8192 * (g >> 1) + 480 * (8 * (g & 1) + q4) + 128 * (g & 1) + 160 * sg + 40 * p4;
    f32x4 acc[2][5];
#pragma unroll
    for (int m = 0; m < 2; ++m)
#pragma unroll
        for (int i = 0; i < 5; ++i) acc[m][i] = f32x4{0.f, 0.f, 0.f, 0.f};
    u32x4 sj[4];
    unsigned wo[20];
#pragma unroll
    for (int j = 0; j < 4; ++j) sj[j] = u32x4{0u, 0u, 0u, 0u};
#pragma unroll
    for (int j = 0; j < 20; ++j) wo[j] = 0u;
    const int gtok = rt * 128 + cg * 8 + (wid - 4);
    const int item = bid * 200 + tid;
    const bool phij = wid == 3 && bi < 4;

    bf16x8 xA[2], wA_[5], xB[2], wB_[5];
#define K1_LOAD(X, W, slot, ks) do { const lds_cptr buf_ = (lds_cptr)lds + (slot) * K1_STAGE; \
        _Pragma("unroll") for (int m = 0; m < 2; ++m) X[m] = LDS_B128(buf_ + ((ks) ? xa1 : xa0) + m * 2048); \
        _Pragma("unroll") for (int i = 0; i < 5; ++i) W[i] = cat8(tr_read(buf_ + wa + (ks) * 16384 + i * 8), tr_read(buf_ + wa + (ks) * 16384 + i * 8 + 1920)); } while (0)
#define K1_MMA1(X, W, i) do { acc[0][i] = MFMA16(W[i], X[0], acc[0][i]); acc[1][i] = MFMA16(W[i], X[1], acc[1][i]); } while (0)
    WAIT_VM(8);
    RAW_BAR();
    K1_LOAD(xA, wA_, 0, 0);
#pragma unroll
    for (int s = 0; s < 10; ++s) {
        K1_LOAD(xB, wB_, s % 3, 1); __builtin_amdgcn_sched_barrier(0);
#pragma unroll
        for (int i = 0; i < 5; ++i) K1_MMA1(xA, wA_, i);
        __builtin_amdgcn_sched_barrier(0);
        if (s < 9) {
            if (s < 8) { WAIT_VM(4); } else { WAIT_VM(0); }
            RAW_BAR();
            K1_LOAD(xA, wA_, (s + 1) % 3, 0); __builtin_amdgcn_sched_barrier(0);
            if (s == 8) {
                if (isx) {
                    const int it = tid < 200 ? item : bid * 200, ch = it / 320, n2 = it - ch * 320, hd = ch / 5, c = ch - hd * 5;
                    const int d0 = c == 1 ? 16 : (c >= 3 ? 4 : 0), d1 = c == 1 ? 16 : d0 + 8;
                    const char* p0 = (const char*)((c == 2 || c == 4) ? woB : woA) + (hd * 20 + d0) * 1280 + n2 * 4;
                    const char* p1 = (const char*)((c == 0 || c == 3) ? woA : woB) + (hd * 20 + d1) * 1280 + n2 * 4;
#define LD4(V, OP, P, S1, S2, S3) asm volatile(OP " %0, %4, off\n\t" OP " %1, %4, off offset:" #S1 "\n\t" OP " %2, %4, off offset:" #S2 "\n\t" OP " %3, %4, off offset:" #S3 \
                     : "+v"(V[0]), "+v"(V[1]), "+v"(V[2]), "+v"(V[3]) : "v"(P))
                    LD4(wo, "global_load_dword", p0, 1280, 2560, 3840);
                    LD4((wo + 4), "global_load_dword", p1, 1280, 2560, 3840);
                    if (phij) {
                        const int m = lane & 31, r = (m & 3) + 4 * (m >> 3), f = r < 10 ? 10 * ((m >> 2) & 1) + r : 0;
                        const char* pf = (const char*)wphi + (4 * xcd + bi) * 800 + f * 2;
                        const char* pa = pf + (lane >> 5) * 320; const char* pb = pa + 160; const char* pc = pf + 640;
                        LD4((wo + 8), "global_load_ushort", pa, 40, 80, 120);
                        LD4((wo + 12), "global_load_ushort", pb, 40, 80, 120);
                        LD4((wo + 16), "global_load_ushort", pc, 40, 80, 120);
                    }
                } else {
                    const char* px = (const char*)x + (size_t)gtok * 1280; const unsigned o16 = lane * 16, o2 = 1024 + (lane & 15) * 16;
                    asm volatile("global_load_dwordx4 %0, %4, %6\n\tglobal_load_dwordx4 %1, %5, %6\n\tglobal_load_dwordx4 %2, %4, %7\n\tglobal_load_dwordx4 %3, %5, %7"
                                 : "+v"(sj[0]), "+v"(sj[1]), "+v"(sj[2]), "+v"(sj[3]) : "v"(o16), "v"(o2), "s"(px), "s"(gatew));
                }
                __builtin_amdgcn_sched_barrier(0);
            }
        }
#pragma unroll
        for (int i = 0; i < 5; ++i) {
            K1_MMA1(xB, wB_, i); __builtin_amdgcn_sched_barrier(0);
            if (i < 4 && s + 3 < 10) { stage1(s + 3, i); __builtin_amdgcn_sched_barrier(0); }
        }
    }
    const int gg = 3 * (cg & 7) + sg, type = gg >> 3, head0 = 4 * (gg & 7), head = head0 + g, tile = rt * 4 + tg;
    const lds_ptr stg = (lds_ptr)lds + K1_STAGE + wid * 6144;
    float kn = 0.f;
#pragma unroll
    for (int m = 0; m < 2; ++m) {
        const int r32 = 16 * m + l15;
        const float sc = (type == 0 && br == 0) ? QSCALE : 1.0f;
        unsigned w[10];
#pragma unroll
        for (int i = 0; i < 5; ++i) { w[2 * i] = cvtpk(acc[m][i][0] * sc, acc[m][i][1] * sc); w[2 * i + 1] = cvtpk(acc[m][i][2] * sc, acc[m][i][3] * sc); }
        if (type < 2) {
            const lds_ptr d = stg + g * 1280;
            *(__attribute__((address_space(3))) u32x4*)(d + r32 * 16) = u32x4{w[0], w[1], w[2], w[3]};
            *(__attribute__((address_space(3))) u32x4*)(d + 512 + r32 * 16) = u32x4{w[4], w[5], w[6], w[7]};
            *(__attribute__((address_space(3))) u32x2*)(d + 1024 + r32 * 8) = u32x2{w[8], w[9]};
            if (type == 1 && br == 0) {
                float n2 = 0.f;
#pragma unroll
                for (int i = 0; i < 5; ++i)
#pragma unroll
                    for (int r = 0; r < 4; ++r) n2 = fmaf(acc[m][i][r], acc[m][i][r], n2);
                kn = fmaxf(kn, n2);
                if (!(n2 == n2)) kn = __builtin_inff();
            }
        } else {
            const lds_ptr d = stg + g * 1536 + r32 * 48;
            *(__attribute__((address_space(3))) u32x4*)(d) = u32x4{w[0], w[1], w[2], w[3]};
            *(__attribute__((address_space(3))) u32x4*)(d + 16) = u32x4{w[4], w[5], w[6], w[7]};
            *(__attribute__((address_space(3))) u32x4*)(d + 32) = u32x4{w[8], w[9], 0x00003f80u, 0u};
        }
    }
    {
        char* dbase; unsigned hstride;
        if (br == 0) {
            if (type == 0) { dbase = ws + WS_QA + ((size_t)head0 * 64 + tile) * QFT; hstride = 64 * QFT; }
            else { dbase = ws + WS_KV + ((size_t)head0 * 64 + tile) * KVT + (type == 2 ? 1280 : 0); hstride = 64 * KVT; }
        } else {
            if (type < 2) { dbase = ws + WS_QKB + ((size_t)head0 * 64 + tile) * (2 * QFT) + (type == 0 ? QFT : 0); hstride = 64 * 2 * QFT; }
            else { dbase = ws + WS_VB + ((size_t)head0 * 64 + tile) * 1536; hstride = 64 * 1536; }
        }
        if (type < 2) {
#pragma unroll
            for (int j = 0; j < 5; ++j) {
                const int i = j * 64 + lane, hj = __umul24(i, 820) >> 16, o = i - hj * 80;
                *(u32x4*)(dbase + hj * hstride + o * 16) = *(const __attribute__((address_space(3))) u32x4*)(stg + i * 16);
            }
        } else {
#pragma unroll
            for (int j = 0; j < 6; ++j) {
                const int i = j * 64 + lane, hj = __umul24(i, 683) >> 16, o = i - hj * 96;
                *(u32x4*)(dbase + hj * hstride + o * 16) = *(const __attribute__((address_space(3))) u32x4*)(stg + i * 16);
            }
        }
    }
    if (type == 1 && br == 0) {
#pragma unroll
        for (int o = 1; o < 16; o <<= 1) kn = fmaxf(kn, __shfl_xor(kn, o));
        if (l15 == 0) ((float*)(ws + WS_KMAX))[head * 64 + rt * 4 + tg] = kn;
    }
    WAIT_VM(5);
    asm volatile("" : "+v"(sj[0]), "+v"(sj[1]), "+v"(sj[2]), "+v"(sj[3]));
    asm volatile("" : "+v"(wo[0]), "+v"(wo[1]), "+v"(wo[2]), "+v"(wo[3]), "+v"(wo[4]), "+v"(wo[5]), "+v"(wo[6]), "+v"(wo[7]), "+v"(wo[8]), "+v"(wo[9]));
    asm volatile("" : "+v"(wo[10]), "+v"(wo[11]), "+v"(wo[12]), "+v"(wo[13]), "+v"(wo[14]), "+v"(wo[15]), "+v"(wo[16]), "+v"(wo[17]), "+v"(wo[18]), "+v"(wo[19]));
    if (!isx) {
        float d = 0.f, e = 0.f;
#pragma unroll
        for (int j = 0; j < 4; ++j) {
            d = fmaf(bflo(sj[0][j]), bflo(sj[2][j]), d); d = fmaf(bfhi(sj[0][j]), bfhi(sj[2][j]), d);
            e = fmaf(bflo(sj[1][j]), bflo(sj[3][j]), e); e = fmaf(bfhi(sj[1][j]), bfhi(sj[3][j]), e);
        }
        d += lane < 16 ? e : 0.f;
#pragma unroll
        for (int o = 1; o < 64; o <<= 1) d += __shfl_xor(d, o);
        if (lane == 0) ((float*)(ws + WS_G))[gtok] = 1.0f / (1.0f + __expf(-d));
    } else if (tid < 200) {
        char* dst = ws + WS_WOP + (size_t)item * 32;
        *(u32x4*)(dst) = u32x4{__builtin_amdgcn_perm(wo[1], wo[0], 0x05040100), __builtin_amdgcn_perm(wo[3], wo[2], 0x05040100),
                               __builtin_amdgcn_perm(wo[5], wo[4], 0x05040100), __builtin_amdgcn_perm(wo[7], wo[6], 0x05040100)};
        *(u32x4*)(dst + 16) = u32x4{__builtin_amdgcn_perm(wo[1], wo[0], 0x07060302), __builtin_amdgcn_perm(wo[3], wo[2], 0x07060302),
                                    __builtin_amdgcn_perm(wo[5], wo[4], 0x07060302), __builtin_amdgcn_perm(wo[7], wo[6], 0x07060302)};
    }
    if (phij) {
        const int hd = 4 * xcd + bi, m = lane & 31, r = (m & 3) + 4 * (m >> 3);
        const unsigned m0 = r < 10 ? 0xffffffffu : 0u, m1 = (r < 10 && lane < 32) ? 0xffffffffu : 0u;
        char* dst = ws + WS_WPH + hd * 1536;
        *(u32x4*)(dst + lane * 16) = u32x4{(wo[8] | (wo[9] << 16)) & m0, (wo[10] | (wo[11] << 16)) & m0, (wo[12] | (wo[13] << 16)) & m0, (wo[14] | (wo[15] << 16)) & m0};
        *(u32x2*)(dst + 1024 + lane * 8) = u32x2{(wo[16] | (wo[17] << 16)) & m1, (wo[18] | (wo[19] << 16)) & m1};
        if (lane < 32) ((unsigned*)(ws + WS_FLAG))[hd * 32 + lane] = 0u;
    }
}

constexpr int SLOT = 8 * KVT;
constexpr int L_VB = 5 * SLOT;
constexpr int L_PHI = L_VB + 12288;
constexpr int L_MISC = L_PHI + 20480 + 128;
constexpr int L_FLAG = L_MISC + 3072;
constexpr int L_HF = L_FLAG + 16;
constexpr int L_KVG = 3 * SLOT;
constexpr int K2_LDS = L_HF + 256;
#define SBAR() __builtin_amdgcn_sched_barrier(0)
#define EXP2(v) __builtin_amdgcn_exp2f(v)
#define SMF(a, lo, acc) __builtin_amdgcn_smfmac_f32_32x32x32_bf16(a, __builtin_bit_cast(bf16x16, lo), acc, 0xDDDD, 0, 0)
#define LO8(s) __builtin_shufflevector(s, s, 0, 1, 2, 3, 4, 5, 6, 7)
#define HI8(s) __builtin_shufflevector(s, s, 8, 9, 10, 11, 12, 13, 14, 15)

struct KFrag { bf16x8 k0; s16x4 k1; };
struct VFrag { bf16x8 v0, v1; };
__device__ __forceinline__ KFrag ld_k(lds_cptr pa, lds_cptr pb, int off) { KFrag k; k.k0 = LDS_B128(pa + off); k.k1 = LDS_B64(pb + off); return k; }
__device__ __forceinline__ VFrag ld_v(lds_cptr p, int off) {
    VFrag v; v.v0 = cat8(tr_read(p + off), tr_read(p + off + 192)); v.v1 = cat8(tr_read(p + off + 768), tr_read(p + off + 960)); return v; }
__device__ __forceinline__ void attn_step(f32x16& sc, f32x16& sn, const KFrag& kn, const bf16x8& vprev1, const bf16x8& vc0,
                                          const bf16x8& qn0, const s16x4& qn1, const f32x16& negbn, f32x16& oprev, f32x16& oc) {
    oprev = SMF(vprev1, HI8(sn), oprev); SBAR();
#pragma unroll
    for (int r = 0; r < 4; ++r) sc[r] = EXP2(sc[r]);
    SBAR(); sn = MFMA32(kn.k0, qn0, negbn); SBAR();
#pragma unroll
    for (int r = 4; r < 8; ++r) sc[r] = EXP2(sc[r]);
    SBAR(); sn = MFMA32K8(kn.k1, qn1, sn); SBAR();
#pragma unroll
    for (int r = 8; r < 12; ++r) sc[r] = EXP2(sc[r]);
    SBAR(); oc = SMF(vc0, LO8(sc), oc); SBAR();
#pragma unroll
    for (int r = 12; r < 16; ++r) sc[r] = EXP2(sc[r]);
    SBAR();
}

__device__ __forceinline__ void feature_map(const u32x4& wp0, const u32x2& wp1, const u32x4& f0, const u32x2& f1, unsigned (&ph)[10]) {
    f32x16 pq = MFMA32(__builtin_bit_cast(bf16x8, wp0), __builtin_bit_cast(bf16x8, f0), f32x16{});
    pq = MFMA32K8(__builtin_bit_cast(s16x4, wp1), __builtin_bit_cast(s16x4, f1), pq);
    float mx = pq[0], mn = pq[0];
#pragma unroll
    for (int u = 1; u < 10; ++u) { mx = fmaxf(mx, pq[u]); mn = fminf(mn, pq[u]); }
    mx = xhalf_max(mx); mn = xhalf_min(mn);
    float ep[10], en[10], sp = 0.f, sn = 0.f;
    const float cmx = -mx * LOG2E, cmn = mn * LOG2E;
#pragma unroll
    for (int u = 0; u < 10; ++u) {
        ep[u] = EXP2(fmaf(pq[u], LOG2E, cmx)); sp += ep[u];
        en[u] = EXP2(fmaf(pq[u], -LOG2E, cmn)); sn += en[u];
    }
    sp = __builtin_amdgcn_rcpf(xhalf_sum(sp)); sn = __builtin_amdgcn_rcpf(xhalf_sum(sn));
#pragma unroll
    for (int u = 0; u < 5; ++u) { ph[u] = cvtpk(ep[2 * u] * sp, ep[2 * u + 1] * sp); ph[5 + u] = cvtpk(en[2 * u] * sn, en[2 * u + 1] * sn); }
}

__device__ __forceinline__ float qnorm2(const u32x4& a, const u32x2& b) {
    float n2 = 0.f;
#pragma unroll
    for (int j = 0; j < 4; ++j) { n2 = fmaf(bflo(a[j]), bflo(a[j]), n2); n2 = fmaf(bfhi(a[j]), bfhi(a[j]), n2); }
#pragma unroll
    for (int j = 0; j < 2; ++j) { n2 = fmaf(bflo(b[j]), bflo(b[j]), n2); n2 = fmaf(bfhi(b[j]), bfhi(b[j]), n2); }
    return xhalf_sum(n2);
}

__global__ __launch_bounds__(512) void k2_attn(char* __restrict__ ws) {
    __shared__ __attribute__((aligned(16))) char lds[K2_LDS];
    const int bid = blockIdx.x, xcd = bid & 7, bi = bid >> 3;
    const int h = xcd * 4 + (bi >> 3), qt = bi & 7;
    const int tid = threadIdx.x, lane = tid & 63, r32 = lane & 31, hh = lane >> 5;
    const int wid = __builtin_amdgcn_readfirstlane(tid >> 6);
    const int qp = wid & 3, kh = wid >> 2;
    const int own = 2 * qp + kh;
    const unsigned lds0 = (unsigned)(uintptr_t)lds;
    const char* kvbase = ws + WS_KV + (size_t)h * 64 * KVT;
    const unsigned l16 = lane * 16;
    u32x4 qa0, qb0, wp0; u32x2 qa1, qb1, wp1; float kmx, gate;
    {
        const char* pq = ws + WS_QA + ((size_t)h * 64 + qt * 8 + 2 * qp) * QFT;
        const unsigned o8 = 1024 + r32 * 8;
        asm volatile("global_load_dwordx4 %0, %1, %2" : "=v"(qa0) : "v"(l16), "s"(pq));
        asm volatile("global_load_dwordx2 %0, %1, %2" : "=v"(qa1) : "v"(o8), "s"(pq));
        asm volatile("global_load_dwordx4 %0, %1, %2 offset:1280" : "=v"(qb0) : "v"(l16), "s"(pq));
        asm volatile("global_load_dwordx2 %0, %1, %2 offset:1280" : "=v"(qb1) : "v"(o8), "s"(pq));
        const char* pm = ws + WS_KMAX + h * 256; const unsigned o4 = lane * 4;
        asm volatile("global_load_dword %0, %1, %2" : "=v"(kmx) : "v"(o4), "s"(pm));
    }
    const int cst = (wid & 3) * 6 - (wid == 3 ? 1 : 0);
    const char* kvc = kvbase + cst * 1024; const unsigned ldc = lds0 + cst * 1024;
    const char* kvf = kvbase + (size_t)((wid >> 1) & 1) * 4 * SLOT + (wid & 1) * 6144; const unsigned ldf = lds0 + ((wid >> 1) & 1) * SLOT + (wid & 1) * 6144;
#define LOAD_FIRST(hf) do { if (kh == 0) { glds_run<4, (hf) * 11264, (hf) * 11264>(kvf, l16, ldf); \
        if (wid & 1) glds_run<1, (hf) * 11264 + 4096, (hf) * 11264 + 4096>(kvf, l16, ldf); else glds_run<2, (hf) * 11264 + 4096, (hf) * 11264 + 4096>(kvf, l16, ldf); } } while (0)
#define LOAD_CHUNK(c, slot) do { if (kh == 0) { glds_run<4, (c) * SLOT, (slot) * SLOT>(kvc, l16, ldc); \
        if (wid & 2) glds_run<1, (c) * SLOT + 4096, (slot) * SLOT + 4096>(kvc, l16, ldc); else glds_run<2, (c) * SLOT + 4096, (slot) * SLOT + 4096>(kvc, l16, ldc); } } while (0)
    LOAD_FIRST(0);
    {
        const char* pw = ws + WS_WPH + h * 1536; const unsigned o8w = 1024 + lane * 8;
        asm volatile("global_load_dwordx4 %0, %1, %2" : "=v"(wp0) : "v"(l16), "s"(pw));
        asm volatile("global_load_dwordx2 %0, %1, %2" : "=v"(wp1) : "v"(o8w), "s"(pw));
        const char* pg = ws + WS_G + (qt * 8 + own) * 128; const unsigned o4b = r32 * 4;
        asm volatile("global_load_dword %0, %1, %2" : "=v"(gate) : "v"(o4b), "s"(pg));
    }
    LOAD_FIRST(1);
    const lds_cptr L = (lds_cptr)lds;
    const unsigned ka = lane * 16, kb = 1024 + r32 * 8;
    const int p4 = lane & 3, q4 = (lane & 15) >> 2, blk = (lane >> 4) & 1;
    const unsigned va = 1280 + (8 * hh + q4) * 48 + (blk ? 32 + (p4 ? 8 : 0) : 8 * p4);
    if (kh == 0) { WAIT_VM(5); } else { WAIT_VM(0); }
    asm volatile("" : "+v"(wp0), "+v"(wp1), "+v"(qa0), "+v"(qa1), "+v"(qb0), "+v"(qb1), "+v"(kmx), "+v"(gate));
    if (hh) { qa1 = u32x2{0u, 0u}; qb1 = u32x2{0u, 0u}; }
    const bf16x8 QA0 = __builtin_bit_cast(bf16x8, qa0), QB0 = __builtin_bit_cast(bf16x8, qb0);
    const s16x4 QA1 = __builtin_bit_cast(s16x4, qa1), QB1 = __builtin_bit_cast(s16x4, qb1);
    float nba, nbb;
    {
#pragma unroll
        for (int of = 1; of < 64; of <<= 1) kmx = fmaxf(kmx, __shfl_xor(kmx, of));
        const float kn = -1.01f * __builtin_amdgcn_sqrtf(kmx);
        nba = kn * __builtin_amdgcn_sqrtf(qnorm2(qa0, qa1)); nbb = kn * __builtin_amdgcn_sqrtf(qnorm2(qb0, qb1));
    }
    unsigned phq[10];
#pragma unroll
    for (int u = 0; u < 10; ++u) phq[u] = 0u;
    RAW_BAR();

    for (int attempt = 0;; ++attempt) {
        f32x16 oa, ob, nga, ngb, sA, sB;
#pragma unroll
        for (int r = 0; r < 16; ++r) { oa[r] = 0.f; ob[r] = 0.f; nga[r] = nba; ngb[r] = nbb; sB[r] = 0.f; }
        asm volatile("" : "+v"(nga), "+v"(ngb));
        lds_cptr pka = L + kh * SLOT + ka, pkb = L + kh * SLOT + kb, pv = L + kh * SLOT + va;
        KFrag k0 = ld_k(pka, pkb, 0), k1 = ld_k(pka, pkb, KVT);
        VFrag v0 = ld_v(pv, 0), v1 = v0;
        sA = MFMA32(k0.k0, QA0, nga); sA = MFMA32K8(k0.k1, QA1, sA);
        for (int c = 0; c < 4; ++c) {
            const int snext = (2 * c + 2 + kh) % 5;
#define K2_PAIR(t, LOADK0, LOADV0, LOADK1) do { \
                bf16x8 vp = v1.v1; \
                v1 = ld_v(pv, ((t) + 1) * KVT); SBAR(); \
                attn_step(sA, sB, k0, vp, v0.v0, QB0, QB1, ngb, ob, oa); \
                LOADK0; SBAR(); \
                attn_step(sB, sA, k1, v0.v1, v0.v0, QA0, QA1, nga, oa, ob); \
                vp = v0.v1; \
                LOADV0; SBAR(); \
                attn_step(sA, sB, k1, vp, v1.v0, QB0, QB1, ngb, ob, oa); \
                LOADK1; SBAR(); \
                attn_step(sB, sA, k0, v1.v1, v1.v0, QA0, QA1, nga, oa, ob); } while (0)
            K2_PAIR(0, k0 = ld_k(pka, pkb, 2 * KVT), v0 = ld_v(pv, 2 * KVT), k1 = ld_k(pka, pkb, 3 * KVT));
            if (c == 0) {
                WAIT_VM(0);
                RAW_BAR();
                const char* sq = ws + WS_QKB + ((size_t)h * 64 + qt * 8 + own) * (2 * QFT);
                const unsigned dq = lds0 + L_PHI + own * 2560;
                glds_run<2, 0, 0>(sq, l16, dq);
                if (lane < 32) glds_run<1, 2048, 2048>(sq, l16, dq);
                LOAD_CHUNK(1, 2); LOAD_CHUNK(5, 3);
            } else if (c == 1 && wid < 3 && attempt == 0) {
                WAIT_VM(0);
                if (lane == 0) __hip_atomic_store((unsigned*)(ws + WS_FLAG) + h * 32 + qt * 4 + wid, 1u, __ATOMIC_RELAXED, __HIP_MEMORY_SCOPE_AGENT);
            }
            K2_PAIR(2, k0 = ld_k(pka, pkb, 4 * KVT), v0 = ld_v(pv, 4 * KVT), k1 = ld_k(pka, pkb, 5 * KVT));
            if (c == 0 && kh == 0) {
                glds_run<3, 0, L_VB>(ws + WS_VB + ((size_t)h * SEQ + qt * 256) * 48 + wid * 3072, l16, lds0 + wid * 3072);
                LOAD_CHUNK(2, 4);
            }
            K2_PAIR(4, k0 = ld_k(pka, pkb, 6 * KVT), v0 = ld_v(pv, 6 * KVT), k1 = ld_k(pka, pkb, 7 * KVT));
            auto turn = [&]() {
                WAIT_VM(0);
                if (c == 0 && attempt == 0) {
                    const lds_cptr f = L + L_PHI + own * 2560;
                    const u32x4 kf0 = *(const __attribute__((address_space(3))) u32x4*)(f + ka), qf0 = *(const __attribute__((address_space(3))) u32x4*)(f + 1280 + ka);
                    const u32x2 kf1 = *(const __attribute__((address_space(3))) u32x2*)(f + kb), qf1 = *(const __attribute__((address_space(3))) u32x2*)(f + 1280 + kb);
                    unsigned phk[10];
                    feature_map(wp0, wp1, kf0, kf1, phk);
                    asm volatile("s_waitcnt lgkmcnt(0)" ::: "memory");
                    lds_ptr d = (lds_ptr)lds + L_PHI + own * 2560 + r32 * 80 + hh * 40;
#pragma unroll
                    for (int u = 0; u < 5; ++u) *(__attribute__((address_space(3))) u32x2*)(d + u * 8) = u32x2{phk[2 * u], phk[2 * u + 1]};
                    feature_map(wp0, wp1, qf0, qf1, phq);
                }
                if (c == 3) {
                    if (wid < 4) {
                        f32x4 sum = {0.f, 0.f, 0.f, 0.f};
#pragma unroll
                        for (int j = 0; j < 8; ++j) sum += *(const __attribute__((address_space(3))) f32x4*)(L + L_KVG + wid * 8192 + j * 1024 + lane * 16);
                        if (tid < 240) {
                            const int ph = tid / 6, d = 4 * (tid - ph * 6), ph_h = ph / 20, u = ph - ph_h * 20;
                            const unsigned w0 = cvtpk(sum[0], sum[1]), w1 = cvtpk(sum[2], sum[3]);
                            lds_ptr dd = (lds_ptr)lds + L_MISC + d * 96 + ph_h * 48 + u * 2;
                            *(__attribute__((address_space(3))) uint16_t*)(dd) = (uint16_t)(w0 & 0xffffu);
                            *(__attribute__((address_space(3))) uint16_t*)(dd + 96) = (uint16_t)(w0 >> 16);
                            *(__attribute__((address_space(3))) uint16_t*)(dd + 192) = (uint16_t)(w1 & 0xffffu);
                            *(__attribute__((address_space(3))) uint16_t*)(dd + 288) = (uint16_t)(w1 >> 16);
                        }
                    }
                    if (tid == 0) *(__attribute__((address_space(3))) unsigned*)((lds_ptr)lds + L_FLAG) = 0u;
                } else {
                    RAW_BAR();
                    if (c == 0) {
                        LOAD_CHUNK(6, 0); LOAD_CHUNK(3, 1);
                        if (wid < 3 && attempt == 0) {
                            const int g = lane >> 4;
                            f32x4 c0 = {0.f, 0.f, 0.f, 0.f}, c1 = {0.f, 0.f, 0.f, 0.f};
                            const unsigned vb = L_VB + (8 * g + q4) * 48 + 8 * p4, vc = L_VB + (8 * g + q4) * 48 + 32 + (p4 ? 8 : 0);
                            const unsigned pb = L_PHI + (8 * g + q4) * 80 + wid * 32 + p4 * 8;
#pragma unroll 4
                            for (int ks = 0; ks < 8; ++ks) {
                                const bf16x8 a0 = cat8(tr_read(L + vb + ks * 1536), tr_read(L + vb + ks * 1536 + 192));
                                const bf16x8 a1 = cat8(tr_read(L + vc + ks * 1536), tr_read(L + vc + ks * 1536 + 192));
                                const bf16x8 b = cat8(tr_read(L + pb + ks * 2560), tr_read(L + pb + ks * 2560 + 320));
                                c0 = MFMA16(a0, b, c0); c1 = MFMA16(a1, b, c1);
                            }
                            char* dst = ws + WS_KVP + ((size_t)h * 8 + qt) * 3840;
                            const auto rs = __builtin_amdgcn_make_buffer_rsrc(dst, 0, 3840, 0x00020000);
                            const unsigned off = (16 * wid + (lane & 15)) * 96 + 16 * g;
                            __builtin_amdgcn_raw_buffer_store_b128(__builtin_bit_cast(u32x4, c0), rs, off, 0, 16);
                            __builtin_amdgcn_raw_buffer_store_b128(__builtin_bit_cast(u32x4, c1), rs, g < 2 ? off + 64 : 4096u, 0, 16);
                        }
                    }
                    else if (c == 1) {
                        LOAD_CHUNK(7, 2);
                        if (wid == 3) glds4_sc1(ws + WS_FLAG + h * 128, lane * 4, lds0 + L_HF);
                    } else if (wid < 4) {
                        const int fi = (lane & 3) == 3 ? (lane & 28) : (lane & 31);
                        const unsigned f0 = *(const __attribute__((address_space(3))) unsigned*)(L + L_HF + fi * 4);
                        if (!__all(f0 == 1u)) {
                            const unsigned* fl = (const unsigned*)(ws + WS_FLAG) + h * 32;
                            for (int spin = 0; spin < (1 << 22); ++spin) {
                                const unsigned f = __hip_atomic_load(fl + fi, __ATOMIC_RELAXED, __HIP_MEMORY_SCOPE_AGENT);
                                if (__all(f == 1u)) break;
                                __builtin_amdgcn_s_sleep(2);
                            }
                        }
                        const char* pk = ws + WS_KVP + (size_t)h * 8 * 3840; const unsigned o16 = (tid < 240 ? tid : 239) * 16;
                        const unsigned lg = lds0 + wid * 8192;
                        glds_run<1, 0 * 3840, L_KVG + 0 * 1024, true>(pk, o16, lg); glds_run<1, 1 * 3840, L_KVG + 1 * 1024, true>(pk, o16, lg);
                        glds_run<1, 2 * 3840, L_KVG + 2 * 1024, true>(pk, o16, lg); glds_run<1, 3 * 3840, L_KVG + 3 * 1024, true>(pk, o16, lg);
                        glds_run<1, 4 * 3840, L_KVG + 4 * 1024, true>(pk, o16, lg); glds_run<1, 5 * 3840, L_KVG + 5 * 1024, true>(pk, o16, lg);
                        glds_run<1, 6 * 3840, L_KVG + 6 * 1024, true>(pk, o16, lg); glds_run<1, 7 * 3840, L_KVG + 7 * 1024, true>(pk, o16, lg);
                        const char* pw = ws + WS_WOP + (size_t)bi * 51200 + wid * 12800;
                        const unsigned lw = lds0 + wid * 4096;
                        glds_run<4, 0, 0>(pw, l16, lw); glds_run<4, 4096, 0>(pw, l16, lw); glds_run<4, 8192, 0>(pw, l16, lw); glds_run<1, 11776, 0>(pw, l16, lw);
                    }
                }
                pka = L + snext * SLOT + ka; pkb = L + snext * SLOT + kb; pv = L + snext * SLOT + va;
                k0 = ld_k(pka, pkb, 0);
            };
            K2_PAIR(6, turn(), v0 = ld_v(pv, 0), k1 = ld_k(pka, pkb, KVT));
        }
        ob = SMF(v1.v1, HI8(sB), ob);
        {
            lds_ptr d = (lds_ptr)lds + L_VB + (own ^ 1) * 3072 + lane * 16;
#pragma unroll
            for (int a = 0; a < 3; ++a)
                *(__attribute__((address_space(3))) f32x4*)(d + a * 1024) = kh ? f32x4{oa[4 * a], oa[4 * a + 1], oa[4 * a + 2], oa[4 * a + 3]} : f32x4{ob[4 * a], ob[4 * a + 1], ob[4 * a + 2], ob[4 * a + 3]};
        }
        RAW_BAR();
        float o[12];
        {
            lds_cptr d = L + L_VB + own * 3072 + lane * 16;
#pragma unroll
            for (int a = 0; a < 3; ++a) {
                const f32x4 t = *(const __attribute__((address_space(3))) f32x4*)(d + a * 1024);
#pragma unroll
                for (int r = 0; r < 4; ++r) o[4 * a + r] = t[r] + (kh ? ob[4 * a + r] : oa[4 * a + r]);
            }
        }
        const lds_cptr a = L + L_MISC + r32 * 96 + hh * 48;
        f32x16 lo = MFMA32(LDS_B128(a), __builtin_bit_cast(bf16x8, u32x4{phq[0], phq[1], phq[2], phq[3]}), f32x16{});
        lo = MFMA32(LDS_B128(a + 16), __builtin_bit_cast(bf16x8, u32x4{phq[4], phq[5], phq[6], phq[7]}), lo);
        lo = MFMA32K8(LDS_B64(a + 32), __builtin_bit_cast(s16x4, u32x2{phq[8], phq[9]}), lo);
        auto rr = __builtin_amdgcn_permlane32_swap(__float_as_uint(o[8]), __float_as_uint(o[8]), false, false);
        const float lsum = __uint_as_float(rr[1]);
        const float sa = gate * __builtin_amdgcn_rcpf(lsum);
        auto rb = __builtin_amdgcn_permlane32_swap(__float_as_uint(lo[8]), __float_as_uint(lo[8]), false, false);
        const float sb = (1.0f - gate) / (__uint_as_float(rb[1]) + 1e-6f);
        char* oc = ws + WS_OC + ((size_t)h * 5 * SEQ + (qt * 8 + own) * 32 + r32) * 16;
        *(u32x4*)(oc + (size_t)(hh ? 3 : 0) * SEQ * 16) = u32x4{cvtpk(o[0] * sa, o[1] * sa), cvtpk(o[2] * sa, o[3] * sa), cvtpk(o[4] * sa, o[5] * sa), cvtpk(o[6] * sa, o[7] * sa)};
        *(u32x4*)(oc + (size_t)(hh ? 4 : 2) * SEQ * 16) = u32x4{cvtpk(lo[0] * sb, lo[1] * sb), cvtpk(lo[2] * sb, lo[3] * sb), cvtpk(lo[4] * sb, lo[5] * sb), cvtpk(lo[6] * sb, lo[7] * sb)};
        if (!hh) *(u32x4*)(oc + (size_t)SEQ * 16) = u32x4{cvtpk(o[8] * sa, o[9] * sa), cvtpk(o[10] * sa, o[11] * sa), cvtpk(lo[8] * sb, lo[9] * sb), cvtpk(lo[10] * sb, lo[11] * sb)};
        if (attempt == 1) break;
        if (__any(!(lsum >= 5.4210109e-20f)))
            *(__attribute__((address_space(3))) unsigned*)((lds_ptr)lds + L_FLAG) = 1u;
        RAW_BAR();
        const unsigned redo = *(const __attribute__((address_space(3))) unsigned*)(L + L_FLAG);
        if (__builtin_amdgcn_readfirstlane(redo) == 0u) break;
        float ma = -__builtin_inff(), mb = -__builtin_inff();
        for (int t = 0; t < 64; ++t) {
            const char* tb = kvbase + (size_t)t * KVT;
            const bf16x8 g0 = *(const bf16x8*)(tb + ka);
            const s16x4 g1 = *(const s16x4*)(tb + kb);
            f32x16 s = MFMA32(g0, QA0, f32x16{}); s = MFMA32K8(g1, QA1, s);
            f32x16 u = MFMA32(g0, QB0, f32x16{}); u = MFMA32K8(g1, QB1, u);
#pragma unroll
            for (int r = 0; r < 16; ++r) { ma = fmaxf(ma, s[r]); mb = fmaxf(mb, u[r]); }
        }
        nba = -xhalf_max(ma); nbb = -xhalf_max(mb);
        RAW_BAR();
        LOAD_FIRST(0); LOAD_FIRST(1);
        WAIT_VM(0);
        RAW_BAR();
    }
}

constexpr int K3_STAGE = 36864;
__global__ __launch_bounds__(512) void k3_out(const char* __restrict__ ws, uint16_t* __restrict__ out) {
    __shared__ __attribute__((aligned(16))) char lds[4 * K3_STAGE];
    const int bid = blockIdx.x, xcd = bid & 7, bi = bid >> 3;
    const int tt = xcd * 4 + (bi & 3), ct = bi >> 2;
    const int tid = threadIdx.x, lane = tid & 63;
    const int wid = __builtin_amdgcn_readfirstlane(tid >> 6);
    const unsigned lds0 = (unsigned)(uintptr_t)lds;
    const char* sbase; unsigned sstep, voff[5], ldst;
    if (wid < 4) {
        sbase = ws + WS_OC + (size_t)tt * 1024; sstep = 16 * SEQ * 16; ldst = wid * 4096;
#pragma unroll
        for (int j = 0; j < 5; ++j) voff[j] = (wid * 4 + j) * (SEQ * 16) + lane * 16;
    } else {
        sbase = ws + WS_WOP + (size_t)ct * 1280; sstep = 16 * 640 * 16; ldst = 16384 + (wid - 4) * 5120;
#pragma unroll
        for (int j = 0; j < 5; ++j) { const int q = ((wid - 4) * 5 + j) * 64 + lane, c = q / 80, n = q - c * 80; voff[j] = c * (640 * 16) + n * 16; }
    }
    auto stage = [&](int s) {
        const char* sb = sbase + (size_t)s * sstep;
        const unsigned dst = lds0 + (s & 3) * K3_STAGE + ldst;
#pragma unroll
        for (int j = 0; j < 4; ++j) glds16(sb, voff[j], dst + j * 1024);
        if (wid >= 4) glds16(sb, voff[4], dst + 4096);
    };
    stage(0); stage(1); stage(2);
    const int g = lane >> 4, l15 = lane & 15;
    const unsigned oa = g * 1024 + (16 * (wid & 3) + l15) * 16;
    const unsigned wa = 16384 + g * 1280 + (20 * (l15 >> 2) + (l15 & 3)) * 16;
    f32x4 acc[5];
#pragma unroll
    for (int i = 0; i < 5; ++i) acc[i] = f32x4{0.f, 0.f, 0.f, 0.f};
#pragma unroll
    for (int s = 0; s < 10; ++s) {
        if (wid < 4) { if (s < 8) { WAIT_VM(8); } else if (s == 8) { WAIT_VM(4); } else { WAIT_VM(0); } }
        else { if (s < 8) { WAIT_VM(10); } else if (s == 8) { WAIT_VM(5); } else { WAIT_VM(0); } }
        RAW_BAR();
        if (s + 3 < 10) stage(s + 3);
        if (wid < 4) {
            const lds_cptr buf = (lds_cptr)lds + (s & 3) * K3_STAGE;
#pragma unroll
            for (int ks = 0; ks < 4; ++ks) {
                const bf16x8 of = LDS_B128(buf + oa + ks * 4096);
#pragma unroll
                for (int i = 0; i < 5; ++i) acc[i] = MFMA16(LDS_B128(buf + wa + ks * 5120 + i * 64), of, acc[i]);
            }
        }
    }
    if (wid < 4) {
        char* dst = (char*)out + (size_t)(tt * 64 + 16 * wid + l15) * 1280 + ct * 160 + g * 40;
#pragma unroll
        for (int i = 0; i < 5; ++i) *(u32x2*)(dst + i * 8) = u32x2{cvtpk(acc[i][0], acc[i][1]), cvtpk(acc[i][2], acc[i][3])};
    }
}

extern "C" void kernel_launch(void* const* d_in, const int* in_sizes, int n_in, void* d_out, int out_size, void* d_ws, size_t ws_size,
                              hipStream_t stream) {
    const uint16_t* x = (const uint16_t*)d_in[0];
    const uint16_t* wA = (const uint16_t*)d_in[1];
    const uint16_t* woA = (const uint16_t*)d_in[2];
    const uint16_t* wB = (const uint16_t*)d_in[4];
    const uint16_t* wphi = (const uint16_t*)d_in[5];
    const uint16_t* woB = (const uint16_t*)d_in[6];
    const uint16_t* gatew = (const uint16_t*)d_in[7];
    char* ws = (char*)d_ws;
    k1_qkv<<<256, 768, 0, stream>>>(x, wA, wB, woA, woB, wphi, gatew, ws);
    k2_attn<<<256, 512, 0, stream>>>(ws);
    k3_out<<<256, 512, 0, stream>>>(ws, (uint16_t*)d_out);
}
```

```cpp
#include <hip/hip_runtime.h>
#include <hip/hip_bf16.h>
#include <stdint.h>

typedef __attribute__((ext_vector_type(8))) __bf16 bf16x8;
typedef __attribute__((ext_vector_type(16))) __bf16 bf16x16;
typedef __attribute__((ext_vector_type(4))) short s16x4;
typedef __attribute__((ext_vector_type(8))) short s16x8;
typedef __attribute__((ext_vector_type(4))) float f32x4;
typedef __attribute__((ext_vector_type(16))) float f32x16;
typedef __attribute__((ext_vector_type(4))) unsigned u32x4;
typedef __attribute__((ext_vector_type(2))) unsigned u32x2;
typedef __attribute__((ext_vector_type(2))) __bf16 bf16x2;
typedef __attribute__((ext_vector_type(2))) float f32x2;
typedef __attribute__((address_space(3))) const char* lds_cptr;
typedef __attribute__((address_space(3))) char* lds_ptr;
typedef unsigned long long u64;
#define AS3(T) __attribute__((address_space(3))) T

#define B_DELAY 32
constexpr int SEQ = 2048, NH = 32;
constexpr float LOG2E = 1.4426950408889634f;
constexpr float QSCALE = 0.22360679774997896f * 1.4426950408889634f;

constexpr int KVT = 2816;
constexpr int QFT = 1280;
constexpr unsigned WS_KV = 0;
constexpr unsigned WS_QA = WS_KV + NH * 64 * KVT;
constexpr unsigned WS_PHQ = WS_QA + NH * 64 * QFT;
constexpr unsigned WS_WOP = WS_PHQ + NH * 64 * 2560;
constexpr unsigned WS_OC = WS_WOP + 160 * 640 * 16;
constexpr unsigned WS_KMAX = WS_OC + 160 * SEQ * 16;
constexpr unsigned WS_G = WS_KMAX + NH * 64 * 4;
constexpr unsigned WS_KVP = WS_G + SEQ * 4;
constexpr unsigned WS_FT = WS_KVP + NH * 16 * 3840;
constexpr unsigned WS_FH = WS_FT + 1024;
constexpr unsigned WS_FOB = WS_FH + 8192;
constexpr unsigned WS_FOA = WS_FOB + 2048;
constexpr unsigned WS_EPOCH = WS_FOA + 2048;
constexpr unsigned WS_END = WS_EPOCH + 128;

__device__ __forceinline__ unsigned cvtpk(float lo, float hi) {
    return __builtin_bit_cast(unsigned, __builtin_convertvector(f32x2{lo, hi}, bf16x2)); }
__device__ __forceinline__ float bflo(unsigned w) { return __uint_as_float(w << 16); }
__device__ __forceinline__ float bfhi(unsigned w) { return __uint_as_float(w & 0xffff0000u); }
#define GL1(P) "global_load_lds_dwordx4 %1, %2" P "\n\t"
#define GL2(P) GL1(P) "global_load_lds_dwordx4 %1, %2 offset:1024" P "\n\t"
#define GL3(P) GL2(P) "global_load_lds_dwordx4 %1, %2 offset:2048" P "\n\t"
#define GL4(P) GL3(P) "global_load_lds_dwordx4 %1, %2 offset:3072" P "\n\t"
#define GLASM(BODY) asm volatile("s_mov_b32 %0, m0\n\ts_add_u32 m0, %3, %4\n\ts_nop 0\n\t" BODY "s_mov_b32 m0, %0" \
                     : "=&s"(sv) : "v"(voff), "s"(t), "s"(lbase), "i"(LOFF) : "memory", "scc")
template <int N, int GOFF, int LOFF, bool SC1>
__device__ __forceinline__ void glds_run(const void* sbase, unsigned voff, unsigned lbase) {
    static_assert(N >= 1 && N <= 4, "");
    unsigned sv, tl, th;
    const unsigned lo = (unsigned)(uintptr_t)sbase, hi = (unsigned)((uintptr_t)sbase >> 32);
    asm volatile("s_add_u32 %0, %2, %4\n\ts_addc_u32 %1, %3, 0" : "=&s"(tl), "=s"(th) : "s"(lo), "s"(hi), "i"(GOFF) : "scc");
    const uint64_t t = ((uint64_t)th << 32) | tl;
    if constexpr (SC1) {
        if constexpr (N == 1) GLASM(GL1(" sc1")); else if constexpr (N == 2) GLASM(GL2(" sc1")); else if constexpr (N == 3) GLASM(GL3(" sc1")); else GLASM(GL4(" sc1"));
    } else {
        if constexpr (N == 1) GLASM(GL1("")); else if constexpr (N == 2) GLASM(GL2("")); else if constexpr (N == 3) GLASM(GL3("")); else GLASM(GL4(""));
    }
}
__device__ __forceinline__ s16x4 tr_read(lds_cptr p) { return __builtin_amdgcn_ds_read_tr16_b64_v4i16((AS3(s16x4)*)p); }
__device__ __forceinline__ bf16x8 cat8(s16x4 a, s16x4 b) {
    s16x8 r = {a[0], a[1], a[2], a[3], b[0], b[1], b[2], b[3]}; return __builtin_bit_cast(bf16x8, r); }
#define LDS_B128(p) (*(const AS3(bf16x8)*)(p))
#define LDS_B64(p) (*(const AS3(s16x4)*)(p))
#define MFMA16(a, b, c) __builtin_amdgcn_mfma_f32_16x16x32_bf16(a, b, c, 0, 0, 0)
#define MFMA32(a, b, c) __builtin_amdgcn_mfma_f32_32x32x16_bf16(a, b, c, 0, 0, 0)
#define MFMA32K8(a, b, c) __builtin_amdgcn_mfma_f32_32x32x8bf16_1k(a, b, c, 0, 0, 0)
#define WAIT_VM(n) asm volatile("s_waitcnt vmcnt(" #n ")" ::: "memory")
#define RAW_BAR() asm volatile("s_waitcnt lgkmcnt(0)\n\ts_barrier" ::: "memory")
#define SBAR() __builtin_amdgcn_sched_barrier(0)
#define EXP2(v) __builtin_amdgcn_exp2f(v)
#define SMF(a, lo, acc) __builtin_amdgcn_smfmac_f32_32x32x32_bf16(a, __builtin_bit_cast(bf16x16, lo), acc, 0xDDDD, 0, 0)
#define LO8(s) __builtin_shufflevector(s, s, 0, 1, 2, 3, 4, 5, 6, 7)
#define HI8(s) __builtin_shufflevector(s, s, 8, 9, 10, 11, 12, 13, 14, 15)
#define ST128(v, off) __builtin_amdgcn_raw_buffer_store_b128(v, wsr, off, 0, 16)
#define ST64(v, off) __builtin_amdgcn_raw_buffer_store_b64(v, wsr, off, 0, 16)
#define ST32(v, off) __builtin_amdgcn_raw_buffer_store_b32(v, wsr, off, 0, 16)
__device__ __forceinline__ float xhalf_max(float v) {
    auto r = __builtin_amdgcn_permlane32_swap(__float_as_uint(v), __float_as_uint(v), false, false);
    return fmaxf(__uint_as_float(r[0]), __uint_as_float(r[1])); }
__device__ __forceinline__ float xhalf_min(float v) {
    auto r = __builtin_amdgcn_permlane32_swap(__float_as_uint(v), __float_as_uint(v), false, false);
    return fminf(__uint_as_float(r[0]), __uint_as_float(r[1])); }
__device__ __forceinline__ float xhalf_sum(float v) {
    auto r = __builtin_amdgcn_permlane32_swap(__float_as_uint(v), __float_as_uint(v), false, false);
    return __uint_as_float(r[0]) + __uint_as_float(r[1]); }
__device__ __forceinline__ float row_max16(float v) {
    v = fmaxf(v, __int_as_float(__builtin_amdgcn_update_dpp(0, __float_as_int(v), 0x128, 0xf, 0xf, false)));
    v = fmaxf(v, __int_as_float(__builtin_amdgcn_update_dpp(0, __float_as_int(v), 0x124, 0xf, 0xf, false)));
    v = fmaxf(v, __int_as_float(__builtin_amdgcn_update_dpp(0, __float_as_int(v), 0x122, 0xf, 0xf, false)));
    v = fmaxf(v, __int_as_float(__builtin_amdgcn_update_dpp(0, __float_as_int(v), 0x121, 0xf, 0xf, false)));
    return v; }
__device__ __forceinline__ void publish(char* ws, unsigned off, u64 tag) {
    __hip_atomic_store((u64*)(ws + off), tag, __ATOMIC_RELAXED, __HIP_MEMORY_SCOPE_AGENT); }
__device__ __forceinline__ void wait_flags(const char* ws, unsigned off, u64 tag) {
    for (int spin = 0; spin < (1 << 20); ++spin) {
        const u64 f = __hip_atomic_load((const u64*)(ws + off), __ATOMIC_RELAXED, __HIP_MEMORY_SCOPE_AGENT);
        if (__all(f == tag)) break;
        __builtin_amdgcn_s_sleep(2);
    }
    asm volatile("" ::: "memory");
}

struct KFrag { bf16x8 k0; s16x4 k1; };
struct VFrag { bf16x8 v0, v1; };
__device__ __forceinline__ KFrag ld_k(lds_cptr pa, lds_cptr pb, int off) { KFrag k; k.k0 = LDS_B128(pa + off); k.k1 = LDS_B64(pb + off); return k; }
__device__ __forceinline__ VFrag ld_v2(lds_cptr p, lds_cptr q, int off) {
    VFrag v; v.v0 = cat8(tr_read(p + off), tr_read(q + off)); v.v1 = cat8(tr_read(p + off + 256), tr_read(q + off + 256)); return v; }
#define ld_v(p, off) ld_v2(p, p##2, off)
__device__ __forceinline__ void attn_step(f32x16& sc, f32x16& sn, const KFrag& kn, const bf16x8& vprev1, const bf16x8& vc0,
                                          const bf16x8& qn0, const s16x4& qn1, const f32x16& negbn, f32x16& oprev, f32x16& oc) {
    oprev = SMF(vprev1, HI8(sn), oprev); SBAR();
#pragma unroll
    for (int r = 0; r < 4; ++r) sc[r] = EXP2(sc[r]);
    SBAR(); sn = MFMA32(kn.k0, qn0, negbn); SBAR();
#pragma unroll
    for (int r = 4; r < 8; ++r) sc[r] = EXP2(sc[r]);
    SBAR(); sn = MFMA32K8(kn.k1, qn1, sn); SBAR();
#pragma unroll
    for (int r = 8; r < 12; ++r) sc[r] = EXP2(sc[r]);
    SBAR(); oc = SMF(vc0, LO8(sc), oc); SBAR();
#pragma unroll
    for (int r = 12; r < 16; ++r) sc[r] = EXP2(sc[r]);
    SBAR();
}
__device__ __forceinline__ void feature_map(const u32x4 wp0, const u32x2 wp1, const u32x4 f0, const u32x2 f1, unsigned (&ph)[10]) {
    f32x16 pq = MFMA32(__builtin_bit_cast(bf16x8, wp0), __builtin_bit_cast(bf16x8, f0), f32x16{});
    pq = MFMA32K8(__builtin_bit_cast(s16x4, wp1), __builtin_bit_cast(s16x4, f1), pq);
    float mx = pq[0], mn = pq[0];
#pragma unroll
    for (int u = 1; u < 10; ++u) { mx = fmaxf(mx, pq[u]); mn = fminf(mn, pq[u]); }
    mx = xhalf_max(mx); mn = xhalf_min(mn);
    float ep[10], en[10], sp = 0.f, sn = 0.f;
    const float cmx = -mx * LOG2E, cmn = mn * LOG2E;
#pragma unroll
    for (int u = 0; u < 10; ++u) {
        ep[u] = EXP2(fmaf(pq[u], LOG2E, cmx)); sp += ep[u];
        en[u] = EXP2(fmaf(pq[u], -LOG2E, cmn)); sn += en[u];
    }
    sp = __builtin_amdgcn_rcpf(xhalf_sum(sp)); sn = __builtin_amdgcn_rcpf(xhalf_sum(sn));
#pragma unroll
    for (int u = 0; u < 5; ++u) { ph[u] = cvtpk(ep[2 * u] * sp, ep[2 * u + 1] * sp); ph[5 + u] = cvtpk(en[2 * u] * sn, en[2 * u + 1] * sn); }
}
__device__ __forceinline__ float qnorm2(const u32x4& a, const u32x2& b) {
    float n2 = 0.f;
#pragma unroll
    for (int j = 0; j < 4; ++j) { n2 = fmaf(bflo(a[j]), bflo(a[j]), n2); n2 = fmaf(bfhi(a[j]), bfhi(a[j]), n2); }
#pragma unroll
    for (int j = 0; j < 2; ++j) { n2 = fmaf(bflo(b[j]), bflo(b[j]), n2); n2 = fmaf(bfhi(b[j]), bfhi(b[j]), n2); }
    return xhalf_sum(n2);
}

constexpr int P1_STAGE = 49152;
constexpr int SLOT = 8 * KVT;
constexpr int L_G = 5 * SLOT;
constexpr int L_MISC = 3 * P1_STAGE;
constexpr int L_GATE = L_MISC + 3072;
constexpr int L_WPH = L_GATE + 1024;
constexpr int L_FLAG = L_WPH + 4096;
static_assert(L_G + 32768 <= L_MISC, "");
constexpr int P3_STAGE = 36864;
constexpr int LDS_BYTES = L_FLAG + 16;
static_assert(LDS_BYTES >= 3 * P1_STAGE && LDS_BYTES >= 4 * P3_STAGE && LDS_BYTES <= 163840, "");

__global__ __launch_bounds__(512) void fused_kernel(const uint16_t* __restrict__ x, const uint16_t* __restrict__ wA, const uint16_t* __restrict__ wB,
                                                    const uint16_t* __restrict__ woA, const uint16_t* __restrict__ woB,
                                                    const uint16_t* __restrict__ wphi, const uint16_t* __restrict__ gatew,
                                                    char* __restrict__ ws, uint16_t* __restrict__ out) {
    __shared__ __attribute__((aligned(16))) char lds[LDS_BYTES];
    const int bid = blockIdx.x, xcd = bid & 7, bi = bid >> 3;
    const int tid = threadIdx.x, lane = tid & 63;
    const int wid = __builtin_amdgcn_readfirstlane(tid >> 6);
    const unsigned lds0 = (unsigned)(uintptr_t)lds;
    const lds_cptr L = (lds_cptr)lds;
    const unsigned l16 = lane * 16;
    const int g = lane >> 4, l15 = lane & 15, q4 = l15 >> 2, p4 = lane & 3, r32 = lane & 31, hh = lane >> 5;
    const auto wsr = __builtin_amdgcn_make_buffer_rsrc(ws, 0, WS_END, 0x00020000);
    u64 tag;
    {
        const int rt = (xcd & 1) * 8 + (bi & 7), br = bi >> 4, hj = (xcd >> 1) * 2 + ((bi >> 3) & 1);
        const char* xb = (const char*)x + (size_t)rt * 128 * 1280;
        const char* wb = (const char*)(br ? wB : wA) + hj * 160;
        unsigned vx[2], vw[4];
#pragma unroll
        for (int j = 0; j < 2; ++j) { const int row = 8 * (wid * 2 + j) + (lane >> 3); vx[j] = row * 1280 + (((lane ^ row) & 7) << 4); }
#pragma unroll
        for (int j = 0; j < 4; ++j) {
            const int p = wid * 4 + j;
            int q = (p & 7) * 64 + lane; const int up = q >= 248; q -= up ? 248 : 0; q = q < 239 ? q : 239;
            const int row = __umul24(q, 2185) >> 16, ch = q - row * 30, sgm = __umul24(ch, 6554) >> 16;
            vw[j] = __umul24(16 * (p >> 3) + 8 * up + row, 3840) + sgm * 1120 + ch * 16;
        }
        const unsigned lx = lds0 + wid * 2048, lw = lds0 + 16384 + wid * 4096;
#define P1_DMA(s, j) do { if ((j) < 2) glds_run<1, (s) * 128, ((s) % 3) * P1_STAGE + ((j) & 1) * 1024, false>(xb, vx[(j) & 1], lx); \
                          else glds_run<1, (s) * 245760, ((s) % 3) * P1_STAGE + (((j) - 2) & 3) * 1024, false>(wb, vw[((j) - 2) & 3], lw); } while (0)
#define P1_STAGE_ALL(s) do { P1_DMA(s, 0); P1_DMA(s, 1); P1_DMA(s, 2); P1_DMA(s, 3); P1_DMA(s, 4); P1_DMA(s, 5); } while (0)
        P1_STAGE_ALL(0);
        asm volatile("s_barrier" ::: "memory");
        P1_STAGE_ALL(1); P1_STAGE_ALL(2);
        u32x2 ep;
        { const unsigned z = 0; asm volatile("global_load_dwordx2 %0, %1, %2 sc1" : "=v"(ep) : "v"(z), "s"(ws + WS_EPOCH)); }
        if (wid < 4) { const unsigned o = wid * 1024 + l16; glds_run<1, 0, L_WPH, false>((const char*)wphi + hj * 3200, o < 3200 ? o : 0u, lds0 + wid * 1024); }

        const int th = wid >> 2, cw = wid & 3;
        const int xrow = 64 * th + l15;
        const unsigned xa0 = xrow * 128 + ((g ^ (xrow & 7)) << 4), xa1 = xa0 ^ 64;
        const unsigned wa0 = 16384 + 8192 * (g >> 1) + 480 * (8 * (g & 1) + q4) + 128 * (g & 1) + 40 * p4 + (cw < 3 ? 160 * cw : 32);
        const unsigned wst = cw < 3 ? 8 : 160;
        unsigned wa[4];
#pragma unroll
        for (int i = 0; i < 4; ++i) wa[i] = wa0 + (i < 3 || cw < 3 ? i : 0) * wst;
        f32x4 acc[4][4];
#pragma unroll
        for (int m = 0; m < 4; ++m)
#pragma unroll
            for (int i = 0; i < 4; ++i) acc[m][i] = f32x4{0.f, 0.f, 0.f, 0.f};
        bf16x8 xA[4], wA_[4], xB[4], wB_[4];
#define P1_LOAD(X, W, slot, ks) do { const lds_cptr buf_ = L + (slot) * P1_STAGE; \
        _Pragma("unroll") for (int i = 0; i < 4; ++i) W[i] = cat8(tr_read(buf_ + wa[i] + (ks) * 16384), tr_read(buf_ + wa[i] + (ks) * 16384 + 1920)); \
        _Pragma("unroll") for (int m = 0; m < 4; ++m) X[m] = LDS_B128(buf_ + ((ks) ? xa1 : xa0) + m * 2048); } while (0)
#define P1_MMA(X, W, i) do { if ((i) < 3 || cw < 3) { _Pragma("unroll") for (int m = 0; m < 4; ++m) acc[m][i] = MFMA16(W[i], X[m], acc[m][i]); } } while (0)
#define P1_ITER(s) do { \
        P1_LOAD(xB, wB_, (s) % 3, 1); SBAR(); \
        P1_MMA(xA, wA_, 0); P1_MMA(xA, wA_, 1); P1_MMA(xA, wA_, 2); P1_MMA(xA, wA_, 3); SBAR(); \
        if ((s) < 9) { \
            if ((s) < 2) { WAIT_VM(7); } else if ((s) < 8) { WAIT_VM(6); } else { WAIT_VM(0); } \
            RAW_BAR(); \
            P1_LOAD(xA, wA_, ((s) + 1) % 3, 0); SBAR(); \
        } \
        P1_MMA(xB, wB_, 0); SBAR(); if ((s) < 7) { P1_DMA((s) < 7 ? (s) + 3 : 0, 0); P1_DMA((s) < 7 ? (s) + 3 : 0, 1); SBAR(); } \
        P1_MMA(xB, wB_, 1); SBAR(); if ((s) < 7) { P1_DMA((s) < 7 ? (s) + 3 : 0, 2); P1_DMA((s) < 7 ? (s) + 3 : 0, 3); SBAR(); } \
        P1_MMA(xB, wB_, 2); SBAR(); if ((s) < 7) { P1_DMA((s) < 7 ? (s) + 3 : 0, 4); P1_DMA((s) < 7 ? (s) + 3 : 0, 5); SBAR(); } \
        P1_MMA(xB, wB_, 3); SBAR(); } while (0)
        WAIT_VM(13);
        RAW_BAR();
        P1_LOAD(xA, wA_, 0, 0);
        P1_ITER(0); P1_ITER(1); P1_ITER(2); P1_ITER(3); P1_ITER(4); P1_ITER(5); P1_ITER(6); P1_ITER(7); P1_ITER(8); P1_ITER(9);
        {
            asm volatile("" : "+v"(ep));
            u64 z = ((u64)(unsigned)__builtin_amdgcn_readfirstlane(ep[1]) << 32) | (unsigned)__builtin_amdgcn_readfirstlane(ep[0]);
            z = z * 0x9E3779B97F4A7C15ull + 0x632BE59BD9B4E019ull; z ^= z >> 29; z *= 0xBF58476D1CE4E5B9ull; z ^= z >> 32;
            tag = z;
        }
        const unsigned ka = lane * 16, kb = 1024 + r32 * 8;
        const unsigned fhoff = tid < 4 ? WS_FH + (((4 * hj + tid) * 2 + br) * 16 + rt) * 8 : WS_FT + (rt * 8 + hj) * 8;
        if (br == 0) {
            const int ht = (4 * hj + g) * 64 + rt * 4 + 2 * th;
            float kn = 0.f;
            if (cw < 3) {
                const unsigned ts = cw == 0 ? QFT : KVT, tb = (cw == 0 ? WS_QA : WS_KV + (cw == 2 ? 1280 : 0)) + ht * ts;
                const unsigned o0 = tb + l15 * 16, o1 = tb + 512 + (cw == 2 ? l15 ^ 4 : l15) * 16;
                if (cw == 0) {
#pragma unroll
                    for (int m = 0; m < 4; ++m)
#pragma unroll
                        for (int i = 0; i < 4; ++i) acc[m][i] *= QSCALE;
                }
#pragma unroll
                for (int m = 0; m < 4; ++m) {
                    const unsigned mo = (m >> 1) * ts + (m & 1) * 256;
                    ST128((u32x4{cvtpk(acc[m][0][0], acc[m][0][1]), cvtpk(acc[m][0][2], acc[m][0][3]), cvtpk(acc[m][1][0], acc[m][1][1]), cvtpk(acc[m][1][2], acc[m][1][3])}), o0 + mo);
                    ST128((u32x4{cvtpk(acc[m][2][0], acc[m][2][1]), cvtpk(acc[m][2][2], acc[m][2][3]), cvtpk(acc[m][3][0], acc[m][3][1]), cvtpk(acc[m][3][2], acc[m][3][3])}), o1 + mo);
                }
                if (cw == 1) {
#pragma unroll
                    for (int m = 0; m < 4; ++m) {
                        float n2 = 0.f;
#pragma unroll
                        for (int i = 0; i < 4; ++i)
#pragma unroll
                            for (int r = 0; r < 4; ++r) n2 = fmaf(acc[m][i][r], acc[m][i][r], n2);
                        kn = (n2 == n2) ? fmaxf(kn, n2) : __builtin_inff();
                    }
                }
            } else {
#pragma unroll
                for (int m = 0; m < 4; ++m) {
                    const unsigned oq = WS_QA + (ht + (m >> 1)) * QFT + 1024, ok = WS_KV + (ht + (m >> 1)) * KVT + 1024;
                    ST64((u32x2{cvtpk(acc[m][0][0] * QSCALE, acc[m][0][1] * QSCALE), cvtpk(acc[m][0][2] * QSCALE, acc[m][0][3] * QSCALE)}), oq + (m & 1) * 128 + l15 * 8);
                    ST64((u32x2{cvtpk(acc[m][1][0], acc[m][1][1]), cvtpk(acc[m][1][2], acc[m][1][3])}), ok + (m & 1) * 128 + l15 * 8);
                    ST128((u32x4{cvtpk(acc[m][2][0], acc[m][2][1]), cvtpk(acc[m][2][2], acc[m][2][3]), 0x00003f80u, 0u}), ok + 1280 + (m & 1) * 256 + (l15 ^ 8) * 16);
                    float n2 = 0.f;
#pragma unroll
                    for (int r = 0; r < 4; ++r) n2 = fmaf(acc[m][1][r], acc[m][1][r], n2);
                    kn = (n2 == n2) ? fmaxf(kn, n2) : __builtin_inff();
                }
            }
            if (cw & 1) {
                kn = row_max16(kn);
                if (l15 == 0) ST32(__float_as_uint(kn), WS_KMAX + ((4 * hj + g) * 64 + (cw == 3 ? 32 : 0) + rt * 2 + th) * 4);
            }
            WAIT_VM(0);
            asm volatile("s_barrier" ::: "memory");
            if (tid < 4) publish(ws, fhoff, tag);
            const int gtok = rt * 128 + hj * 16 + 2 * wid + hh;
            const u32x4* px = (const u32x4*)((const char*)x + (size_t)gtok * 1280);
            const u32x4* pg = (const u32x4*)gatew;
            u32x4 xv[3], gv[3];
#pragma unroll
            for (int j = 0; j < 3; ++j) { const int o = j < 2 ? j * 32 + r32 : 64 + l15; xv[j] = px[o]; gv[j] = pg[o]; }
            const int it = (rt * 8 + hj) * 400 + (tid < 400 ? tid : 0), wbr = it >= 25600, i2 = it - wbr * 25600, ch = i2 / 320, n2 = i2 - ch * 320;
            const int r0 = ch < 64 ? (ch >> 1) * 20 + (ch & 1) * 4 : (ch - 64) * 40 + 16, r1 = r0 + (ch < 64 ? 8 : 20);
            const unsigned* pw = (const unsigned*)(wbr ? woB : woA) + n2;
            unsigned wo[8];
#pragma unroll
            for (int e = 0; e < 4; ++e) { wo[e] = pw[(r0 + e) * 320]; wo[4 + e] = pw[(r1 + e) * 320]; }
            float d = 0.f, e = 0.f;
#pragma unroll
            for (int j = 0; j < 4; ++j) {
                d = fmaf(bflo(xv[0][j]), bflo(gv[0][j]), d); d = fmaf(bfhi(xv[0][j]), bfhi(gv[0][j]), d);
                d = fmaf(bflo(xv[1][j]), bflo(gv[1][j]), d); d = fmaf(bfhi(xv[1][j]), bfhi(gv[1][j]), d);
                e = fmaf(bflo(xv[2][j]), bflo(gv[2][j]), e); e = fmaf(bfhi(xv[2][j]), bfhi(gv[2][j]), e);
            }
            d += r32 < 16 ? e : 0.f;
#pragma unroll
            for (int o = 1; o < 32; o <<= 1) d += __shfl_xor(d, o);
            if (r32 == 0) ST32(__float_as_uint(1.0f / (1.0f + __expf(-d))), WS_G + gtok * 4);
            if (tid < 400) {
                ST128((u32x4{__builtin_amdgcn_perm(wo[1], wo[0], 0x05040100), __builtin_amdgcn_perm(wo[3], wo[2], 0x05040100),
                             __builtin_amdgcn_perm(wo[5], wo[4], 0x05040100), __builtin_amdgcn_perm(wo[7], wo[6], 0x05040100)}), WS_WOP + it * 32);
                ST128((u32x4{__builtin_amdgcn_perm(wo[1], wo[0], 0x07060302), __builtin_amdgcn_perm(wo[3], wo[2], 0x07060302),
                             __builtin_amdgcn_perm(wo[5], wo[4], 0x07060302), __builtin_amdgcn_perm(wo[7], wo[6], 0x07060302)}), WS_WOP + it * 32 + 16);
            }
        } else {
            const lds_ptr img = (lds_ptr)lds + P1_STAGE;
            if (cw < 3) {
#pragma unroll
                for (int m = 0; m < 4; ++m) {
                    const lds_ptr d = img + ((cw * 4 + 2 * th + (m >> 1)) * 4 + g) * 1536 + (m & 1) * 256;
                    *(AS3(u32x4)*)(d + l15 * 16) = u32x4{cvtpk(acc[m][0][0], acc[m][0][1]), cvtpk(acc[m][0][2], acc[m][0][3]), cvtpk(acc[m][1][0], acc[m][1][1]), cvtpk(acc[m][1][2], acc[m][1][3])};
                    *(AS3(u32x4)*)(d + 512 + (cw == 2 ? l15 ^ 4 : l15) * 16) = u32x4{cvtpk(acc[m][2][0], acc[m][2][1]), cvtpk(acc[m][2][2], acc[m][2][3]), cvtpk(acc[m][3][0], acc[m][3][1]), cvtpk(acc[m][3][2], acc[m][3][3])};
                }
            } else {
#pragma unroll
                for (int m = 0; m < 4; ++m) {
                    const lds_ptr d = img + ((2 * th + (m >> 1)) * 4 + g) * 1536 + 1024;
                    *(AS3(u32x2)*)(d + (m & 1) * 128 + l15 * 8) = u32x2{cvtpk(acc[m][0][0], acc[m][0][1]), cvtpk(acc[m][0][2], acc[m][0][3])};
                    *(AS3(u32x2)*)(d + 16 * 1536 + (m & 1) * 128 + l15 * 8) = u32x2{cvtpk(acc[m][1][0], acc[m][1][1]), cvtpk(acc[m][1][2], acc[m][1][3])};
                    *(AS3(u32x4)*)(d + 32 * 1536 + (m & 1) * 256 + (l15 ^ 8) * 16) = u32x4{cvtpk(acc[m][2][0], acc[m][2][1]), cvtpk(acc[m][2][2], acc[m][2][3]), 0x00003f80u, 0u};
                }
            }
            const int fr = (r32 & 3) + 4 * (r32 >> 3);
            const lds_cptr wp = L + L_WPH + cw * 800 + (fr < 10 ? 10 * ((r32 >> 2) & 1) + fr : 0) * 2;
            unsigned wv[12];
#pragma unroll
            for (int j = 0; j < 8; ++j) wv[j] = *(const AS3(uint16_t)*)(wp + hh * 320 + j * 40);
#pragma unroll
            for (int j = 0; j < 4; ++j) wv[8 + j] = *(const AS3(uint16_t)*)(wp + 640 + j * 40);
            const unsigned m0 = fr < 10 ? 0xffffffffu : 0u, m1 = (fr < 10 && hh == 0) ? 0xffffffffu : 0u;
            const u32x4 wp0 = {(wv[0] | (wv[1] << 16)) & m0, (wv[2] | (wv[3] << 16)) & m0, (wv[4] | (wv[5] << 16)) & m0, (wv[6] | (wv[7] << 16)) & m0};
            const u32x2 wp1 = {(wv[8] | (wv[9] << 16)) & m1, (wv[10] | (wv[11] << 16)) & m1};
            RAW_BAR();
#pragma unroll
            for (int t2 = 0; t2 < 2; ++t2) {
                const lds_cptr f = L + P1_STAGE + ((4 + 2 * th + t2) * 4 + cw) * 1536;
                unsigned ph[10];
                feature_map(wp0, wp1, *(const AS3(u32x4)*)(f + ka), *(const AS3(u32x2)*)(f + kb), ph);
                lds_ptr d = (lds_ptr)lds + (cw * 128 + (2 * th + t2) * 32 + r32) * 80 + hh * 40;
#pragma unroll
                for (int u = 0; u < 5; ++u) *(AS3(u32x2)*)(d + u * 8) = u32x2{ph[2 * u], ph[2 * u + 1]};
            }
            RAW_BAR();
#pragma unroll
            for (int k2 = 0; k2 < 2; ++k2) {
                const int u = wid + 8 * k2, uh = u / 3, un = u - 3 * uh;
                if (u < 12) {
                    f32x4 c0 = {0.f, 0.f, 0.f, 0.f}, c1 = {0.f, 0.f, 0.f, 0.f};
                    const unsigned vo = P1_STAGE + (32 + uh) * 1536;
                    const unsigned vb = 512 * (p4 >> 1) + 256 * (g >> 1) + 64 * ((2 * (g & 1)) ^ (p4 >> 1)) + 16 * q4 + 8 * (p4 & 1);
                    const unsigned vc = 1024 + 256 * (g >> 1) + 64 * ((2 * (g & 1)) ^ 2) + 16 * q4 + (p4 ? 8 : 0);
                    const unsigned pb = (uh * 128 + 8 * g + q4) * 80 + un * 32 + p4 * 8;
#pragma unroll
                    for (int ks = 0; ks < 4; ++ks) {
                        const bf16x8 a0 = cat8(tr_read(L + vo + vb + ks * 6144), tr_read(L + vo + (vb ^ 64) + ks * 6144));
                        const bf16x8 a1 = cat8(tr_read(L + vo + vc + ks * 6144), tr_read(L + vo + (vc ^ 64) + ks * 6144));
                        const bf16x8 b = cat8(tr_read(L + pb + ks * 2560), tr_read(L + pb + ks * 2560 + 320));
                        c0 = MFMA16(a0, b, c0); c1 = MFMA16(a1, b, c1);
                    }
                    const int ph = 16 * un + l15;
                    const unsigned off = WS_KVP + ((4 * hj + uh) * 16 + rt) * 3840 + ph * 96 + 16 * g;
                    if (ph < 40) { ST128(__builtin_bit_cast(u32x4, c0), off); if (g < 2) ST128(__builtin_bit_cast(u32x4, c1), off + 64); }
                }
            }
#pragma unroll
            for (int t2 = 0; t2 < 2; ++t2) {
                const lds_cptr f = L + P1_STAGE + ((2 * th + t2) * 4 + cw) * 1536;
                unsigned ph[10];
                feature_map(wp0, wp1, *(const AS3(u32x4)*)(f + ka), *(const AS3(u32x2)*)(f + kb), ph);
                const unsigned off = WS_PHQ + ((4 * hj + cw) * 64 + rt * 4 + 2 * th + t2) * 2560;
                ST128((u32x4{ph[0], ph[1], ph[2], ph[3]}), off + l16); ST128((u32x4{ph[4], ph[5], ph[6], ph[7]}), off + 1024 + l16);
                ST64((u32x2{ph[8], ph[9]}), off + 2048 + lane * 8);
            }
        }

        const int h = xcd * 4 + (bi >> 3), qt = bi & 7;
        const int qp = wid & 3, kh = wid >> 2;
        const int own = 2 * qp + kh;
        const char* kvbase = ws + WS_KV + (size_t)h * 64 * KVT;
        const int blk = (lane >> 4) & 1;
        if (wid == 0) wait_flags(ws, WS_FH + (h * 32 + l15) * 8, tag);
        RAW_BAR();
        u32x4 qa0, qb0; u32x2 qa1, qb1; float kmx;
        {
            const char* pq = ws + WS_QA + ((size_t)h * 64 + qt * 8 + 2 * qp) * QFT;
            asm volatile("global_load_dwordx4 %0, %1, %2 sc1" : "=v"(qa0) : "v"(l16), "s"(pq));
            asm volatile("global_load_dwordx2 %0, %1, %2 sc1" : "=v"(qa1) : "v"(kb), "s"(pq));
            asm volatile("global_load_dwordx4 %0, %1, %2 offset:1280 sc1" : "=v"(qb0) : "v"(l16), "s"(pq));
            asm volatile("global_load_dwordx2 %0, %1, %2 offset:1280 sc1" : "=v"(qb1) : "v"(kb), "s"(pq));
            const char* pm = ws + WS_KMAX + h * 256; const unsigned o4 = lane * 4;
            asm volatile("global_load_dword %0, %1, %2 sc1" : "=v"(kmx) : "v"(o4), "s"(pm) : "memory");
        }
        const int cst = (wid & 3) * 6 - (wid == 3 ? 1 : 0);
        const char* kvc = kvbase + cst * 1024; const unsigned ldc = lds0 + cst * 1024;
        const char* kvf = kvbase + (size_t)((wid >> 1) & 1) * 4 * SLOT + (wid & 1) * 6144; const unsigned ldf = lds0 + ((wid >> 1) & 1) * SLOT + (wid & 1) * 6144;
#define LOAD_HALF(hf) do { glds_run<4, (hf) * 11264, (hf) * 11264, true>(kvf, l16, ldf); \
        if (wid & 1) glds_run<1, (hf) * 11264 + 4096, (hf) * 11264 + 4096, true>(kvf, l16, ldf); else glds_run<2, (hf) * 11264 + 4096, (hf) * 11264 + 4096, true>(kvf, l16, ldf); } while (0)
#define LOAD_FIRST() do { if (kh == 0) LOAD_HALF(0); else LOAD_HALF(1); } while (0)
#define LOAD_CHUNK(c, slot) do { if (kh == 0) { glds_run<4, (c) * SLOT, (slot) * SLOT, true>(kvc, l16, ldc); \
        if (wid & 2) glds_run<1, (c) * SLOT + 4096, (slot) * SLOT + 4096, true>(kvc, l16, ldc); else glds_run<2, (c) * SLOT + 4096, (slot) * SLOT + 4096, true>(kvc, l16, ldc); } } while (0)
        LOAD_FIRST();
        const int vj = blk ? 2 : p4 >> 1;
        const unsigned va = 1280 + 512 * vj + 64 * ((2 * hh) ^ vj) + 16 * q4 + (blk ? (p4 ? 8 : 0) : 8 * (p4 & 1)), vb2 = va ^ 64;
        WAIT_VM(5);
        asm volatile("" : "+v"(qa0), "+v"(qa1), "+v"(qb0), "+v"(qb1), "+v"(kmx));
        if (hh) { qa1 = u32x2{0u, 0u}; qb1 = u32x2{0u, 0u}; }
        const bf16x8 QA0 = __builtin_bit_cast(bf16x8, qa0), QB0 = __builtin_bit_cast(bf16x8, qb0);
        const s16x4 QA1 = __builtin_bit_cast(s16x4, qa1), QB1 = __builtin_bit_cast(s16x4, qb1);
        float nba, nbb;
        {
            kmx = row_max16(kmx);
            auto r = __builtin_amdgcn_permlane16_swap(__float_as_uint(kmx), __float_as_uint(kmx), false, false);
            kmx = fmaxf(__uint_as_float(r[0]), __uint_as_float(r[1]));
            const float kn = -1.01f * __builtin_amdgcn_sqrtf(xhalf_sum(kmx));
            nba = kn * __builtin_amdgcn_sqrtf(qnorm2(qa0, qa1)); nbb = kn * __builtin_amdgcn_sqrtf(qnorm2(qb0, qb1));
            asm volatile("" : "+v"(nba), "+v"(nbb));
        }
        if (kh == 0) WAIT_VM(0);
        RAW_BAR();
        f32x4 ksum = {0.f, 0.f, 0.f, 0.f};
        const unsigned ocb = WS_OC + ((qt * 8 + own) * 32 + r32) * 16;
        float gate = 0.f;
        const char* pkv = ws + WS_KVP + (size_t)h * 16 * 3840; const unsigned kvo = (tid < 240 ? tid : 239) * 16, kvl = lds0 + wid * 8192;
#define KV_G1(j0, j) glds_run<1, ((j0) + (j)) * 3840, L_G + (j) * 1024, true>(pkv, kvo, kvl)
#define KV_GATHER(j0) do { KV_G1(j0, 0); KV_G1(j0, 1); KV_G1(j0, 2); KV_G1(j0, 3); KV_G1(j0, 4); KV_G1(j0, 5); KV_G1(j0, 6); KV_G1(j0, 7); } while (0)
        for (int attempt = 0;; ++attempt) {
            f32x16 oa, ob, nga, ngb, sA, sB;
#pragma unroll
            for (int r = 0; r < 16; ++r) { oa[r] = 0.f; ob[r] = 0.f; nga[r] = nba; ngb[r] = nbb; sB[r] = 0.f; }
            asm volatile("" : "+v"(nga), "+v"(ngb));
            lds_cptr pka = L + kh * SLOT + ka, pkb = L + kh * SLOT + kb, pv = L + kh * SLOT + va, pv2 = L + kh * SLOT + vb2;
            KFrag k0 = ld_k(pka, pkb, 0), k1 = ld_k(pka, pkb, KVT);
            VFrag v0 = ld_v(pv, 0), v1 = v0;
            sA = MFMA32(k0.k0, QA0, nga); sA = MFMA32K8(k0.k1, QA1, sA);
            for (int c = 0; c < 4; ++c) {
                const int snext = (2 * c + 2 + kh) % 5;
#define K2_PAIR(t, LOADK0, LOADV0, LOADK1) do { \
                bf16x8 vp = v1.v1; \
                v1 = ld_v(pv, ((t) + 1) * KVT); SBAR(); \
                attn_step(sA, sB, k0, vp, v0.v0, QB0, QB1, ngb, ob, oa); \
                LOADK0; SBAR(); \
                attn_step(sB, sA, k1, v0.v1, v0.v0, QA0, QA1, nga, oa, ob); \
                vp = v0.v1; \
                LOADV0; SBAR(); \
                attn_step(sA, sB, k1, vp, v1.v0, QB0, QB1, ngb, ob, oa); \
                LOADK1; SBAR(); \
                attn_step(sB, sA, k0, v1.v1, v1.v0, QA0, QA1, nga, oa, ob); } while (0)
                K2_PAIR(0, k0 = ld_k(pka, pkb, 2 * KVT), v0 = ld_v(pv, 2 * KVT), k1 = ld_k(pka, pkb, 3 * KVT));
                if (c == 0) {
                    WAIT_VM(0);
                    RAW_BAR();
                    if ((br ? tid < 4 : tid == 4) && attempt == 0) publish(ws, fhoff, tag);
                    LOAD_CHUNK(1, 2); LOAD_CHUNK(5, 3);
                } else if (c == 1) {
                    if (kh == 0 && attempt == 0) {
                        WAIT_VM(0);
#pragma unroll
                        for (int j = 0; j < 8; ++j) ksum += *(const AS3(f32x4)*)(L + L_G + wid * 8192 + j * 1024 + l16);
                        asm volatile("s_waitcnt lgkmcnt(0)" : "+v"(ksum) :: "memory");
                        KV_GATHER(8);
                    }
                    LOAD_CHUNK(3, 1);
                } else if (c == 2 && attempt == 0) {
                    WAIT_VM(0);
                    gate = *(const AS3(float)*)(L + L_GATE + (own * 32 + r32) * 4);
                    const lds_cptr a = L + L_MISC + r32 * 96 + hh * 48, f = L + L_G + own * 2560;
                    f32x16 lo = MFMA32(LDS_B128(a), LDS_B128(f + l16), f32x16{});
                    lo = MFMA32(LDS_B128(a + 16), LDS_B128(f + 1024 + l16), lo);
                    lo = MFMA32K8(LDS_B64(a + 32), LDS_B64(f + 2048 + lane * 8), lo);
                    auto rb = __builtin_amdgcn_permlane32_swap(__float_as_uint(lo[8]), __float_as_uint(lo[8]), false, false);
                    const float sb = (1.0f - gate) / (__uint_as_float(rb[1]) + 1e-6f);
                    ST128((u32x4{cvtpk(lo[0] * sb, lo[1] * sb), cvtpk(lo[2] * sb, lo[3] * sb), cvtpk(lo[4] * sb, lo[5] * sb), cvtpk(lo[6] * sb, lo[7] * sb)}),
                          ocb + (80 + 2 * h + hh) * (SEQ * 16));
                    if (!hh) ST64((u32x2{cvtpk(lo[8] * sb, lo[9] * sb), cvtpk(lo[10] * sb, lo[11] * sb)}), ocb + (80 + 64 + (h >> 1)) * (SEQ * 16) + (h & 1) * 8);
                }
                K2_PAIR(2, k0 = ld_k(pka, pkb, 4 * KVT), v0 = ld_v(pv, 4 * KVT), k1 = ld_k(pka, pkb, 5 * KVT));
                if (c == 0) LOAD_CHUNK(2, 4);
                else if (c == 1 && wid < 2 && attempt == 0) {
                    wait_flags(ws, WS_FT + ((tid + qt * 16) & 127) * 8, tag);
                    if (wid == 0) glds_run<1, 0, L_GATE, true>(ws + WS_G + qt * 1024, l16, lds0);
                }
                K2_PAIR(4, k0 = ld_k(pka, pkb, 6 * KVT), v0 = ld_v(pv, 6 * KVT), k1 = ld_k(pka, pkb, 7 * KVT));
                auto turn = [&]() {
                    if (c < 3) {
                        if (c == 0 && kh == 0 && attempt == 0) wait_flags(ws, WS_FH + (h * 32 + 16 + l15) * 8, tag);
                        WAIT_VM(0);
                        if (c == 1 && kh == 0 && attempt == 0) {
#pragma unroll
                            for (int j = 0; j < 8; ++j) ksum += *(const AS3(f32x4)*)(L + L_G + wid * 8192 + j * 1024 + l16);
                            if (tid < 240) {
                                const int ph = tid / 6, d = 4 * (tid - ph * 6), ph_h = ph / 20, u = ph - ph_h * 20;
                                const unsigned w0 = cvtpk(ksum[0], ksum[1]), w1 = cvtpk(ksum[2], ksum[3]);
                                lds_ptr dd = (lds_ptr)lds + L_MISC + d * 96 + ph_h * 48 + u * 2;
                                *(AS3(uint16_t)*)(dd) = (uint16_t)(w0 & 0xffffu); *(AS3(uint16_t)*)(dd + 96) = (uint16_t)(w0 >> 16);
                                *(AS3(uint16_t)*)(dd + 192) = (uint16_t)(w1 & 0xffffu); *(AS3(uint16_t)*)(dd + 288) = (uint16_t)(w1 >> 16);
                            }
                        }
                        if (c == 2 && tid == 0) *(AS3(unsigned)*)((lds_ptr)lds + L_FLAG) = 0u;
                        RAW_BAR();
                        if (c == 0) {
                            if (kh == 0 && attempt == 0) KV_GATHER(0);
                            LOAD_CHUNK(6, 0);
                        } else if (c == 1) {
                            if (attempt == 0) {
                                const char* sq = ws + WS_PHQ + ((size_t)h * 64 + qt * 8 + own) * 2560;
                                const unsigned dq = lds0 + own * 2560;
                                glds_run<2, 0, L_G, true>(sq, l16, dq);
                                if (lane < 32) glds_run<1, 2048, L_G + 2048, true>(sq, l16, dq);
                            }
                            LOAD_CHUNK(7, 2);
                        } else if (attempt == 0) {
                            if (tid == 0) publish(ws, WS_FOB + (qt * 32 + h) * 8, tag);
                            if (kh == 0) {
                                const char* pw = ws + WS_WOP + (size_t)bi * 51200 + wid * 12800; const unsigned lw2 = lds0 + wid * 4096;
                                glds_run<4, 0, 0, true>(pw, l16, lw2); glds_run<4, 4096, 0, true>(pw, l16, lw2); glds_run<4, 8192, 0, true>(pw, l16, lw2); glds_run<1, 11776, 0, true>(pw, l16, lw2);
                            }
                        }
                    }
                    pka = L + snext * SLOT + ka; pkb = L + snext * SLOT + kb; pv = L + snext * SLOT + va; pv2 = L + snext * SLOT + vb2;
                    k0 = ld_k(pka, pkb, 0);
                };
                K2_PAIR(6, turn(), v0 = ld_v(pv, 0), k1 = ld_k(pka, pkb, KVT));
            }
            ob = SMF(v1.v1, HI8(sB), ob);
            {
                lds_ptr d = (lds_ptr)lds + L_G + (own ^ 1) * 3072 + l16;
#pragma unroll
                for (int a = 0; a < 3; ++a)
                    *(AS3(f32x4)*)(d + a * 1024) = kh ? f32x4{oa[4 * a], oa[4 * a + 1], oa[4 * a + 2], oa[4 * a + 3]} : f32x4{ob[4 * a], ob[4 * a + 1], ob[4 * a + 2], ob[4 * a + 3]};
            }
            RAW_BAR();
            float o[12];
#pragma unroll
            for (int a = 0; a < 3; ++a) {
                const f32x4 t = *(const AS3(f32x4)*)(L + L_G + own * 3072 + l16 + a * 1024);
#pragma unroll
                for (int r = 0; r < 4; ++r) o[4 * a + r] = t[r] + (kh ? ob[4 * a + r] : oa[4 * a + r]);
            }
            auto rr = __builtin_amdgcn_permlane32_swap(__float_as_uint(o[8]), __float_as_uint(o[8]), false, false);
            const float lsum = __uint_as_float(rr[1]);
            const float sa = gate * __builtin_amdgcn_rcpf(lsum);
            ST128((u32x4{cvtpk(o[0] * sa, o[1] * sa), cvtpk(o[2] * sa, o[3] * sa), cvtpk(o[4] * sa, o[5] * sa), cvtpk(o[6] * sa, o[7] * sa)}), ocb + (2 * h + hh) * (SEQ * 16));
            if (!hh) ST64((u32x2{cvtpk(o[8] * sa, o[9] * sa), cvtpk(o[10] * sa, o[11] * sa)}), ocb + (64 + (h >> 1)) * (SEQ * 16) + (h & 1) * 8);
            if (attempt == 0 && __any(!(lsum >= 5.4210109e-20f)))
                *(AS3(unsigned)*)((lds_ptr)lds + L_FLAG) = 1u;
            WAIT_VM(0);
            RAW_BAR();
            const unsigned redo = *(const AS3(unsigned)*)(L + L_FLAG);
            if (__builtin_amdgcn_readfirstlane(redo) == 0u) break;
            float ma = -__builtin_inff(), mb = -__builtin_inff();
            for (int t = 0; t < 64; ++t) {
                const u32x4 g0 = __builtin_amdgcn_raw_buffer_load_b128(wsr, WS_KV + (h * 64 + t) * KVT + ka, 0, 16);
                const u32x2 g1 = __builtin_amdgcn_raw_buffer_load_b64(wsr, WS_KV + (h * 64 + t) * KVT + kb, 0, 16);
                f32x16 s = MFMA32(__builtin_bit_cast(bf16x8, g0), QA0, f32x16{}); s = MFMA32K8(__builtin_bit_cast(s16x4, g1), QA1, s);
                f32x16 u = MFMA32(__builtin_bit_cast(bf16x8, g0), QB0, f32x16{}); u = MFMA32K8(__builtin_bit_cast(s16x4, g1), QB1, u);
#pragma unroll
                for (int r = 0; r < 16; ++r) { ma = fmaxf(ma, s[r]); mb = fmaxf(mb, u[r]); }
            }
            nba = -xhalf_max(ma); nbb = -xhalf_max(mb);
            RAW_BAR();
            LOAD_FIRST();
            WAIT_VM(0);
            RAW_BAR();
        }
        if (tid == 0) publish(ws, WS_FOA + (qt * 32 + h) * 8, tag);
    }

    {
        const int tt = xcd * 4 + (bi & 3), ct = bi >> 2;
        const bool iso = wid < 4;
        const char* sb = iso ? ws + WS_OC + tt * 1024 : ws + WS_WOP + ct * 1280;
        unsigned voff[5];
#pragma unroll
        for (int j = 0; j < 5; ++j) {
            const int q = ((wid - 4) * 5 + j) * 64 + lane, c = __umul24(q, 52429) >> 22, n = q - c * 80;
            voff[j] = iso ? (wid * 4 + j) * (SEQ * 16) + l16 : c * (640 * 16) + n * 16;
        }
        const unsigned ld = lds0 + (iso ? wid * 4096 : 16384 + (wid - 4) * 5120);
#define P3_DMA(s) do { constexpr int c0_ = 16 * (((s) + 5) % 10), lo_ = ((s) & 3) * P3_STAGE; \
        if (iso) { glds_run<1, c0_ * SEQ * 16, lo_, true>(sb, voff[0], ld); glds_run<1, c0_ * SEQ * 16, lo_ + 1024, true>(sb, voff[1], ld); \
                   glds_run<1, c0_ * SEQ * 16, lo_ + 2048, true>(sb, voff[2], ld); glds_run<1, c0_ * SEQ * 16, lo_ + 3072, true>(sb, voff[3], ld); } \
        else { glds_run<1, c0_ * 10240, lo_, true>(sb, voff[0], ld); glds_run<1, c0_ * 10240, lo_ + 1024, true>(sb, voff[1], ld); glds_run<1, c0_ * 10240, lo_ + 2048, true>(sb, voff[2], ld); \
               glds_run<1, c0_ * 10240, lo_ + 3072, true>(sb, voff[3], ld); glds_run<1, c0_ * 10240, lo_ + 4096, true>(sb, voff[4], ld); } } while (0)
        if (iso) wait_flags(ws, WS_FOB + (xcd * 32 + r32) * 8, tag);
        P3_DMA(0); P3_DMA(1); P3_DMA(2);
        const unsigned oa = g * 1024 + (16 * (wid & 3) + l15) * 16;
        const unsigned wa = 16384 + g * 1280 + (20 * q4 + p4) * 16;
        f32x4 acc[5];
#pragma unroll
        for (int i = 0; i < 5; ++i) acc[i] = f32x4{0.f, 0.f, 0.f, 0.f};
#define P3_ITER(s) do { \
        if (iso) { if ((s) < 8) { WAIT_VM(8); } else if ((s) == 8) { WAIT_VM(4); } else { WAIT_VM(0); } } \
        else { if ((s) < 8) { WAIT_VM(10); } else if ((s) == 8) { WAIT_VM(5); } else { WAIT_VM(0); } } \
        RAW_BAR(); \
        if ((s) == 2 && iso) { wait_flags(ws, WS_FOA + (xcd * 32 + r32) * 8, tag); } \
        if ((s) < 7) P3_DMA((s) < 7 ? (s) + 3 : 0); \
        if (iso) { const lds_cptr buf = L + ((s) & 3) * P3_STAGE; \
            _Pragma("unroll") for (int ks = 0; ks < 4; ++ks) { const bf16x8 of = LDS_B128(buf + oa + ks * 4096); \
                _Pragma("unroll") for (int i = 0; i < 5; ++i) acc[i] = MFMA16(LDS_B128(buf + wa + ks * 5120 + i * 64), of, acc[i]); } } } while (0)
        P3_ITER(0); P3_ITER(1); P3_ITER(2);
        P3_ITER(3); P3_ITER(4); P3_ITER(5); P3_ITER(6); P3_ITER(7); P3_ITER(8); P3_ITER(9);
        if (iso) {
            char* dst = (char*)out + (size_t)(tt * 64 + 16 * wid + l15) * 1280 + ct * 160 + g * 40;
#pragma unroll
            for (int i = 0; i < 5; ++i) *(u32x2*)(dst + i * 8) = u32x2{cvtpk(acc[i][0], acc[i][1]), cvtpk(acc[i][2], acc[i][3])};
        }
    }
    if (bid == 0 && tid == 0) publish(ws, WS_EPOCH, tag);
}

extern "C" void kernel_launch(void* const* d_in, const int* in_sizes, int n_in, void* d_out, int out_size, void* d_ws, size_t ws_size,
                              hipStream_t stream) {
    fused_kernel<<<256, 512, 0, stream>>>((const uint16_t*)d_in[0], (const uint16_t*)d_in[1], (const uint16_t*)d_in[4], (const uint16_t*)d_in[2],
                                          (const uint16_t*)d_in[6], (const uint16_t*)d_in[5], (const uint16_t*)d_in[7], (char*)d_ws, (uint16_t*)d_out);
}
```

```cpp
#include <hip/hip_runtime.h>
#include <hip/hip_bf16.h>
#include <stdint.h>

typedef __attribute__((ext_vector_type(8))) __bf16 bf16x8;
typedef __attribute__((ext_vector_type(16))) __bf16 bf16x16;
typedef __attribute__((ext_vector_type(4))) short s16x4;
typedef __attribute__((ext_vector_type(8))) short s16x8;
typedef __attribute__((ext_vector_type(4))) float f32x4;
typedef __attribute__((ext_vector_type(16))) float f32x16;
typedef __attribute__((ext_vector_type(4))) unsigned u32x4;
typedef __attribute__((ext_vector_type(2))) unsigned u32x2;
typedef __attribute__((ext_vector_type(2))) __bf16 bf16x2;
typedef __attribute__((ext_vector_type(2))) float f32x2;
typedef __attribute__((address_space(3))) const char* lds_cptr;
typedef __attribute__((address_space(3))) char* lds_ptr;
typedef unsigned long long u64;
#define AS3(T) __attribute__((address_space(3))) T

#define WARMUP 1
#define HB_SPLIT 1
constexpr int SEQ = 2048, NH = 32;
constexpr unsigned OOB = 0x7ffffff0u;
constexpr float LOG2E = 1.4426950408889634f;
constexpr float QSCALE = 0.22360679774997896f * 1.4426950408889634f;

constexpr int KVT = 3072;
constexpr int QFT = 1280;
constexpr unsigned WS_KV = 0;
constexpr unsigned WS_QA = WS_KV + NH * 64 * KVT;
constexpr unsigned WS_PHQ = WS_QA + NH * 64 * QFT;
constexpr unsigned WS_WOP = WS_PHQ + NH * 64 * 2560;
constexpr unsigned WS_OC = WS_WOP + 160 * 640 * 16;
constexpr unsigned WS_KMAX = WS_OC + 160 * SEQ * 16;
constexpr unsigned WS_G = WS_KMAX + NH * 64 * 4;
constexpr unsigned WS_KVP = WS_G + SEQ * 4;
constexpr unsigned WS_FT = WS_KVP + NH * 16 * 3840;
constexpr unsigned WS_FH = WS_FT + 2048;
constexpr unsigned WS_FOB = WS_FH + 16384;
constexpr unsigned WS_FOA = WS_FOB + 2048;
constexpr unsigned WS_EPOCH = WS_FOA + 2048;
constexpr unsigned WS_END = WS_EPOCH + 128;

__device__ __forceinline__ unsigned cvtpk(float lo, float hi) {
    return __builtin_bit_cast(unsigned, __builtin_convertvector(f32x2{lo, hi}, bf16x2)); }
__device__ __forceinline__ float bflo(unsigned w) { return __uint_as_float(w << 16); }
__device__ __forceinline__ float bfhi(unsigned w) { return __uint_as_float(w & 0xffff0000u); }
#define GL1(P) "global_load_lds_dwordx4 %1, %2" P "\n\t"
#define GL2(P) GL1(P) "global_load_lds_dwordx4 %1, %2 offset:1024" P "\n\t"
#define GL3(P) GL2(P) "global_load_lds_dwordx4 %1, %2 offset:2048" P "\n\t"
#define GL4(P) GL3(P) "global_load_lds_dwordx4 %1, %2 offset:3072" P "\n\t"
#define GLASM(BODY) asm volatile("s_mov_b32 %0, m0\n\ts_add_u32 m0, %3, %4\n\ts_nop 0\n\t" BODY "s_mov_b32 m0, %0" \
                     : "=&s"(sv) : "v"(voff), "s"(t), "s"(lbase), "i"(LOFF) : "memory", "scc")
template <int N, int GOFF, int LOFF, bool SC1>
__device__ __forceinline__ void glds_run(const void* sbase, unsigned voff, unsigned lbase) {
    static_assert(N >= 1 && N <= 4, "");
    unsigned sv, tl, th;
    const unsigned lo = (unsigned)(uintptr_t)sbase, hi = (unsigned)((uintptr_t)sbase >> 32);
    asm volatile("s_add_u32 %0, %2, %4\n\ts_addc_u32 %1, %3, 0" : "=&s"(tl), "=s"(th) : "s"(lo), "s"(hi), "i"(GOFF) : "scc");
    const uint64_t t = ((uint64_t)th << 32) | tl;
    if constexpr (SC1) {
        if constexpr (N == 1) GLASM(GL1(" sc1")); else if constexpr (N == 2) GLASM(GL2(" sc1")); else if constexpr (N == 3) GLASM(GL3(" sc1")); else GLASM(GL4(" sc1"));
    } else {
        if constexpr (N == 1) GLASM(GL1("")); else if constexpr (N == 2) GLASM(GL2("")); else if constexpr (N == 3) GLASM(GL3("")); else GLASM(GL4(""));
    }
}
template <int LOFF>
__device__ __forceinline__ void glds_ptr(const void* p, unsigned lbase) {
    unsigned sv;
    asm volatile("s_mov_b32 %0, m0\n\ts_add_u32 m0, %2, %3\n\ts_nop 0\n\tglobal_load_lds_dwordx4 %1, off\n\ts_mov_b32 m0, %0"
                 : "=&s"(sv) : "v"(p), "s"(lbase), "i"(LOFF) : "memory", "scc"); }
__device__ __forceinline__ void touch(const void* p, unsigned lds_scratch) {
    unsigned sv;
    asm volatile("s_mov_b32 %0, m0\n\ts_mov_b32 m0, %2\n\ts_nop 0\n\tglobal_load_lds_dword %1, off sc1\n\ts_mov_b32 m0, %0" : "=&s"(sv) : "v"(p), "s"(lds_scratch) : "memory"); }
__device__ __forceinline__ s16x4 tr_read(lds_cptr p) { return __builtin_amdgcn_ds_read_tr16_b64_v4i16((AS3(s16x4)*)p); }
__device__ __forceinline__ bf16x8 cat8(s16x4 a, s16x4 b) {
    s16x8 r = {a[0], a[1], a[2], a[3], b[0], b[1], b[2], b[3]}; return __builtin_bit_cast(bf16x8, r); }
#define LDS_B128(p) (*(const AS3(bf16x8)*)(p))
#define LDS_B64(p) (*(const AS3(s16x4)*)(p))
#define MFMA16(a, b, c) __builtin_amdgcn_mfma_f32_16x16x32_bf16(a, b, c, 0, 0, 0)
#define MFMA32(a, b, c) __builtin_amdgcn_mfma_f32_32x32x16_bf16(a, b, c, 0, 0, 0)
#define MFMA32K8(a, b, c) __builtin_amdgcn_mfma_f32_32x32x8bf16_1k(a, b, c, 0, 0, 0)
#define WAIT_VM_(n) asm volatile("s_waitcnt vmcnt(" #n ")" ::: "memory")
#define WAIT_VM(n) WAIT_VM_(n)
#define RAW_BAR() asm volatile("s_waitcnt lgkmcnt(0)\n\ts_barrier" ::: "memory")
#define SBAR() __builtin_amdgcn_sched_barrier(0)
#define EXP2(v) __builtin_amdgcn_exp2f(v)
#define SMF(a, lo, acc) __builtin_amdgcn_smfmac_f32_32x32x32_bf16(a, __builtin_bit_cast(bf16x16, lo), acc, 0xDDDD, 0, 0)
#define LO8(s) __builtin_shufflevector(s, s, 0, 1, 2, 3, 4, 5, 6, 7)
#define HI8(s) __builtin_shufflevector(s, s, 8, 9, 10, 11, 12, 13, 14, 15)
#define ST128(v, off) __builtin_amdgcn_raw_buffer_store_b128(v, wsr, off, 0, 16)
#define ST64(v, off) __builtin_amdgcn_raw_buffer_store_b64(v, wsr, off, 0, 16)
#define ST32(v, off) __builtin_amdgcn_raw_buffer_store_b32(v, wsr, off, 0, 16)
__device__ __forceinline__ float xhalf_max(float v) {
    auto r = __builtin_amdgcn_permlane32_swap(__float_as_uint(v), __float_as_uint(v), false, false);
    return fmaxf(__uint_as_float(r[0]), __uint_as_float(r[1])); }
__device__ __forceinline__ float xhalf_min(float v) {
    auto r = __builtin_amdgcn_permlane32_swap(__float_as_uint(v), __float_as_uint(v), false, false);
    return fminf(__uint_as_float(r[0]), __uint_as_float(r[1])); }
__device__ __forceinline__ float xhalf_sum(float v) {
    auto r = __builtin_amdgcn_permlane32_swap(__float_as_uint(v), __float_as_uint(v), false, false);
    return __uint_as_float(r[0]) + __uint_as_float(r[1]); }
__device__ __forceinline__ float row_max16(float v) {
    v = fmaxf(v, __int_as_float(__builtin_amdgcn_update_dpp(0, __float_as_int(v), 0x128, 0xf, 0xf, false)));
    v = fmaxf(v, __int_as_float(__builtin_amdgcn_update_dpp(0, __float_as_int(v), 0x124, 0xf, 0xf, false)));
    v = fmaxf(v, __int_as_float(__builtin_amdgcn_update_dpp(0, __float_as_int(v), 0x122, 0xf, 0xf, false)));
    v = fmaxf(v, __int_as_float(__builtin_amdgcn_update_dpp(0, __float_as_int(v), 0x121, 0xf, 0xf, false)));
    return v; }
__device__ __forceinline__ float row_sum16(float v) {
    v += __int_as_float(__builtin_amdgcn_update_dpp(0, __float_as_int(v), 0x128, 0xf, 0xf, false));
    v += __int_as_float(__builtin_amdgcn_update_dpp(0, __float_as_int(v), 0x124, 0xf, 0xf, false));
    v += __int_as_float(__builtin_amdgcn_update_dpp(0, __float_as_int(v), 0x122, 0xf, 0xf, false));
    v += __int_as_float(__builtin_amdgcn_update_dpp(0, __float_as_int(v), 0x121, 0xf, 0xf, false));
    return v; }
__device__ __forceinline__ void publish(char* ws, unsigned off, u64 tag) {
    __hip_atomic_store((u64*)(ws + off), tag, __ATOMIC_RELAXED, __HIP_MEMORY_SCOPE_AGENT); }
__device__ __forceinline__ void wait_flags(const char* ws, unsigned off, u64 tag) {
    for (int spin = 0; spin < (1 << 20); ++spin) {
        const u64 f = __hip_atomic_load((const u64*)(ws + off), __ATOMIC_RELAXED, __HIP_MEMORY_SCOPE_AGENT);
        if (__all(f == tag)) break;
        __builtin_amdgcn_s_sleep(2);
    }
    asm volatile("" ::: "memory");
}

struct KFrag { bf16x8 k0; s16x4 k1; };
struct VFrag { bf16x8 v0, v1; };
__device__ __forceinline__ KFrag ld_k(lds_cptr pa, lds_cptr pb, int off) { KFrag k; k.k0 = LDS_B128(pa + off); k.k1 = LDS_B64(pb + off); return k; }
__device__ __forceinline__ VFrag ld_v2(lds_cptr p, lds_cptr q, int off) {
    VFrag v; v.v0 = cat8(tr_read(p + off), tr_read(q + off)); v.v1 = cat8(tr_read(p + off + 256), tr_read(q + off + 256)); return v; }
#define ld_v(p, off) ld_v2(p, p##2, off)
__device__ __forceinline__ void attn_step(f32x16& sc, f32x16& sn, const KFrag& kn, const bf16x8& vprev1, const bf16x8& vc0,
                                          const bf16x8& qn0, const s16x4& qn1, f32x16& oprev, f32x16& oc) {
    oprev = SMF(vprev1, HI8(sn), oprev); SBAR();
#pragma unroll
    for (int r = 0; r < 4; ++r) sc[r] = EXP2(sc[r]);
    SBAR(); sn = MFMA32(kn.k0, qn0, f32x16{}); SBAR();
#pragma unroll
    for (int r = 4; r < 8; ++r) sc[r] = EXP2(sc[r]);
    SBAR(); sn = MFMA32K8(kn.k1, qn1, sn); SBAR();
#pragma unroll
    for (int r = 8; r < 12; ++r) sc[r] = EXP2(sc[r]);
    SBAR(); oc = SMF(vc0, LO8(sc), oc); SBAR();
#pragma unroll
    for (int r = 12; r < 16; ++r) sc[r] = EXP2(sc[r]);
    SBAR();
}
__device__ __forceinline__ void feature_map(const u32x4 wp0, const u32x2 wp1, const u32x4 f0, const u32x2 f1, unsigned (&ph)[10]) {
    f32x16 pq = MFMA32(__builtin_bit_cast(bf16x8, wp0), __builtin_bit_cast(bf16x8, f0), f32x16{});
    pq = MFMA32K8(__builtin_bit_cast(s16x4, wp1), __builtin_bit_cast(s16x4, f1), pq);
    float mx = fmaxf(pq[0], pq[1]), mn = fminf(pq[0], pq[1]);
#pragma unroll
    for (int u = 2; u < 10; u += 2) { mx = fmaxf(fmaxf(mx, pq[u]), pq[u + 1]); mn = fminf(fminf(mn, pq[u]), pq[u + 1]); }
    mx = xhalf_max(mx); mn = xhalf_min(mn);
    const f32x2 lp = {LOG2E, LOG2E}, cmx = {-mx * LOG2E, -mx * LOG2E}, cmn = {mn * LOG2E, mn * LOG2E};
    f32x2 ep[5], en[5], sp2 = {0.f, 0.f}, sn2 = {0.f, 0.f};
#pragma unroll
    for (int u = 0; u < 5; ++u) {
        const f32x2 p = {pq[2 * u], pq[2 * u + 1]};
        const f32x2 a = __builtin_elementwise_fma(p, lp, cmx), b = __builtin_elementwise_fma(p, -lp, cmn);
        ep[u] = f32x2{EXP2(a[0]), EXP2(a[1])}; en[u] = f32x2{EXP2(b[0]), EXP2(b[1])};
        sp2 += ep[u]; sn2 += en[u];
    }
    const float sp = __builtin_amdgcn_rcpf(xhalf_sum(sp2[0] + sp2[1])), sn = __builtin_amdgcn_rcpf(xhalf_sum(sn2[0] + sn2[1]));
#pragma unroll
    for (int u = 0; u < 5; ++u) {
        const f32x2 a = ep[u] * sp, b = en[u] * sn;
        ph[u] = cvtpk(a[0], a[1]); ph[5 + u] = cvtpk(b[0], b[1]);
    }
}
__device__ __forceinline__ u32x2 split3(float v) {
    const unsigned hi = cvtpk(v, 0.f) & 0xffffu; const float r1 = v - bflo(hi);
    const unsigned mid = cvtpk(r1, 0.f) & 0xffffu; const float r2 = r1 - bflo(mid);
    return u32x2{hi | (mid << 16), cvtpk(r2, 0.f) & 0xffffu};
}
__device__ __forceinline__ float qnorm2(const u32x4& a, const u32x2& b) {
    float n2 = 0.f;
#pragma unroll
    for (int j = 0; j < 4; ++j) { n2 = fmaf(bflo(a[j]), bflo(a[j]), n2); n2 = fmaf(bfhi(a[j]), bfhi(a[j]), n2); }
#pragma unroll
    for (int j = 0; j < 2; ++j) { n2 = fmaf(bflo(b[j]), bflo(b[j]), n2); n2 = fmaf(bfhi(b[j]), bfhi(b[j]), n2); }
    return xhalf_sum(n2);
}

#define LD128(off) __builtin_amdgcn_raw_buffer_load_b128(wsr, off, 0, 16)
#define LD64(off) __builtin_amdgcn_raw_buffer_load_b64(wsr, off, 0, 16)
#define LD32(off) __builtin_amdgcn_raw_buffer_load_b32(wsr, off, 0, 16)
#define BAR() asm volatile("s_barrier" ::: "memory")

__device__ __forceinline__ const char* w_source(int p, int lane, const char* g0, const char* g1, const char* g2) {
    int q = (p & 7) * 64 + lane; const int up = q >= 248; q -= up ? 248 : 0; q = q < 239 ? q : 239;
    const int row = __umul24(q, 2185) >> 16, ch = q - row * 30, sgm = __umul24(ch, 6554) >> 16;
    const uintptr_t b = sgm == 0 ? (uintptr_t)g0 : sgm == 1 ? (uintptr_t)g1 : (uintptr_t)g2;
    return (const char*)(b + (__umul24(16 * (p >> 3) + 8 * up + row, 3840) + (ch - 10 * sgm) * 16));
}

constexpr int P1_STAGE = 49152;
constexpr int L_WPH = 3 * P1_STAGE;
constexpr int L_GW = L_WPH + 3200;
constexpr int L_WO = L_GW + 1280;
constexpr int L_SCR = L_WO + 10240;
constexpr int L_FLAG = L_SCR + 256;
constexpr int LDS_BYTES = L_FLAG + 16;
constexpr int L_IMG = P1_STAGE;
constexpr int L_PH = L_IMG + 32 * 1536;
constexpr int L_Q = L_PH + 40960 + 256;
constexpr int SLOT = 8 * KVT;
constexpr int L_X = 0;
constexpr int L_MISC = L_WO;
constexpr int L_GATE = L_MISC + 3072;
constexpr int L_RA = 4 * SLOT, L_RB = SLOT, L_RC = 2 * SLOT, L_RD = 0, L_RE = L_RC + 6 * 5120;
constexpr int L_CX = L_RC;
static_assert(L_Q + 10240 + 256 <= L_WO && L_RA + 10 * 5120 <= L_WO && L_RE + 3 * 5120 <= L_RA && LDS_BYTES <= 163840, "");
__device__ __forceinline__ constexpr int p3_off(int br, int ks) {
    return br ? (ks < 10 ? L_RA + ks * 5120 : ks < 14 ? L_RB + (ks - 10) * 5120 : L_RC + (ks - 14) * 5120)
              : (ks < 4 ? L_RD + ks * 5120 : ks < 7 ? L_RE + (ks - 4) * 5120 : ks < 17 ? L_RA + (ks - 7) * 5120 : L_RB + (ks - 17) * 5120); }

__global__ __launch_bounds__(768) void fused_kernel(const uint16_t* __restrict__ x, const uint16_t* __restrict__ wA, const uint16_t* __restrict__ wB,
                                                    const uint16_t* __restrict__ woA, const uint16_t* __restrict__ woB,
                                                    const uint16_t* __restrict__ wphi, const uint16_t* __restrict__ gatew,
                                                    char* __restrict__ ws, uint16_t* __restrict__ out) {
    __shared__ __attribute__((aligned(16))) char lds[LDS_BYTES];
    const int bid = blockIdx.x, xcd = bid & 7, bi = bid >> 3;
    const int tid = threadIdx.x, lane = tid & 63;
    const int wid = __builtin_amdgcn_readfirstlane(tid >> 6);
    const unsigned lds0 = (unsigned)(uintptr_t)lds;
    const lds_cptr L = (lds_cptr)lds;
    const unsigned l16 = lane * 16;
    const int g = lane >> 4, l15 = lane & 15, q4 = l15 >> 2, p4 = lane & 3, r32 = lane & 31, hh = lane >> 5;
    const auto wsr = __builtin_amdgcn_make_buffer_rsrc(ws, 0, WS_END, 0x00020000);
    const int rt = (xcd & 1) * 8 + (bi & 7), kd = bi >> 4, hj = (xcd >> 1) * 2 + ((bi >> 3) & 1);
    const int h = xcd * 4 + (bi >> 3), qt = bi & 7;
    const int tt = xcd * 4 + (bi & 3), ct = bi >> 2;
    const char* xb = (const char*)x + (size_t)rt * 128 * 1280;
    const unsigned ka = lane * 16, kb = 1024 + r32 * 8;
    const char* wg0 = (const char*)wA + hj * 160 + (kd ? 2560 : 0);
    const char* wg1 = kd ? (const char*)wB + hj * 160 + 1280 : (const char*)wA + hj * 160 + 1280;
    const char* wg2 = (const char*)wB + hj * 160 + (kd ? 2560 : 0);
#define wsrc(p) w_source(p, lane, wg0, wg1, wg2)
#define P3_WKS(br, ks, dst) do { _Pragma("unroll") for (int j_ = 0; j_ < 5; ++j_) { const int q_ = j_ * 64 + lane, c_ = __umul24(q_, 52429) >> 22, n_ = q_ - c_ * 80; \
        glds_run<1, 0, 0, true>(ws + WS_WOP + ct * 1280 + ((br) * 80 + 4 * (ks)) * 10240, c_ * 10240 + n_ * 16, lds0 + (dst) + j_ * 1024); } } while (0)
    u64 tag;
    {
        const u64 e = __hip_atomic_load((const u64*)(ws + WS_EPOCH), __ATOMIC_RELAXED, __HIP_MEMORY_SCOPE_AGENT);
        tag = e;
    }
#define MAKE_TAG() do { u64 z = ((u64)(unsigned)__builtin_amdgcn_readfirstlane((unsigned)(tag >> 32)) << 32) | (unsigned)__builtin_amdgcn_readfirstlane((unsigned)tag); \
        z = z * 0x9E3779B97F4A7C15ull + 0x632BE59BD9B4E019ull; z ^= z >> 29; z *= 0xBF58476D1CE4E5B9ull; z ^= z >> 32; tag = z; } while (0)

    if (wid >= 8) {
        const int hw = wid - 8;
        __builtin_amdgcn_s_setprio(2);
        unsigned hbt = 0;
        if (tid == 512) *(AS3(unsigned)*)((lds_ptr)lds + L_FLAG + 4) = 0u;
#define HBAR() do { hbt += 4; asm volatile("s_waitcnt lgkmcnt(0)" ::: "memory"); \
        if (lane == 0) __hip_atomic_fetch_add((AS3(unsigned)*)((lds_ptr)lds + L_FLAG + 4), 1u, __ATOMIC_RELAXED, __HIP_MEMORY_SCOPE_WORKGROUP); \
        while (__builtin_amdgcn_readfirstlane(__hip_atomic_load((AS3(unsigned)*)((lds_ptr)lds + L_FLAG + 4), __ATOMIC_RELAXED, __HIP_MEMORY_SCOPE_WORKGROUP)) < hbt) __builtin_amdgcn_s_sleep(1); \
        asm volatile("" ::: "memory"); } while (0)
        const char* pw[4];
#pragma unroll
        for (int j = 0; j < 4; ++j) pw[j] = wsrc(16 + 4 * hw + j);
        auto wstage = [&](int slot) {
            const unsigned lw = lds0 + slot * P1_STAGE + 32768 + hw * 4096;
            glds_ptr<0>(pw[0], lw); glds_ptr<1024>(pw[1], lw); glds_ptr<2048>(pw[2], lw); glds_ptr<3072>(pw[3], lw);
#pragma unroll
            for (int j = 0; j < 4; ++j) pw[j] += 245760;
        };
        {
            const unsigned o = hw * 1024 + l16;
            if (o < 3200) glds_run<1, 0, L_WPH, false>((const char*)wphi + hj * 3200, o, lds0 + hw * 1024);
            if (hw < 2 && o < 1280) glds_run<1, 0, L_GW, false>(gatew, o, lds0 + hw * 1024);
        }
        wstage(0);
        RAW_BAR();
        wstage(1); wstage(2);
        {
            const int cb = bid < 160 ? bid : 0, ch = cb < 80 ? cb : cb - 80;
            const int r0 = (ch < 64 ? (ch >> 1) * 20 + (ch & 1) * 4 : (ch - 64) * 40 + 16) + hw, dr = (ch < 64 ? 8 : 20) * 1280;
            const char* pr = (const char*)(cb < 80 ? woA : woB) + r0 * 1280; const unsigned lr = lds0 + hw * 1280;
            glds_run<1, 0, L_WO, false>(pr, l16, lr); glds_run<1, 0, L_WO + 5120, false>(pr + dr, l16, lr);
            if (lane < 16) { glds_run<1, 1024, L_WO + 1024, false>(pr, l16, lr); glds_run<1, 1024, L_WO + 6144, false>(pr + dr, l16, lr); }
        }
        const char* tp = hw == 0 ? xb + ((bi >> 3) * 32 + r32) * 1280 + 3 * 128
                       : hw == 1 ? ((lane % 6) < 2 ? wg0 : (lane % 6) < 4 ? wg1 : wg2) + ((bi & 7) * 8 + (lane < 48 ? lane / 6 : 0) + 192) * 3840 + (lane & 1) * 156 : xb;
        const unsigned tstep = hw == 0 ? 128u : hw == 1 ? 245760u : 0u;
        touch(tp, lds0 + L_SCR); tp += tstep; touch(tp, lds0 + L_SCR); tp += tstep;
        WAIT_VM(14);
        BAR();
        for (int s = 0; s < 9; ++s) {
            if (s == 0) { WAIT_VM(10); } else if (s == 1) { WAIT_VM(11); } else if (s < 6) { WAIT_VM(6); } else if (s == 6) { WAIT_VM(5); } else if (s == 7) { WAIT_VM(4); } else { WAIT_VM(0); }
            BAR();
            if (s < 7) wstage(s % 3);
            if (s < 5) { touch(tp, lds0 + L_SCR); tp += tstep; }
            if (s == 3 && bid < 160) {
                const lds_cptr r = L + L_WO + q4 * 1280 + g * 32 + p4 * 8 + hw * 128;
#pragma unroll
                for (int j = 0; j < 3; ++j)
                    if (j < 2 || hw < 2) ST128(__builtin_bit_cast(u32x4, cat8(tr_read(r + j * 512), tr_read(r + j * 512 + 5120))), WS_WOP + (bid * 640 + (j * 4 + hw) * 64 + lane) * 16);
            }
        }
        MAKE_TAG();
        BAR();
        BAR();
        const unsigned fhoff = WS_FH + (((4 * hj + (lane & 3)) * 4 + kd) * 16 + rt) * 8;
        if (hw == 0 && lane < 5) publish(ws, lane < 4 ? fhoff : WS_FT + bid * 8, tag);
        if (kd) BAR();
        const char* kvbase = ws + WS_KV + (size_t)h * 64 * KVT;
        const char* kvw = kvbase + (size_t)(hw >> 1) * 4 * SLOT + (hw & 1) * 12288; const unsigned ldw = lds0 + (hw >> 1) * SLOT + (hw & 1) * 12288;
#define H_CHUNK(j) do { glds_run<4, (j) * SLOT, ((j) & 1) * 2 * SLOT, true>(kvw, l16, ldw); glds_run<4, (j) * SLOT + 4096, ((j) & 1) * 2 * SLOT + 4096, true>(kvw, l16, ldw); \
                        glds_run<4, (j) * SLOT + 8192, ((j) & 1) * 2 * SLOT + 8192, true>(kvw, l16, ldw); } while (0)
        if (hw == 0) wait_flags(ws, WS_FH + (h * 64 + r32) * 8, tag);
        BAR();
        {
            const char* pq = ws + WS_QA + ((size_t)h * 64 + qt * 8) * QFT;
            if (hw < 2) glds_run<3, 0, L_Q, true>(pq + hw * 3072, l16, lds0 + hw * 3072); else glds_run<2, 0, L_Q, true>(pq + 2048 + hw * 2048, l16, lds0 + 2048 + hw * 2048);
            if (hw == 3 && lane < 16) glds_run<1, 0, L_Q + 10240, true>(ws + WS_KMAX + h * 256, l16, lds0);
        }
        WAIT_VM(0);
        BAR();
        {
            if (kd) { if (hw == 0 && lane < 4) publish(ws, fhoff + 256, tag); }
            else {
#pragma unroll
                for (int j = 0; j < 10; ++j) ST128(*(const AS3(u32x4)*)(L + L_PH + hw * 10240 + j * 1024 + l16), WS_PHQ + ((4 * hj + hw) * 64 + rt * 4) * 2560 + j * 1024 + l16);
                WAIT_VM(0);
                if (lane == hw) publish(ws, fhoff + 256, tag);
            }
            H_CHUNK(1);
            wait_flags(ws, WS_FT + (64 * hw + lane) * 8, tag);
            WAIT_VM(0);
            BAR();
            {
                wait_flags(ws, WS_FH + (lane < 32 ? h * 64 + 32 + lane : 2 * qt + (lane & 1)) * 8, tag);
                H_CHUNK(2);
                const int e = 64 * hw + lane;
                f32x4 ks = {0.f, 0.f, 0.f, 0.f};
#pragma unroll
                for (int j = 0; j < 16; ++j) ks += __builtin_bit_cast(f32x4, LD128(WS_KVP + (h * 16 + j) * 3840 + (e < 240 ? e : 0) * 16));
                u32x4 pf[2][2]; u32x2 pg[2]; float gt[2];
#pragma unroll
                for (int t = 0; t < 2; ++t) {
                    const unsigned o = WS_PHQ + (h * 64 + qt * 8 + 2 * hw + t) * 2560;
                    pf[t][0] = LD128(o + l16); pf[t][1] = LD128(o + 1024 + l16); pg[t] = LD64(o + 2048 + lane * 8);
                    gt[t] = __uint_as_float(LD32(WS_G + (qt * 256 + (2 * hw + t) * 32 + r32) * 4));
                }
                if (e < 240) {
                    const int ph = e / 6, d = 4 * (e - ph * 6), ph_h = ph / 20, u = ph - ph_h * 20;
                    const unsigned w0 = cvtpk(ks[0], ks[1]), w1 = cvtpk(ks[2], ks[3]);
                    lds_ptr dd = (lds_ptr)lds + L_MISC + d * 96 + ph_h * 48 + u * 2;
                    *(AS3(uint16_t)*)(dd) = (uint16_t)(w0 & 0xffffu); *(AS3(uint16_t)*)(dd + 96) = (uint16_t)(w0 >> 16);
                    *(AS3(uint16_t)*)(dd + 192) = (uint16_t)(w1 & 0xffffu); *(AS3(uint16_t)*)(dd + 288) = (uint16_t)(w1 >> 16);
                }
                *(AS3(float)*)((lds_ptr)lds + L_GATE + (64 * hw + lane) * 4) = hh ? gt[1] : gt[0];
                WAIT_VM(0);
                RAW_BAR();
                H_CHUNK(3);
                const lds_cptr a = L + L_MISC + r32 * 96 + hh * 48;
                const bf16x8 a0 = LDS_B128(a), a1 = LDS_B128(a + 16); const s16x4 a2 = LDS_B64(a + 32);
#pragma unroll
                for (int t = 0; t < 2; ++t) {
                    f32x16 lo = MFMA32(a0, __builtin_bit_cast(bf16x8, pf[t][0]), f32x16{});
                    lo = MFMA32(a1, __builtin_bit_cast(bf16x8, pf[t][1]), lo);
                    lo = MFMA32K8(a2, __builtin_bit_cast(s16x4, pg[t]), lo);
                    auto rb = __builtin_amdgcn_permlane32_swap(__float_as_uint(lo[8]), __float_as_uint(lo[8]), false, false);
                    const float sb = (1.0f - gt[t]) / (__uint_as_float(rb[1]) + 1e-6f);
                    const unsigned oc = WS_OC + ((qt * 8 + 2 * hw + t) * 32 + r32) * 16;
                    ST128((u32x4{cvtpk(lo[0] * sb, lo[1] * sb), cvtpk(lo[2] * sb, lo[3] * sb), cvtpk(lo[4] * sb, lo[5] * sb), cvtpk(lo[6] * sb, lo[7] * sb)}), oc + (80 + 2 * h + hh) * (SEQ * 16));
                    if (!hh) ST64((u32x2{cvtpk(lo[8] * sb, lo[9] * sb), cvtpk(lo[10] * sb, lo[11] * sb)}), oc + (80 + 64 + (h >> 1)) * (SEQ * 16) + (h & 1) * 8);
                }
            }
            WAIT_VM(0);
            HBAR();
            if (tid == 512) { publish(ws, WS_FOB + (qt * 32 + h) * 8, tag); *(AS3(unsigned)*)((lds_ptr)lds + L_FLAG) = 0u; }
            for (int ks = hw; ks < 10; ks += 4) P3_WKS(1, ks, L_RA + ks * 5120);
            RAW_BAR();
            P3_WKS(1, 10 + hw, L_RB + hw * 5120);
            { const int ln = hw * 64 + lane; touch(ws + WS_WOP + bi * 25600 + (ln < 200 ? ln : 0) * 128, lds0 + L_SCR); }
            if (hw == 0) wait_flags(ws, WS_FOB + (xcd * 32 + r32) * 8, tag);
            WAIT_VM(0);
            RAW_BAR();
            BAR();
            const unsigned redo = *(const AS3(unsigned)*)(L + L_FLAG);
            if (__builtin_amdgcn_readfirstlane(redo) != 0u) {
                RAW_BAR();
                H_CHUNK(0); WAIT_VM(0); BAR();
                H_CHUNK(1); WAIT_VM(0); BAR();
                H_CHUNK(2); WAIT_VM(0); BAR();
                H_CHUNK(3);
                if (tid == 512) *(AS3(unsigned)*)((lds_ptr)lds + L_FLAG) = 0u;
                WAIT_VM(0); RAW_BAR();
                P3_WKS(1, 10 + hw, L_RB + hw * 5120); WAIT_VM(0);
                BAR(); BAR();
            }
        }
        if (tid == 512) publish(ws, WS_FOA + (qt * 32 + h) * 8, tag);
        for (int ks = 14 + hw; ks < 20; ks += 4) P3_WKS(1, ks, L_RC + (ks - 14) * 5120);
        WAIT_VM(0);
        BAR();
        for (int ks = hw; ks < 7; ks += 4) P3_WKS(0, ks, ks < 4 ? L_RD + ks * 5120 : L_RE + (ks - 4) * 5120);
        BAR();
        if (hw == 3) wait_flags(ws, WS_FOA + (xcd * 32 + r32) * 8, tag);
        WAIT_VM(0);
        BAR();
        BAR();
        BAR();
    } else {
        const int th = wid >> 2, cw = wid & 3;
        {
            unsigned vx[2];
#pragma unroll
            for (int j = 0; j < 2; ++j) { const int row = 8 * (wid * 2 + j) + (lane >> 3); vx[j] = row * 1280 + (((lane ^ row) & 7) << 4); }
            const unsigned lx = lds0 + wid * 2048;
            const char* pw0 = wsrc(2 * wid); const char* pw1 = wsrc(2 * wid + 1);
#define P1_X(s, slot) do { glds_run<1, 0, 0, false>(xb + (s) * 128, vx[0], lx + (slot) * P1_STAGE); glds_run<1, 0, 1024, false>(xb + (s) * 128, vx[1], lx + (slot) * P1_STAGE); \
                           glds_ptr<16384>(pw0, lx + (slot) * P1_STAGE); glds_ptr<17408>(pw1, lx + (slot) * P1_STAGE); pw0 += 245760; pw1 += 245760; } while (0)
            P1_X(0, 0);
            BAR();
            P1_X(1, 1); P1_X(2, 2);
            const bool t3 = cw < 3 || (hj == 0 && kd == 0);
            const int xrow = 64 * th + l15;
            lds_cptr xa0 = L + xrow * 128 + ((g ^ (xrow & 7)) << 4), xa1 = L + ((xrow * 128 + ((g ^ (xrow & 7)) << 4)) ^ 64);
            const unsigned wa0 = 16384 + 8192 * (g >> 1) + 480 * (8 * (g & 1) + q4) + 128 * (g & 1) + 40 * p4 + (cw < 3 ? 160 * cw : 32);
            const unsigned wst = cw < 3 ? 8 : 160;
            lds_cptr wa[4];
#pragma unroll
            for (int i = 0; i < 3; ++i) wa[i] = L + wa0 + i * wst;
            wa[3] = cw < 3 ? L + wa0 + 24 : L + L_GW + g * 16;
            f32x4 acc[4][4];
#pragma unroll
            for (int m = 0; m < 4; ++m)
#pragma unroll
                for (int i = 0; i < 4; ++i) acc[m][i] = f32x4{0.f, 0.f, 0.f, 0.f};
            bf16x8 xA[4], wA_[4], xB[4], wB_[4];
#define P1_LOAD(X, W, ks, NT, G3) do { \
        _Pragma("unroll") for (int i = 0; i < 3; ++i) W[i] = cat8(tr_read(wa[i] + (ks) * 16384), tr_read(wa[i] + (ks) * 16384 + 1920)); \
        if (NT == 4) { if (G3) W[3] = LDS_B128(wa[3] + (ks) * 64); else W[3] = cat8(tr_read(wa[3] + (ks) * 16384), tr_read(wa[3] + (ks) * 16384 + 1920)); } \
        _Pragma("unroll") for (int m = 0; m < 4; ++m) X[m] = LDS_B128(((ks) ? xa1 : xa0) + m * 2048); } while (0)
#define P1_MMA(X, W, i, NT) do { if ((i) < NT) { _Pragma("unroll") for (int m = 0; m < 4; ++m) acc[m][i] = MFMA16(W[i], X[m], acc[m][i]); } } while (0)
#define P1_HALF(X, W, XN, WN, ks, NT, G3, MID1, MID2) do { \
        _Pragma("unroll") for (int i = 0; i < NT; ++i) { \
            s16x4 lo_, hi_; \
            acc[0][i] = MFMA16(W[i], X[0], acc[0][i]); SBAR(); \
            if (i == 3 && G3) WN[3] = LDS_B128(wa[3] + (ks) * 64); else lo_ = tr_read(wa[i] + (ks) * 16384); SBAR(); \
            acc[1][i] = MFMA16(W[i], X[1], acc[1][i]); SBAR(); \
            if (!(i == 3 && G3)) { hi_ = tr_read(wa[i] + (ks) * 16384 + 1920); WN[i] = cat8(lo_, hi_); } SBAR(); \
            acc[2][i] = MFMA16(W[i], X[2], acc[2][i]); SBAR(); \
            XN[i] = LDS_B128(((ks) ? xa1 : xa0) + i * 2048); SBAR(); \
            acc[3][i] = MFMA16(W[i], X[3], acc[3][i]); SBAR(); \
            if (NT == 3 && i == 2) XN[3] = LDS_B128(((ks) ? xa1 : xa0) + 3 * 2048); \
            if (i == 0) { MID1; } if (i == 1) { MID2; } SBAR(); \
        } } while (0)
#define P1_DX() do { unsigned sv_; const char* xs_ = xb + (s + 3) * 128; \
                  asm volatile("s_cmp_gt_u32 %1, 6\n\ts_cbranch_scc1 1f\n\ts_mov_b32 %0, m0\n\ts_mov_b32 m0, %5\n\ts_nop 0\n\tglobal_load_lds_dwordx4 %2, %4\n\t" \
                               "s_add_u32 m0, %5, 1024\n\ts_nop 0\n\tglobal_load_lds_dwordx4 %3, %4\n\ts_mov_b32 m0, %0\n1:" \
                               : "=&s"(sv_) : "s"(s), "v"(vx[0]), "v"(vx[1]), "s"(xs_), "s"(lx + slot * P1_STAGE) : "memory", "scc"); } while (0)
#define P1_DW() do { unsigned sv_; \
                  asm volatile("s_cmp_gt_u32 %1, 6\n\ts_cbranch_scc1 1f\n\ts_mov_b32 %0, m0\n\ts_add_u32 m0, %4, 16384\n\ts_nop 0\n\tglobal_load_lds_dwordx4 %2, off\n\t" \
                               "s_add_u32 m0, %4, 17408\n\ts_nop 0\n\tglobal_load_lds_dwordx4 %3, off\n\ts_mov_b32 m0, %0\n1:" \
                               : "=&s"(sv_) : "s"(s), "v"(pw0), "v"(pw1), "s"(lx + slot * P1_STAGE) : "memory", "scc"); pw0 += 245760; pw1 += 245760; } while (0)
#define P1_LOOP(NT, G3) do { \
            P1_LOAD(xA, wA_, 0, NT, G3); \
            int slot = 0; \
            _Pragma("nounroll") for (int s = 0; s < 10; ++s) { \
                P1_HALF(xA, wA_, xB, wB_, 1, NT, G3, (void)0, (void)0); \
                  \
                asm volatile("s_cmp_gt_u32 %0, 7\n\ts_cbranch_scc1 1f\n\ts_waitcnt vmcnt(4) lgkmcnt(0)\n\ts_barrier\n\ts_branch 2f\n" \
                             "1:\n\ts_cmp_eq_u32 %0, 9\n\ts_cbranch_scc1 2f\n\ts_waitcnt vmcnt(0) lgkmcnt(0)\n\ts_barrier\n2:" :: "s"(s) : "memory", "scc"); \
                const int d = slot == 2 ? -2 * P1_STAGE : P1_STAGE; \
                xa0 += d; xa1 += d; wa[0] += d; wa[1] += d; wa[2] += d; wa[3] += G3 ? 128 : d; SBAR(); \
                P1_HALF(xB, wB_, xA, wA_, 0, NT, G3, P1_DX(), P1_DW()); \
                slot = slot == 2 ? 0 : slot + 1; \
            } } while (0)
            WAIT_VM(8);
            BAR();
            if (cw < 3) P1_LOOP(4, false); else if (t3) P1_LOOP(4, true); else P1_LOOP(3, false);
            const lds_ptr img = (lds_ptr)lds + L_IMG;
            {
                const int ht = (4 * hj + g) * 64 + rt * 4 + 2 * th;
                float kn = 0.f;
                if (cw < 3) {
                    const bool isv = kd ? cw != 1 : false, toA = kd ? cw == 0 : cw < 2;
                    if (kd == 0 && cw == 0) {
#pragma unroll
                        for (int m = 0; m < 4; ++m)
#pragma unroll
                            for (int i = 0; i < 4; ++i) acc[m][i] *= QSCALE;
                    }
                    const unsigned ts = (kd == 0 && cw == 0) ? QFT : KVT, tb = ((kd == 0 && cw == 0) ? WS_QA : WS_KV + (kd ? 1536 : 0)) + ht * ts;
                    const unsigned o0 = l15 * 16, o1 = 512 + (isv ? l15 ^ 4 : l15) * 16;
#pragma unroll
                    for (int m = 0; m < 4; ++m) {
                        const u32x4 w0 = {cvtpk(acc[m][0][0], acc[m][0][1]), cvtpk(acc[m][0][2], acc[m][0][3]), cvtpk(acc[m][1][0], acc[m][1][1]), cvtpk(acc[m][1][2], acc[m][1][3])};
                        const u32x4 w1 = {cvtpk(acc[m][2][0], acc[m][2][1]), cvtpk(acc[m][2][2], acc[m][2][3]), cvtpk(acc[m][3][0], acc[m][3][1]), cvtpk(acc[m][3][2], acc[m][3][3])};
                        if (toA) { const unsigned mo = tb + (m >> 1) * ts + (m & 1) * 256; ST128(w0, mo + o0); ST128(w1, mo + o1); }
                        else { const lds_ptr d = img + (((isv ? 4 : 0) + 2 * th + (m >> 1)) * 4 + g) * 1536 + (m & 1) * 256; *(AS3(u32x4)*)(d + o0) = w0; *(AS3(u32x4)*)(d + o1) = w1; }
                    }
                    if (kd == 0 && cw == 1) {
#pragma unroll
                        for (int m = 0; m < 4; ++m) {
                            float n2 = 0.f;
#pragma unroll
                            for (int i = 0; i < 4; ++i)
#pragma unroll
                                for (int r = 0; r < 4; ++r) n2 = fmaf(acc[m][i][r], acc[m][i][r], n2);
                            kn = (n2 == n2) ? fmaxf(kn, n2) : __builtin_inff();
                        }
                    }
                } else {
#pragma unroll
                    for (int m = 0; m < 4; ++m) {
                        const unsigned ok = WS_KV + (ht + (m >> 1)) * KVT + 1024, o8 = (m & 1) * 128 + l15 * 8, o16 = (m & 1) * 256 + (l15 ^ 8) * 16;
                        const lds_ptr d = img + ((2 * th + (m >> 1)) * 4 + g) * 1536 + 1024;
                        const u32x2 w1 = {cvtpk(acc[m][1][0], acc[m][1][1]), cvtpk(acc[m][1][2], acc[m][1][3])}, w2 = {cvtpk(acc[m][2][0], acc[m][2][1]), cvtpk(acc[m][2][2], acc[m][2][3])};
                        if (kd == 0) {
                            ST64((u32x2{cvtpk(acc[m][0][0] * QSCALE, acc[m][0][1] * QSCALE), cvtpk(acc[m][0][2] * QSCALE, acc[m][0][3] * QSCALE)}), WS_QA + (ht + (m >> 1)) * QFT + 1024 + o8);
                            ST128((u32x4{w1[0], w1[1], 0x3f803f80u, 0x00003f80u}), ok + (m & 1) * 256 + l15 * 16);
                            *(AS3(u32x2)*)(d + o8) = w2;
                            float n2 = 0.f;
#pragma unroll
                            for (int r = 0; r < 4; ++r) n2 = fmaf(acc[m][1][r], acc[m][1][r], n2);
                            kn = (n2 == n2) ? fmaxf(kn, n2) : __builtin_inff();
                        } else {
                            ST128((u32x4{cvtpk(acc[m][0][0], acc[m][0][1]), cvtpk(acc[m][0][2], acc[m][0][3]), 0x00003f80u, 0u}), ok + 1536 + o16);
                            *(AS3(u32x2)*)(d + o8) = w1;
                            *(AS3(u32x4)*)(d + 16 * 1536 + o16) = u32x4{w2[0], w2[1], 0x00003f80u, 0u};
                        }
                    }
                }
                if (cw == 3 && t3 && g == 0) {
#pragma unroll
                    for (int m = 0; m < 4; ++m) ST32(__float_as_uint(1.0f / (1.0f + __expf(-acc[m][3][0]))), WS_G + (rt * 128 + 64 * th + 16 * m + l15) * 4);
                }
                if (kd == 0 && (cw & 1)) {
                    kn = row_max16(kn);
                    if (l15 == 0) ST32(__float_as_uint(kn), WS_KMAX + ((4 * hj + g) * 64 + (cw == 3 ? 32 : 0) + rt * 2 + th) * 4);
                }
            }
            u32x4 wp0; u32x2 wp1;
            {
                const int fr = (r32 & 3) + 4 * (r32 >> 3);
                const lds_cptr wp = L + L_WPH + cw * 800 + (fr < 10 ? 10 * ((r32 >> 2) & 1) + fr : 0) * 2;
                unsigned wv[12];
#pragma unroll
                for (int j = 0; j < 8; ++j) wv[j] = *(const AS3(uint16_t)*)(wp + hh * 320 + j * 40);
#pragma unroll
                for (int j = 0; j < 4; ++j) wv[8 + j] = *(const AS3(uint16_t)*)(wp + 640 + j * 40);
                const unsigned m0 = fr < 10 ? 0xffffffffu : 0u, m1 = (fr < 10 && hh == 0) ? 0xffffffffu : 0u;
                wp0 = u32x4{(wv[0] | (wv[1] << 16)) & m0, (wv[2] | (wv[3] << 16)) & m0, (wv[4] | (wv[5] << 16)) & m0, (wv[6] | (wv[7] << 16)) & m0};
                wp1 = u32x2{(wv[8] | (wv[9] << 16)) & m1, (wv[10] | (wv[11] << 16)) & m1};
            }
            RAW_BAR();
#pragma unroll
            for (int t2 = 0; t2 < 2; ++t2) {
                const lds_cptr f = L + L_IMG + ((2 * th + t2) * 4 + cw) * 1536;
                unsigned ph[10];
                feature_map(wp0, wp1, *(const AS3(u32x4)*)(f + ka), *(const AS3(u32x2)*)(f + kb), ph);
                const lds_ptr d = (lds_ptr)lds + L_PH + (cw * 4 + 2 * th + t2) * 2560;
                if (kd) {
#pragma unroll
                    for (int u = 0; u < 5; ++u) *(AS3(u32x2)*)(d + r32 * 80 + hh * 40 + u * 8) = u32x2{ph[2 * u], ph[2 * u + 1]};
                } else {
                    *(AS3(u32x4)*)(d + l16) = u32x4{ph[0], ph[1], ph[2], ph[3]}; *(AS3(u32x4)*)(d + 1024 + l16) = u32x4{ph[4], ph[5], ph[6], ph[7]};
                    *(AS3(u32x2)*)(d + 2048 + lane * 8) = u32x2{ph[8], ph[9]};
                }
                if (t2 == 0) {
                    SBAR();
                    WAIT_VM(0);
                    BAR();
                    SBAR();
                }
            }
            if (kd) {
                RAW_BAR();
#pragma unroll
                for (int k2 = 0; k2 < 2; ++k2) {
                    if (k2 == 0 || wid < 4) {
                        const int u = wid + 8 * k2, uh = u / 3, un = u - 3 * uh;
                        f32x4 c0 = {0.f, 0.f, 0.f, 0.f}, c1 = {0.f, 0.f, 0.f, 0.f};
                        const unsigned vo = L_IMG + (16 + uh) * 1536;
                        const unsigned vb = 512 * (p4 >> 1) + 256 * (g >> 1) + 64 * ((2 * (g & 1)) ^ (p4 >> 1)) + 16 * q4 + 8 * (p4 & 1);
                        const unsigned vc = 1024 + 256 * (g >> 1) + 64 * ((2 * (g & 1)) ^ 2) + 16 * q4 + (p4 ? 8 : 0);
                        const unsigned pb = L_PH + (uh * 128 + 8 * g + q4) * 80 + un * 32 + p4 * 8;
#pragma unroll
                        for (int ks = 0; ks < 4; ++ks) {
                            const bf16x8 a0 = cat8(tr_read(L + vo + vb + ks * 6144), tr_read(L + vo + (vb ^ 64) + ks * 6144));
                            const bf16x8 a1 = cat8(tr_read(L + vo + vc + ks * 6144), tr_read(L + vo + (vc ^ 64) + ks * 6144));
                            const bf16x8 b = cat8(tr_read(L + pb + ks * 2560), tr_read(L + pb + ks * 2560 + 320));
                            c0 = MFMA16(a0, b, c0); c1 = MFMA16(a1, b, c1);
                        }
                        const int ph = 16 * un + l15;
                        const unsigned off = ph < 40 ? WS_KVP + ((4 * hj + uh) * 16 + rt) * 3840 + ph * 96 + 16 * g : OOB;
                        ST128(__builtin_bit_cast(u32x4, c0), off); ST128(__builtin_bit_cast(u32x4, c1), g < 2 ? off + 64 : OOB);
                    }
                }
            }
        }
        const int qp = wid & 3, kh = wid >> 2;
        const int own = 2 * qp + kh;
        const int blk = (lane >> 4) & 1, vj = blk ? 2 : p4 >> 1;
        const unsigned kb2 = 1024 + r32 * 16 + hh * 8;
        const unsigned va = 1536 + 512 * vj + 64 * ((2 * hh) ^ vj) + 16 * q4 + (blk ? (p4 ? 8 : 0) : 8 * (p4 & 1)), vb2 = va ^ 64;
        RAW_BAR();
        {
            const char* src = ws + WS_KV + ((size_t)h * 64 + kh * 32) * KVT + qp * 6144; const unsigned dst = lds0 + kh * SLOT + qp * 6144;
            glds_run<4, 0, 0, true>(src, l16, dst); glds_run<2, 4096, 4096, true>(src, l16, dst);
        }
        WAIT_VM(0);
        BAR();
        const lds_cptr lq = L + L_Q + 2 * qp * QFT;
        const u32x4 qa0 = *(const AS3(u32x4)*)(lq + ka), qb0 = *(const AS3(u32x4)*)(lq + QFT + ka);
        u32x2 qa1 = *(const AS3(u32x2)*)(lq + kb), qb1 = *(const AS3(u32x2)*)(lq + QFT + kb);
        float kmx = *(const AS3(float)*)(L + L_Q + 10240 + lane * 4);
        if (hh) { qa1 = u32x2{0u, 0u}; qb1 = u32x2{0u, 0u}; }
        const bf16x8 QA0 = __builtin_bit_cast(bf16x8, qa0), QB0 = __builtin_bit_cast(bf16x8, qb0);
        float nba, nbb;
        {
            kmx = row_max16(kmx);
            auto r = __builtin_amdgcn_permlane16_swap(__float_as_uint(kmx), __float_as_uint(kmx), false, false);
            kmx = fmaxf(__uint_as_float(r[0]), __uint_as_float(r[1]));
            const float kn = -1.01f * __builtin_amdgcn_sqrtf(xhalf_sum(kmx));
            nba = kn * __builtin_amdgcn_sqrtf(qnorm2(qa0, qa1)); nbb = kn * __builtin_amdgcn_sqrtf(qnorm2(qb0, qb1));
        }
        for (int attempt = 0;; ++attempt) {
            f32x16 oa, ob, sA, sB;
#pragma unroll
            for (int r = 0; r < 16; ++r) { oa[r] = 0.f; ob[r] = 0.f; sB[r] = 0.f; }
            const s16x4 QA1 = __builtin_bit_cast(s16x4, hh ? split3(nba) : qa1), QB1 = __builtin_bit_cast(s16x4, hh ? split3(nbb) : qb1);
            lds_cptr pka = L + kh * SLOT + ka, pkb = L + kh * SLOT + kb2, pv = L + kh * SLOT + va, pv2 = L + kh * SLOT + vb2;
            KFrag k0 = ld_k(pka, pkb, 0), k1 = ld_k(pka, pkb, KVT);
            VFrag v0 = ld_v(pv, 0), v1 = v0;
            sA = MFMA32(k0.k0, QA0, f32x16{}); sA = MFMA32K8(k0.k1, QA1, sA);
            for (int c = 0; c < 4; ++c) {
                const int dnext = (c & 1) ? -2 * SLOT : 2 * SLOT;
#define K2_PAIR(t, LOADK0, LOADV0, LOADK1) do { \
                bf16x8 vp = v1.v1; \
                v1 = ld_v(pv, ((t) + 1) * KVT); SBAR(); \
                attn_step(sA, sB, k0, vp, v0.v0, QB0, QB1, ob, oa); \
                LOADK0; SBAR(); \
                attn_step(sB, sA, k1, v0.v1, v0.v0, QA0, QA1, oa, ob); \
                vp = v0.v1; \
                LOADV0; SBAR(); \
                attn_step(sA, sB, k1, vp, v1.v0, QB0, QB1, ob, oa); \
                LOADK1; SBAR(); \
                attn_step(sB, sA, k0, v1.v1, v1.v0, QA0, QA1, oa, ob); } while (0)
                K2_PAIR(0, k0 = ld_k(pka, pkb, 2 * KVT), v0 = ld_v(pv, 2 * KVT), k1 = ld_k(pka, pkb, 3 * KVT));
                K2_PAIR(2, k0 = ld_k(pka, pkb, 4 * KVT), v0 = ld_v(pv, 4 * KVT), k1 = ld_k(pka, pkb, 5 * KVT));
                K2_PAIR(4, k0 = ld_k(pka, pkb, 6 * KVT), v0 = ld_v(pv, 6 * KVT), k1 = ld_k(pka, pkb, 7 * KVT));
#define TURN() do { asm volatile("s_cmp_eq_u32 %0, 3\n\ts_cbranch_scc1 1f\n\ts_waitcnt vmcnt(0) lgkmcnt(0)\n\ts_barrier\n1:" :: "s"(c) : "memory", "scc"); \
                    pka += dnext; pkb += dnext; pv += dnext; pv2 += dnext; k0 = ld_k(pka, pkb, 0); } while (0)
                K2_PAIR(6, TURN(), v0 = ld_v(pv, 0), k1 = ld_k(pka, pkb, KVT));
            }
            ob = SMF(v1.v1, HI8(sB), ob);
            {
                lds_ptr d = (lds_ptr)lds + L_X + (own ^ 1) * 3072 + l16;
#pragma unroll
                for (int a = 0; a < 3; ++a)
                    *(AS3(f32x4)*)(d + a * 1024) = kh ? f32x4{oa[4 * a], oa[4 * a + 1], oa[4 * a + 2], oa[4 * a + 3]} : f32x4{ob[4 * a], ob[4 * a + 1], ob[4 * a + 2], ob[4 * a + 3]};
            }
            RAW_BAR();
            float o[12];
#pragma unroll
            for (int a = 0; a < 3; ++a) {
                const f32x4 t = *(const AS3(f32x4)*)(L + L_X + own * 3072 + l16 + a * 1024);
#pragma unroll
                for (int r = 0; r < 4; ++r) o[4 * a + r] = t[r] + (kh ? ob[4 * a + r] : oa[4 * a + r]);
            }
            auto rr = __builtin_amdgcn_permlane32_swap(__float_as_uint(o[8]), __float_as_uint(o[8]), false, false);
            const float lsum = __uint_as_float(rr[1]);
            const float sa = *(const AS3(float)*)(L + L_GATE + (own * 32 + r32) * 4) * __builtin_amdgcn_rcpf(lsum);
            const unsigned ocb = WS_OC + ((qt * 8 + own) * 32 + r32) * 16;
            ST128((u32x4{cvtpk(o[0] * sa, o[1] * sa), cvtpk(o[2] * sa, o[3] * sa), cvtpk(o[4] * sa, o[5] * sa), cvtpk(o[6] * sa, o[7] * sa)}), ocb + (2 * h + hh) * (SEQ * 16));
            if (!hh) ST64((u32x2{cvtpk(o[8] * sa, o[9] * sa), cvtpk(o[10] * sa, o[11] * sa)}), ocb + (64 + (h >> 1)) * (SEQ * 16) + (h & 1) * 8);
            if (attempt == 0 && __any(!(lsum >= 5.4210109e-20f)))
                *(AS3(unsigned)*)((lds_ptr)lds + L_FLAG) = 1u;
            WAIT_VM(0);
            RAW_BAR();
            const unsigned redo = *(const AS3(unsigned)*)(L + L_FLAG);
            if (__builtin_amdgcn_readfirstlane(redo) == 0u) break;
            float ma = -__builtin_inff(), mb = -__builtin_inff();
            for (int t = 0; t < 64; ++t) {
                const u32x4 g0 = LD128(WS_KV + (h * 64 + t) * KVT + ka);
                const u32x2 g1 = LD64(WS_KV + (h * 64 + t) * KVT + kb2);
                f32x16 s = MFMA32(__builtin_bit_cast(bf16x8, g0), QA0, f32x16{}); s = MFMA32K8(__builtin_bit_cast(s16x4, g1), __builtin_bit_cast(s16x4, qa1), s);
                f32x16 u = MFMA32(__builtin_bit_cast(bf16x8, g0), QB0, f32x16{}); u = MFMA32K8(__builtin_bit_cast(s16x4, g1), __builtin_bit_cast(s16x4, qb1), u);
#pragma unroll
                for (int r = 0; r < 16; ++r) { ma = fmaxf(ma, s[r]); mb = fmaxf(mb, u[r]); }
            }
            nba = -xhalf_max(ma); nbb = -xhalf_max(mb);
            RAW_BAR();
            BAR();
        }
        {
            const int mt = wid & 3, kp = wid >> 2;
            const unsigned ofo = WS_OC + ((4 * kp + g) * SEQ + tt * 64 + 16 * mt + l15) * 16;
            const lds_cptr wa = L + g * 1280 + (20 * q4 + p4) * 16;
            f32x4 acc[5];
#pragma unroll
            for (int i = 0; i < 5; ++i) acc[i] = f32x4{0.f, 0.f, 0.f, 0.f};
            u32x4 of[10];
#define P3_MMA(br) do { if (kp) { _Pragma("unroll") for (int k = 0; k < 10; ++k) _Pragma("unroll") for (int i = 0; i < 5; ++i) \
                                      acc[i] = MFMA16(LDS_B128(wa + p3_off(br, 2 * k + 1) + i * 64), __builtin_bit_cast(bf16x8, of[k]), acc[i]); } \
                        else { _Pragma("unroll") for (int k = 0; k < 10; ++k) _Pragma("unroll") for (int i = 0; i < 5; ++i) \
                                      acc[i] = MFMA16(LDS_B128(wa + p3_off(br, 2 * k) + i * 64), __builtin_bit_cast(bf16x8, of[k]), acc[i]); } } while (0)
#pragma unroll
            for (int k = 0; k < 10; ++k) of[k] = LD128(ofo + (80 + 8 * k) * (SEQ * 16));
            BAR();
            P3_MMA(1);
            RAW_BAR();
            for (int ks = 7 + wid; ks < 20; ks += 8) P3_WKS(0, ks, ks < 17 ? L_RA + (ks - 7) * 5120 : L_RB + (ks - 17) * 5120);
            BAR();
#pragma unroll
            for (int k = 0; k < 10; ++k) of[k] = LD128(ofo + (8 * k) * (SEQ * 16));
            WAIT_VM(10);
            BAR();
            P3_MMA(0);
            if (kp) {
#pragma unroll
                for (int i = 0; i < 5; ++i) *(AS3(f32x4)*)((lds_ptr)lds + L_CX + (mt * 5 + i) * 1024 + l16) = acc[i];
            }
            RAW_BAR();
            if (!kp) {
                char* dst = (char*)out + (size_t)(tt * 64 + 16 * mt + l15) * 1280 + ct * 160 + g * 40;
#pragma unroll
                for (int i = 0; i < 5; ++i) {
                    const f32x4 v = acc[i] + *(const AS3(f32x4)*)(L + L_CX + (mt * 5 + i) * 1024 + l16);
                    *(u32x2*)(dst + i * 8) = u32x2{cvtpk(v[0], v[1]), cvtpk(v[2], v[3])};
                }
            }
        }
    }
    if (bid == 0 && tid == 512) publish(ws, WS_EPOCH, tag);
}

extern "C" void kernel_launch(void* const* d_in, const int* in_sizes, int n_in, void* d_out, int out_size, void* d_ws, size_t ws_size,
                              hipStream_t stream) {
    fused_kernel<<<256, 768, 0, stream>>>((const uint16_t*)d_in[0], (const uint16_t*)d_in[1], (const uint16_t*)d_in[4], (const uint16_t*)d_in[2],
                                          (const uint16_t*)d_in[6], (const uint16_t*)d_in[5], (const uint16_t*)d_in[7], (char*)d_ws, (uint16_t*)d_out);
}
```

```cpp
#include <hip/hip_runtime.h>
#include <hip/hip_bf16.h>
#include <stdint.h>

typedef __attribute__((ext_vector_type(8))) __bf16 bf16x8;
typedef __attribute__((ext_vector_type(16))) __bf16 bf16x16;
typedef __attribute__((ext_vector_type(4))) short s16x4;
typedef __attribute__((ext_vector_type(8))) short s16x8;
typedef __attribute__((ext_vector_type(4))) float f32x4;
typedef __attribute__((ext_vector_type(16))) float f32x16;
typedef __attribute__((ext_vector_type(4))) unsigned u32x4;
typedef __attribute__((ext_vector_type(2))) unsigned u32x2;
typedef __attribute__((ext_vector_type(2))) __bf16 bf16x2;
typedef __attribute__((ext_vector_type(2))) float f32x2;
typedef __attribute__((address_space(3))) const char* lds_cptr;
typedef __attribute__((address_space(3))) char* lds_ptr;
typedef unsigned long long u64;
#define AS3(T) __attribute__((address_space(3))) T

#define WARMUP 1
#define HB_SPLIT 1
constexpr unsigned CODE_BYTES = 30000;
#define PRIO_A (void)0
#define PRIO_B PRIO(0)
#define PRIO_C PRIO(1)
constexpr int SEQ = 2048, NH = 32;
constexpr unsigned OOB = 0x7ffffff0u;
constexpr float LOG2E = 1.4426950408889634f;
constexpr float QSCALE = 0.22360679774997896f * 1.4426950408889634f;

constexpr int KVT = 3072;
constexpr int QFT = 1280;
constexpr unsigned WS_KV = 0;
constexpr unsigned WS_QA = WS_KV + NH * 64 * KVT;
constexpr unsigned WS_PHQ = WS_QA + NH * 64 * QFT;
constexpr unsigned WS_WOP = WS_PHQ + NH * 64 * 2560;
constexpr unsigned WS_OC = WS_WOP + 160 * 640 * 16;
constexpr unsigned WS_KMAX = WS_OC + 160 * SEQ * 16;
constexpr unsigned WS_G = WS_KMAX + NH * 64 * 4;
constexpr unsigned WS_KVP = WS_G + SEQ * 4;
constexpr unsigned WS_FT = WS_KVP + NH * 16 * 3840;
constexpr unsigned WS_FH = WS_FT + 2048;
constexpr unsigned WS_FOB = WS_FH + 16384;
constexpr unsigned WS_FOA = WS_FOB + 2048;
constexpr unsigned WS_EPOCH = WS_FOA + 2048;
constexpr unsigned WS_END = WS_EPOCH + 128;

__device__ __forceinline__ unsigned cvtpk(float lo, float hi) {
    return __builtin_bit_cast(unsigned, __builtin_convertvector(f32x2{lo, hi}, bf16x2)); }
__device__ __forceinline__ float bflo(unsigned w) { return __uint_as_float(w << 16); }
__device__ __forceinline__ float bfhi(unsigned w) { return __uint_as_float(w & 0xffff0000u); }
#define GL1(P) "global_load_lds_dwordx4 %1, %2" P "\n\t"
#define GL2(P) GL1(P) "global_load_lds_dwordx4 %1, %2 offset:1024" P "\n\t"
#define GL3(P) GL2(P) "global_load_lds_dwordx4 %1, %2 offset:2048" P "\n\t"
#define GL4(P) GL3(P) "global_load_lds_dwordx4 %1, %2 offset:3072" P "\n\t"
#define GLASM(BODY) asm volatile("s_mov_b32 %0, m0\n\ts_add_u32 m0, %3, %4\n\ts_nop 0\n\t" BODY "s_mov_b32 m0, %0" \
                     : "=&s"(sv) : "v"(voff), "s"(t), "s"(lbase), "i"(LOFF) : "memory", "scc")
template <int N, int GOFF, int LOFF, bool SC1>
__device__ __forceinline__ void glds_run(const void* sbase, unsigned voff, unsigned lbase) {
    static_assert(N >= 1 && N <= 4, "");
    unsigned sv, tl, th;
    lbase = __builtin_amdgcn_readfirstlane(lbase);
    const unsigned lo = __builtin_amdgcn_readfirstlane((unsigned)(uintptr_t)sbase), hi = __builtin_amdgcn_readfirstlane((unsigned)((uintptr_t)sbase >> 32));
    asm volatile("s_add_u32 %0, %2, %4\n\ts_addc_u32 %1, %3, 0" : "=&s"(tl), "=s"(th) : "s"(lo), "s"(hi), "i"(GOFF) : "scc");
    const uint64_t t = ((uint64_t)th << 32) | tl;
    if constexpr (SC1) {
        if constexpr (N == 1) GLASM(GL1(" sc1")); else if constexpr (N == 2) GLASM(GL2(" sc1")); else if constexpr (N == 3) GLASM(GL3(" sc1")); else GLASM(GL4(" sc1"));
    } else {
        if constexpr (N == 1) GLASM(GL1("")); else if constexpr (N == 2) GLASM(GL2("")); else if constexpr (N == 3) GLASM(GL3("")); else GLASM(GL4(""));
    }
}
template <int LOFF>
__device__ __forceinline__ void glds_ptr(const void* p, unsigned lbase) {
    unsigned sv; lbase = __builtin_amdgcn_readfirstlane(lbase);
    asm volatile("s_mov_b32 %0, m0\n\ts_add_u32 m0, %2, %3\n\ts_nop 0\n\tglobal_load_lds_dwordx4 %1, off\n\ts_mov_b32 m0, %0"
                 : "=&s"(sv) : "v"(p), "s"(lbase), "i"(LOFF) : "memory", "scc"); }
__device__ __forceinline__ void touch(const void* p, unsigned lds_scratch) {
    unsigned sv;
    asm volatile("s_mov_b32 %0, m0\n\ts_mov_b32 m0, %2\n\ts_nop 0\n\tglobal_load_lds_dword %1, off sc1\n\ts_mov_b32 m0, %0" : "=&s"(sv) : "v"(p), "s"(lds_scratch) : "memory"); }
__device__ __forceinline__ s16x4 tr_read(lds_cptr p) { return __builtin_amdgcn_ds_read_tr16_b64_v4i16((AS3(s16x4)*)p); }
__device__ __forceinline__ bf16x8 cat8(s16x4 a, s16x4 b) {
    s16x8 r = {a[0], a[1], a[2], a[3], b[0], b[1], b[2], b[3]}; return __builtin_bit_cast(bf16x8, r); }
#define LDS_B128(p) (*(const AS3(bf16x8)*)(p))
#define LDS_B64(p) (*(const AS3(s16x4)*)(p))
#define MFMA16(a, b, c) __builtin_amdgcn_mfma_f32_16x16x32_bf16(a, b, c, 0, 0, 0)
#define MFMA32(a, b, c) __builtin_amdgcn_mfma_f32_32x32x16_bf16(a, b, c, 0, 0, 0)
#define MFMA32K8(a, b, c) __builtin_amdgcn_mfma_f32_32x32x8bf16_1k(a, b, c, 0, 0, 0)
#define WAIT_VM_(n) asm volatile("s_waitcnt vmcnt(" #n ")" ::: "memory")
#define WAIT_VM(n) WAIT_VM_(n)
#define RAW_BAR() asm volatile("s_waitcnt lgkmcnt(0)\n\ts_barrier" ::: "memory")
#define SBAR() __builtin_amdgcn_sched_barrier(0)
#define EXP2(v) __builtin_amdgcn_exp2f(v)
#define SMF(a, lo, acc) __builtin_amdgcn_smfmac_f32_32x32x32_bf16(a, __builtin_bit_cast(bf16x16, lo), acc, 0xDDDD, 0, 0)
#define LO8(s) __builtin_shufflevector(s, s, 0, 1, 2, 3, 4, 5, 6, 7)
#define HI8(s) __builtin_shufflevector(s, s, 8, 9, 10, 11, 12, 13, 14, 15)
#define ST128(v, off) __builtin_amdgcn_raw_buffer_store_b128(v, wsr, off, 0, 16)
#define ST64(v, off) __builtin_amdgcn_raw_buffer_store_b64(v, wsr, off, 0, 16)
#define ST32(v, off) __builtin_amdgcn_raw_buffer_store_b32(v, wsr, off, 0, 16)
__device__ __forceinline__ float xhalf_max(float v) {
    auto r = __builtin_amdgcn_permlane32_swap(__float_as_uint(v), __float_as_uint(v), false, false);
    return fmaxf(__uint_as_float(r[0]), __uint_as_float(r[1])); }
__device__ __forceinline__ float xhalf_min(float v) {
    auto r = __builtin_amdgcn_permlane32_swap(__float_as_uint(v), __float_as_uint(v), false, false);
    return fminf(__uint_as_float(r[0]), __uint_as_float(r[1])); }
__device__ __forceinline__ float xhalf_sum(float v) {
    auto r = __builtin_amdgcn_permlane32_swap(__float_as_uint(v), __float_as_uint(v), false, false);
    return __uint_as_float(r[0]) + __uint_as_float(r[1]); }
__device__ __forceinline__ float row_max16(float v) {
    v = fmaxf(v, __int_as_float(__builtin_amdgcn_update_dpp(0, __float_as_int(v), 0x128, 0xf, 0xf, false)));
    v = fmaxf(v, __int_as_float(__builtin_amdgcn_update_dpp(0, __float_as_int(v), 0x124, 0xf, 0xf, false)));
    v = fmaxf(v, __int_as_float(__builtin_amdgcn_update_dpp(0, __float_as_int(v), 0x122, 0xf, 0xf, false)));
    v = fmaxf(v, __int_as_float(__builtin_amdgcn_update_dpp(0, __float_as_int(v), 0x121, 0xf, 0xf, false)));
    return v; }
__device__ __forceinline__ float row_sum16(float v) {
    v += __int_as_float(__builtin_amdgcn_update_dpp(0, __float_as_int(v), 0x128, 0xf, 0xf, false));
    v += __int_as_float(__builtin_amdgcn_update_dpp(0, __float_as_int(v), 0x124, 0xf, 0xf, false));
    v += __int_as_float(__builtin_amdgcn_update_dpp(0, __float_as_int(v), 0x122, 0xf, 0xf, false));
    v += __int_as_float(__builtin_amdgcn_update_dpp(0, __float_as_int(v), 0x121, 0xf, 0xf, false));
    return v; }
__device__ __forceinline__ void publish(char* ws, unsigned off, u64 tag) {
    __hip_atomic_store((u64*)(ws + off), tag, __ATOMIC_RELAXED, __HIP_MEMORY_SCOPE_AGENT); }
__device__ __forceinline__ void wait_flags(const char* ws, unsigned off, u64 tag) {
    for (int spin = 0; spin < (1 << 20); ++spin) {
        const u64 f = __hip_atomic_load((const u64*)(ws + off), __ATOMIC_RELAXED, __HIP_MEMORY_SCOPE_AGENT);
        if (__all(f == tag)) break;
        __builtin_amdgcn_s_sleep(2);
    }
    asm volatile("" ::: "memory");
}

struct KFrag { bf16x8 k0; s16x4 k1; };
struct VFrag { bf16x8 v0, v1; };
__device__ __forceinline__ KFrag ld_k(lds_cptr pa, lds_cptr pb, int off) { KFrag k; k.k0 = LDS_B128(pa + off); k.k1 = LDS_B64(pb + off); return k; }
__device__ __forceinline__ VFrag ld_v2(lds_cptr p, lds_cptr q, int off) {
    VFrag v; v.v0 = cat8(tr_read(p + off), tr_read(q + off)); v.v1 = cat8(tr_read(p + off + 256), tr_read(q + off + 256)); return v; }
#define ld_v(p, off) ld_v2(p, p##2, off)
__device__ __forceinline__ void attn_step(f32x16& sc, f32x16& sn, const KFrag& kn, const bf16x8& vprev1, const bf16x8& vc0,
                                          const bf16x8& qn0, const s16x4& qn1, f32x16& oprev, f32x16& oc) {
    oprev = SMF(vprev1, HI8(sn), oprev); SBAR();
#pragma unroll
    for (int r = 0; r < 4; ++r) sc[r] = EXP2(sc[r]);
    SBAR(); sn = MFMA32(kn.k0, qn0, f32x16{}); SBAR();
#pragma unroll
    for (int r = 4; r < 8; ++r) sc[r] = EXP2(sc[r]);
    SBAR(); sn = MFMA32K8(kn.k1, qn1, sn); SBAR();
#pragma unroll
    for (int r = 8; r < 12; ++r) sc[r] = EXP2(sc[r]);
    SBAR(); oc = SMF(vc0, LO8(sc), oc); SBAR();
#pragma unroll
    for (int r = 12; r < 16; ++r) sc[r] = EXP2(sc[r]);
    SBAR();
}
__device__ __forceinline__ void feature_map(const u32x4 wp0, const u32x2 wp1, const u32x4 f0, const u32x2 f1, unsigned (&ph)[10]) {
    f32x16 pq = MFMA32(__builtin_bit_cast(bf16x8, wp0), __builtin_bit_cast(bf16x8, f0), f32x16{});
    pq = MFMA32K8(__builtin_bit_cast(s16x4, wp1), __builtin_bit_cast(s16x4, f1), pq);
    float mx = fmaxf(pq[0], pq[1]), mn = fminf(pq[0], pq[1]);
#pragma unroll
    for (int u = 2; u < 10; u += 2) { mx = fmaxf(fmaxf(mx, pq[u]), pq[u + 1]); mn = fminf(fminf(mn, pq[u]), pq[u + 1]); }
    mx = xhalf_max(mx); mn = xhalf_min(mn);
    const f32x2 lp = {LOG2E, LOG2E}, cmx = {-mx * LOG2E, -mx * LOG2E}, cmn = {mn * LOG2E, mn * LOG2E};
    f32x2 ep[5], en[5], sp2 = {0.f, 0.f}, sn2 = {0.f, 0.f};
#pragma unroll
    for (int u = 0; u < 5; ++u) {
        const f32x2 p = {pq[2 * u], pq[2 * u + 1]};
        const f32x2 a = __builtin_elementwise_fma(p, lp, cmx), b = __builtin_elementwise_fma(p, -lp, cmn);
        ep[u] = f32x2{EXP2(a[0]), EXP2(a[1])}; en[u] = f32x2{EXP2(b[0]), EXP2(b[1])};
        sp2 += ep[u]; sn2 += en[u];
    }
    const float sp = __builtin_amdgcn_rcpf(xhalf_sum(sp2[0] + sp2[1])), sn = __builtin_amdgcn_rcpf(xhalf_sum(sn2[0] + sn2[1]));
#pragma unroll
    for (int u = 0; u < 5; ++u) {
        const f32x2 a = ep[u] * sp, b = en[u] * sn;
        ph[u] = cvtpk(a[0], a[1]); ph[5 + u] = cvtpk(b[0], b[1]);
    }
}
__device__ __forceinline__ u32x2 split3(float v) {
    const unsigned hi = cvtpk(v, 0.f) & 0xffffu; const float r1 = v - bflo(hi);
    const unsigned mid = cvtpk(r1, 0.f) & 0xffffu; const float r2 = r1 - bflo(mid);
    return u32x2{hi | (mid << 16), cvtpk(r2, 0.f) & 0xffffu};
}
__device__ __forceinline__ float qnorm2(const u32x4& a, const u32x2& b) {
    float n2 = 0.f;
#pragma unroll
    for (int j = 0; j < 4; ++j) { n2 = fmaf(bflo(a[j]), bflo(a[j]), n2); n2 = fmaf(bfhi(a[j]), bfhi(a[j]), n2); }
#pragma unroll
    for (int j = 0; j < 2; ++j) { n2 = fmaf(bflo(b[j]), bflo(b[j]), n2); n2 = fmaf(bfhi(b[j]), bfhi(b[j]), n2); }
    return xhalf_sum(n2);
}

#define LD128(off) __builtin_amdgcn_raw_buffer_load_b128(wsr, off, 0, 16)
#define LD64(off) __builtin_amdgcn_raw_buffer_load_b64(wsr, off, 0, 16)
#define LD32(off) __builtin_amdgcn_raw_buffer_load_b32(wsr, off, 0, 16)
#define BAR() asm volatile("s_barrier" ::: "memory")

__device__ __forceinline__ const char* w_source(int p, int lane, const char* g0, const char* g1, const char* g2) {
    int q = (p & 7) * 64 + lane; const int up = q >= 248; q -= up ? 248 : 0; q = q < 239 ? q : 239;
    const int row = __umul24(q, 2185) >> 16, ch = q - row * 30, sgm = __umul24(ch, 6554) >> 16;
    const uintptr_t b = sgm == 0 ? (uintptr_t)g0 : sgm == 1 ? (uintptr_t)g1 : (uintptr_t)g2;
    return (const char*)(b + (__umul24(16 * (p >> 3) + 8 * up + row, 3840) + (ch - 10 * sgm) * 16));
}

constexpr int P1_STAGE = 49152;
constexpr int L_WPH = 3 * P1_STAGE;
constexpr int L_GW = L_WPH + 3200;
constexpr int L_WO = L_GW + 1280;
constexpr int L_SCR = L_WO + 10240;
constexpr int L_FLAG = L_SCR + 256;
constexpr int LDS_BYTES = L_FLAG + 16;
constexpr int L_IMG = P1_STAGE;
constexpr int L_PH = L_IMG + 32 * 1536;
constexpr int L_Q = L_PH + 40960 + 256;
constexpr int SLOT = 8 * KVT;
constexpr int L_X = 0;
constexpr int L_MISC = L_WO;
constexpr int L_GATE = L_MISC + 3072;
constexpr int L_RA = 4 * SLOT, L_RB = SLOT, L_RC = 2 * SLOT, L_RD = 0, L_RE = L_RC + 6 * 5120;
constexpr int L_CX = L_RC;
static_assert(L_Q + 10240 + 256 <= L_WO && L_RA + 10 * 5120 <= L_WO && L_RE + 3 * 5120 <= L_RA && LDS_BYTES <= 163840, "");
__device__ __forceinline__ constexpr int p3_off(int br, int ks) {
    return br ? (ks < 10 ? L_RA + ks * 5120 : ks < 14 ? L_RB + (ks - 10) * 5120 : L_RC + (ks - 14) * 5120)
              : (ks < 4 ? L_RD + ks * 5120 : ks < 7 ? L_RE + (ks - 4) * 5120 : ks < 17 ? L_RA + (ks - 7) * 5120 : L_RB + (ks - 17) * 5120); }

__global__ __launch_bounds__(768) void fused_kernel(const uint16_t* __restrict__ x, const uint16_t* __restrict__ wA, const uint16_t* __restrict__ wB,
                                                    const uint16_t* __restrict__ woA, const uint16_t* __restrict__ woB,
                                                    const uint16_t* __restrict__ wphi, const uint16_t* __restrict__ gatew,
                                                    char* __restrict__ ws, uint16_t* __restrict__ out) {
    __shared__ __attribute__((aligned(16))) char lds[LDS_BYTES];
    uint64_t pc0; asm volatile("s_getpc_b64 %0" : "=s"(pc0));
    const int bid = blockIdx.x, xcd = bid & 7, bi = bid >> 3;
    const int tid = threadIdx.x, lane = tid & 63;
    const int wid = __builtin_amdgcn_readfirstlane(tid >> 6);
    const unsigned lds0 = (unsigned)(uintptr_t)lds;
    const lds_cptr L = (lds_cptr)lds;
    const unsigned l16 = lane * 16;
    const int g = lane >> 4, l15 = lane & 15, q4 = l15 >> 2, p4 = lane & 3, r32 = lane & 31, hh = lane >> 5;
    const auto wsr = __builtin_amdgcn_make_buffer_rsrc(ws, 0, WS_END, 0x00020000);
    const int rt = (xcd & 1) * 8 + (bi & 7), kd = bi >> 4, hj = (xcd >> 1) * 2 + ((bi >> 3) & 1);
    const int h = xcd * 4 + (bi >> 3), qt = bi & 7;
    const int tt = xcd * 4 + (bi & 3), ct = bi >> 2;
    const char* xb = (const char*)x + (size_t)rt * 128 * 1280;
    const unsigned ka = lane * 16, kb = 1024 + r32 * 8;
    const char* wg0 = (const char*)wA + hj * 160 + (kd ? 2560 : 0);
    const char* wg1 = kd ? (const char*)wB + hj * 160 + 1280 : (const char*)wA + hj * 160 + 1280;
    const char* wg2 = (const char*)wB + hj * 160 + (kd ? 2560 : 0);
#define wsrc(p) w_source(p, lane, wg0, wg1, wg2)
#define P3_WKS(br, ks, dst) do { _Pragma("unroll") for (int j_ = 0; j_ < 5; ++j_) { const int q_ = j_ * 64 + lane, c_ = __umul24(q_, 52429) >> 22, n_ = q_ - c_ * 80; \
        glds_run<1, 0, 0, true>(ws + WS_WOP + ct * 1280 + ((br) * 80 + 4 * (ks)) * 10240, c_ * 10240 + n_ * 16, lds0 + (dst) + j_ * 1024); } } while (0)
    u64 tag = 0;
#define MAKE_TAG() do { u64 z = ((u64)(unsigned)__builtin_amdgcn_readfirstlane((unsigned)(tag >> 32)) << 32) | (unsigned)__builtin_amdgcn_readfirstlane((unsigned)tag); \
        z = z * 0x9E3779B97F4A7C15ull + 0x632BE59BD9B4E019ull; z ^= z >> 29; z *= 0xBF58476D1CE4E5B9ull; z ^= z >> 32; tag = z; } while (0)

    if (wid >= 8) {
        const int hw = wid - 8;
        __builtin_amdgcn_s_setprio(2);
        unsigned hbt = 0;
        if (tid == 512) *(AS3(unsigned)*)((lds_ptr)lds + L_FLAG + 4) = 0u;
#define HBAR() do { hbt += 4; asm volatile("s_waitcnt lgkmcnt(0)" ::: "memory"); \
        if (lane == 0) __hip_atomic_fetch_add((AS3(unsigned)*)((lds_ptr)lds + L_FLAG + 4), 1u, __ATOMIC_RELAXED, __HIP_MEMORY_SCOPE_WORKGROUP); \
        while (__builtin_amdgcn_readfirstlane(__hip_atomic_load((AS3(unsigned)*)((lds_ptr)lds + L_FLAG + 4), __ATOMIC_RELAXED, __HIP_MEMORY_SCOPE_WORKGROUP)) < hbt) __builtin_amdgcn_s_sleep(1); \
        asm volatile("" ::: "memory"); } while (0)
        const char* pw[4];
#pragma unroll
        for (int j = 0; j < 4; ++j) pw[j] = wsrc(16 + 4 * hw + j);
        auto wstage = [&](int slot) {
            const unsigned lw = lds0 + slot * P1_STAGE + 32768 + hw * 4096;
            glds_ptr<0>(pw[0], lw); glds_ptr<1024>(pw[1], lw); glds_ptr<2048>(pw[2], lw); glds_ptr<3072>(pw[3], lw);
#pragma unroll
            for (int j = 0; j < 4; ++j) pw[j] += 245760;
        };
        if (hj == 0 && kd == 0 && hw < 2 && hw * 1024 + l16 < 1280) glds_run<1, 0, L_GW, false>(gatew, hw * 1024 + l16, lds0 + hw * 1024);
        wstage(0);
        RAW_BAR();
        wstage(1);
        WAIT_VM(4);
        BAR();
        wstage(2);
        const char* tp = hw == 0 ? xb + ((bi >> 3) * 32 + r32) * 1280 + 3 * 128
                       : hw == 1 ? ((lane % 6) < 2 ? wg0 : (lane % 6) < 4 ? wg1 : wg2) + ((bi & 7) * 8 + (lane < 48 ? lane / 6 : 0) + 192) * 3840 + (lane & 1) * 156 : xb;
        const unsigned tstep = hw == 0 ? 128u : hw == 1 ? 245760u : 0u;
        touch(tp, lds0 + L_SCR); tp += tstep; touch(tp, lds0 + L_SCR); tp += tstep;
        { const unsigned o = (hw * 64 + lane) * 128; touch((const char*)pc0 + (o < CODE_BYTES ? o : 0u), lds0 + L_SCR); }
        for (int s = 0; s < 9; ++s) {
            if (s == 0) { WAIT_VM(7); } else if (s == 1) { WAIT_VM(8); } else if (s == 2 || s == 3) { WAIT_VM(11); } else if (s < 6) { WAIT_VM(6); } else if (s == 6) { WAIT_VM(5); } else if (s == 7) { WAIT_VM(4); } else { WAIT_VM(0); }
            BAR();
            if (s < 7) wstage(s % 3);
            if (s < 5) { touch(tp, lds0 + L_SCR); tp += tstep; }
            if (s == 1) {
                const int cb = bid < 160 ? bid : 0, ch = cb < 80 ? cb : cb - 80;
                const int r0 = (ch < 64 ? (ch >> 1) * 20 + (ch & 1) * 4 : (ch - 64) * 40 + 16) + hw, dr = (ch < 64 ? 8 : 20) * 1280;
                const char* pr = (const char*)(cb < 80 ? woA : woB) + r0 * 1280; const unsigned lr = lds0 + hw * 1280;
                glds_run<1, 0, L_WO, false>(pr, l16, lr); glds_run<1, 0, L_WO + 5120, false>(pr + dr, l16, lr);
                if (lane < 16) { glds_run<1, 1024, L_WO + 1024, false>(pr, l16, lr); glds_run<1, 1024, L_WO + 6144, false>(pr + dr, l16, lr); }
                if (hw * 1024 + l16 < 3200) glds_run<1, 0, L_WPH, false>((const char*)wphi + hj * 3200, hw * 1024 + l16, lds0 + hw * 1024);
            }
            if (s == 7) tag = __hip_atomic_load((const u64*)(ws + WS_EPOCH), __ATOMIC_RELAXED, __HIP_MEMORY_SCOPE_AGENT);
            if (s == 8 && bid < 160) {
                const lds_cptr r = L + L_WO + q4 * 1280 + g * 32 + p4 * 8 + hw * 128;
#pragma unroll
                for (int j = 0; j < 3; ++j)
                    if (j < 2 || hw < 2) ST128(__builtin_bit_cast(u32x4, cat8(tr_read(r + j * 512), tr_read(r + j * 512 + 5120))), WS_WOP + (bid * 640 + (j * 4 + hw) * 64 + lane) * 16);
            }
        }
        MAKE_TAG();
        WAIT_VM(0);
        BAR();
        BAR();
        const unsigned fhoff = WS_FH + (((4 * hj + (lane & 3)) * 4 + kd) * 16 + rt) * 8;
        if (hw == 0 && lane < 5) publish(ws, lane < 4 ? fhoff : WS_FT + bid * 8, tag);
        if (kd) BAR();
        const char* kvbase = ws + WS_KV + (size_t)h * 64 * KVT;
        const char* kvw = kvbase + (size_t)(hw >> 1) * 4 * SLOT + (hw & 1) * 12288; const unsigned ldw = lds0 + (hw >> 1) * SLOT + (hw & 1) * 12288;
#define H_CHUNK(j) do { glds_run<4, (j) * SLOT, ((j) & 1) * 2 * SLOT, true>(kvw, l16, ldw); glds_run<4, (j) * SLOT + 4096, ((j) & 1) * 2 * SLOT + 4096, true>(kvw, l16, ldw); \
                        glds_run<4, (j) * SLOT + 8192, ((j) & 1) * 2 * SLOT + 8192, true>(kvw, l16, ldw); } while (0)
        if (hw == 0) wait_flags(ws, WS_FH + (h * 64 + r32) * 8, tag);
        BAR();
        {
            const char* pq = ws + WS_QA + ((size_t)h * 64 + qt * 8) * QFT;
            if (hw < 2) glds_run<3, 0, L_Q, true>(pq + hw * 3072, l16, lds0 + hw * 3072); else glds_run<2, 0, L_Q, true>(pq + 2048 + hw * 2048, l16, lds0 + 2048 + hw * 2048);
            if (hw == 3 && lane < 16) glds_run<1, 0, L_Q + 10240, true>(ws + WS_KMAX + h * 256, l16, lds0);
        }
        WAIT_VM(0);
        BAR();
        {
            if (kd) { if (hw == 0 && lane < 4) publish(ws, fhoff + 256, tag); }
            else {
#pragma unroll
                for (int j = 0; j < 10; ++j) ST128(*(const AS3(u32x4)*)(L + L_PH + hw * 10240 + j * 1024 + l16), WS_PHQ + ((4 * hj + hw) * 64 + rt * 4) * 2560 + j * 1024 + l16);
                WAIT_VM(0);
                if (lane == hw) publish(ws, fhoff + 256, tag);
            }
            H_CHUNK(1);
            wait_flags(ws, WS_FT + (64 * hw + lane) * 8, tag);
            WAIT_VM(0);
            BAR();
            {
                wait_flags(ws, WS_FH + (lane < 32 ? h * 64 + 32 + lane : 2 * qt + (lane & 1)) * 8, tag);
                H_CHUNK(2);
                const int e = 64 * hw + lane;
                f32x4 ks = {0.f, 0.f, 0.f, 0.f};
#pragma unroll
                for (int j = 0; j < 16; ++j) ks += __builtin_bit_cast(f32x4, LD128(WS_KVP + (h * 16 + j) * 3840 + (e < 240 ? e : 0) * 16));
                u32x4 pf[2][2]; u32x2 pg[2]; float gt[2];
#pragma unroll
                for (int t = 0; t < 2; ++t) {
                    const unsigned o = WS_PHQ + (h * 64 + qt * 8 + 2 * hw + t) * 2560;
                    pf[t][0] = LD128(o + l16); pf[t][1] = LD128(o + 1024 + l16); pg[t] = LD64(o + 2048 + lane * 8);
                    gt[t] = __uint_as_float(LD32(WS_G + (qt * 256 + (2 * hw + t) * 32 + r32) * 4));
                }
                if (e < 240) {
                    const int ph = e / 6, d = 4 * (e - ph * 6), ph_h = ph / 20, u = ph - ph_h * 20;
                    const unsigned w0 = cvtpk(ks[0], ks[1]), w1 = cvtpk(ks[2], ks[3]);
                    lds_ptr dd = (lds_ptr)lds + L_MISC + d * 96 + ph_h * 48 + u * 2;
                    *(AS3(uint16_t)*)(dd) = (uint16_t)(w0 & 0xffffu); *(AS3(uint16_t)*)(dd + 96) = (uint16_t)(w0 >> 16);
                    *(AS3(uint16_t)*)(dd + 192) = (uint16_t)(w1 & 0xffffu); *(AS3(uint16_t)*)(dd + 288) = (uint16_t)(w1 >> 16);
                }
                *(AS3(float)*)((lds_ptr)lds + L_GATE + (64 * hw + lane) * 4) = hh ? gt[1] : gt[0];
                WAIT_VM(0);
                RAW_BAR();
                H_CHUNK(3);
                const lds_cptr a = L + L_MISC + r32 * 96 + hh * 48;
                const bf16x8 a0 = LDS_B128(a), a1 = LDS_B128(a + 16); const s16x4 a2 = LDS_B64(a + 32);
#pragma unroll
                for (int t = 0; t < 2; ++t) {
                    f32x16 lo = MFMA32(a0, __builtin_bit_cast(bf16x8, pf[t][0]), f32x16{});
                    lo = MFMA32(a1, __builtin_bit_cast(bf16x8, pf[t][1]), lo);
                    lo = MFMA32K8(a2, __builtin_bit_cast(s16x4, pg[t]), lo);
                    auto rb = __builtin_amdgcn_permlane32_swap(__float_as_uint(lo[8]), __float_as_uint(lo[8]), false, false);
                    const float sb = (1.0f - gt[t]) / (__uint_as_float(rb[1]) + 1e-6f);
                    const unsigned oc = WS_OC + ((qt * 8 + 2 * hw + t) * 32 + r32) * 16;
                    ST128((u32x4{cvtpk(lo[0] * sb, lo[1] * sb), cvtpk(lo[2] * sb, lo[3] * sb), cvtpk(lo[4] * sb, lo[5] * sb), cvtpk(lo[6] * sb, lo[7] * sb)}), oc + (80 + 2 * h + hh) * (SEQ * 16));
                    if (!hh) ST64((u32x2{cvtpk(lo[8] * sb, lo[9] * sb), cvtpk(lo[10] * sb, lo[11] * sb)}), oc + (80 + 64 + (h >> 1)) * (SEQ * 16) + (h & 1) * 8);
                }
            }
            WAIT_VM(0);
            HBAR();
            if (tid == 512) { publish(ws, WS_FOB + (qt * 32 + h) * 8, tag); *(AS3(unsigned)*)((lds_ptr)lds + L_FLAG) = 0u; }
            for (int ks = hw; ks < 10; ks += 4) P3_WKS(1, ks, L_RA + ks * 5120);
            RAW_BAR();
            P3_WKS(1, 10 + hw, L_RB + hw * 5120);
            { const int ln = hw * 64 + lane; touch(ws + WS_WOP + bi * 25600 + (ln < 200 ? ln : 0) * 128, lds0 + L_SCR); }
            if (hw == 0) wait_flags(ws, WS_FOB + (xcd * 32 + r32) * 8, tag);
            WAIT_VM(0);
            RAW_BAR();
            BAR();
            const unsigned redo = *(const AS3(unsigned)*)(L + L_FLAG);
            if (__builtin_amdgcn_readfirstlane(redo) != 0u) {
                RAW_BAR();
                H_CHUNK(0); WAIT_VM(0); BAR();
                H_CHUNK(1); WAIT_VM(0); BAR();
                H_CHUNK(2); WAIT_VM(0); BAR();
                H_CHUNK(3);
                if (tid == 512) *(AS3(unsigned)*)((lds_ptr)lds + L_FLAG) = 0u;
                WAIT_VM(0); RAW_BAR();
                P3_WKS(1, 10 + hw, L_RB + hw * 5120); WAIT_VM(0);
                BAR(); BAR();
            }
        }
        if (tid == 512) publish(ws, WS_FOA + (qt * 32 + h) * 8, tag);
        for (int ks = 14 + hw; ks < 20; ks += 4) P3_WKS(1, ks, L_RC + (ks - 14) * 5120);
        WAIT_VM(0);
        BAR();
        for (int ks = hw; ks < 7; ks += 4) P3_WKS(0, ks, ks < 4 ? L_RD + ks * 5120 : L_RE + (ks - 4) * 5120);
        BAR();
        if (hw == 3) wait_flags(ws, WS_FOA + (xcd * 32 + r32) * 8, tag);
        WAIT_VM(0);
        BAR();
        BAR();
        BAR();
    } else {
        const int th = wid >> 2, cw = wid & 3;
        {
            unsigned vx[2];
#pragma unroll
            for (int j = 0; j < 2; ++j) { const int row = 8 * (wid * 2 + j) + (lane >> 3); vx[j] = row * 1280 + (((lane ^ row) & 7) << 4); }
            const unsigned lx = lds0 + wid * 2048;
            const char* pw0 = wsrc(2 * wid); const char* pw1 = wsrc(2 * wid + 1);
#define P1_X(s, slot) do { glds_run<1, 0, 0, false>(xb + (s) * 128, vx[0], lx + (slot) * P1_STAGE); glds_run<1, 0, 1024, false>(xb + (s) * 128, vx[1], lx + (slot) * P1_STAGE); \
                           glds_ptr<16384>(pw0, lx + (slot) * P1_STAGE); glds_ptr<17408>(pw1, lx + (slot) * P1_STAGE); pw0 += 245760; pw1 += 245760; } while (0)
            P1_X(0, 0);
            BAR();
            P1_X(1, 1);
            const bool t3 = cw < 3 || (hj == 0 && kd == 0);
            const int xrow = 64 * th + l15;
            lds_cptr xa0 = L + xrow * 128 + ((g ^ (xrow & 7)) << 4), xa1 = L + ((xrow * 128 + ((g ^ (xrow & 7)) << 4)) ^ 64);
            const unsigned wa0 = 16384 + 8192 * (g >> 1) + 480 * (8 * (g & 1) + q4) + 128 * (g & 1) + 40 * p4 + (cw < 3 ? 160 * cw : 32);
            const unsigned wst = cw < 3 ? 8 : 160;
            lds_cptr wa[4];
#pragma unroll
            for (int i = 0; i < 3; ++i) wa[i] = L + wa0 + i * wst;
            wa[3] = cw < 3 ? L + wa0 + 24 : L + L_GW + g * 16;
            f32x4 acc[4][4];
#pragma unroll
            for (int m = 0; m < 4; ++m)
#pragma unroll
                for (int i = 0; i < 4; ++i) acc[m][i] = f32x4{0.f, 0.f, 0.f, 0.f};
            bf16x8 xA[4], wA_[4], xB[4], wB_[4];
#define P1_LOAD(X, W, ks, NT, G3) do { \
        _Pragma("unroll") for (int i = 0; i < 3; ++i) W[i] = cat8(tr_read(wa[i] + (ks) * 16384), tr_read(wa[i] + (ks) * 16384 + 1920)); \
        if (NT == 4) { if (G3) W[3] = LDS_B128(wa[3] + (ks) * 64); else W[3] = cat8(tr_read(wa[3] + (ks) * 16384), tr_read(wa[3] + (ks) * 16384 + 1920)); } \
        _Pragma("unroll") for (int m = 0; m < 4; ++m) X[m] = LDS_B128(((ks) ? xa1 : xa0) + m * 2048); } while (0)
#define P1_MMA(X, W, i, NT) do { if ((i) < NT) { _Pragma("unroll") for (int m = 0; m < 4; ++m) acc[m][i] = MFMA16(W[i], X[m], acc[m][i]); } } while (0)
#define P1_HALF(X, W, XN, WN, ks, NT, G3, MID1, MID2) do { \
        _Pragma("unroll") for (int i = 0; i < NT; ++i) { \
            s16x4 lo_, hi_; \
            acc[0][i] = MFMA16(W[i], X[0], acc[0][i]); SBAR(); \
            if (i == 3 && G3) WN[3] = LDS_B128(wa[3] + (ks) * 64); else lo_ = tr_read(wa[i] + (ks) * 16384); SBAR(); \
            acc[1][i] = MFMA16(W[i], X[1], acc[1][i]); SBAR(); \
            if (!(i == 3 && G3)) { hi_ = tr_read(wa[i] + (ks) * 16384 + 1920); WN[i] = cat8(lo_, hi_); } SBAR(); \
            acc[2][i] = MFMA16(W[i], X[2], acc[2][i]); SBAR(); \
            XN[i] = LDS_B128(((ks) ? xa1 : xa0) + i * 2048); SBAR(); \
            acc[3][i] = MFMA16(W[i], X[3], acc[3][i]); SBAR(); \
            if (NT == 3 && i == 2) XN[3] = LDS_B128(((ks) ? xa1 : xa0) + 3 * 2048); \
            if (i == 0) { MID1; } if (i == 1) { MID2; } SBAR(); \
        } } while (0)
#define P1_DX(lim, st, sl) do { unsigned sv_; const char* xs_ = xb + (st) * 128; \
                  asm volatile("s_cmp_gt_u32 %1, " #lim "\n\ts_cbranch_scc1 1f\n\ts_mov_b32 %0, m0\n\ts_mov_b32 m0, %5\n\ts_nop 0\n\tglobal_load_lds_dwordx4 %2, %4\n\t" \
                               "s_add_u32 m0, %5, 1024\n\ts_nop 0\n\tglobal_load_lds_dwordx4 %3, %4\n\ts_mov_b32 m0, %0\n1:" \
                               : "=&s"(sv_) : "s"(s), "v"(vx[0]), "v"(vx[1]), "s"(xs_), "s"(lx + (sl) * P1_STAGE) : "memory", "scc"); } while (0)
#define P1_DW(lim, sl) do { unsigned sv_; \
                  asm volatile("s_cmp_gt_u32 %3, " #lim "\n\ts_cbranch_scc1 1f\n\ts_mov_b32 %0, m0\n\ts_add_u32 m0, %4, 16384\n\ts_nop 0\n\tglobal_load_lds_dwordx4 %1, off\n\t" \
                               "s_add_u32 m0, %4, 17408\n\tv_lshl_add_u64 %1, %1, 0, %5\n\tglobal_load_lds_dwordx4 %2, off\n\ts_mov_b32 m0, %0\n\tv_lshl_add_u64 %2, %2, 0, %5\n1:" \
                               : "=&s"(sv_), "+v"(pw0), "+v"(pw1) : "s"(s), "s"(lx + (sl) * P1_STAGE), "s"(245760ull) : "memory", "scc"); } while (0)
#define P1_LOOP(NT, G3) do { \
            P1_LOAD(xA, wA_, 0, NT, G3); \
            int slot = 0; \
            _Pragma("nounroll") for (int s = 0; s < 10; ++s) { \
                P1_HALF(xA, wA_, xB, wB_, 1, NT, G3, P1_DX(0, 2, 2), P1_DW(0, 2)); \
                  \
                asm volatile("s_cmp_gt_u32 %0, 7\n\ts_cbranch_scc1 1f\n\ts_waitcnt vmcnt(4) lgkmcnt(0)\n\ts_barrier\n\ts_branch 2f\n" \
                             "1:\n\ts_cmp_eq_u32 %0, 9\n\ts_cbranch_scc1 2f\n\ts_waitcnt vmcnt(0) lgkmcnt(0)\n\ts_barrier\n2:" :: "s"(s) : "memory", "scc"); \
                const int d = slot == 2 ? -2 * P1_STAGE : P1_STAGE; \
                xa0 += d; xa1 += d; wa[0] += d; wa[1] += d; wa[2] += d; wa[3] += G3 ? 128 : d; SBAR(); \
                P1_HALF(xB, wB_, xA, wA_, 0, NT, G3, P1_DX(6, s + 3, slot), P1_DW(6, slot)); \
                slot = slot == 2 ? 0 : slot + 1; \
            } } while (0)
            WAIT_VM(4);
            BAR();
            if (cw < 3) P1_LOOP(4, false); else if (t3) P1_LOOP(4, true); else P1_LOOP(3, false);
            const lds_ptr img = (lds_ptr)lds + L_IMG;
            {
                const int ht = (4 * hj + g) * 64 + rt * 4 + 2 * th;
                float kn = 0.f;
                if (cw < 3) {
                    const bool isv = kd ? cw != 1 : false, toA = kd ? cw == 0 : cw < 2;
                    if (kd == 0 && cw == 0) {
#pragma unroll
                        for (int m = 0; m < 4; ++m)
#pragma unroll
                            for (int i = 0; i < 4; ++i) acc[m][i] *= QSCALE;
                    }
                    const unsigned ts = (kd == 0 && cw == 0) ? QFT : KVT, tb = ((kd == 0 && cw == 0) ? WS_QA : WS_KV + (kd ? 1536 : 0)) + ht * ts;
                    const unsigned o0 = l15 * 16, o1 = 512 + (isv ? l15 ^ 4 : l15) * 16;
#pragma unroll
                    for (int m = 0; m < 4; ++m) {
                        const u32x4 w0 = {cvtpk(acc[m][0][0], acc[m][0][1]), cvtpk(acc[m][0][2], acc[m][0][3]), cvtpk(acc[m][1][0], acc[m][1][1]), cvtpk(acc[m][1][2], acc[m][1][3])};
                        const u32x4 w1 = {cvtpk(acc[m][2][0], acc[m][2][1]), cvtpk(acc[m][2][2], acc[m][2][3]), cvtpk(acc[m][3][0], acc[m][3][1]), cvtpk(acc[m][3][2], acc[m][3][3])};
                        if (toA) { const unsigned mo = tb + (m >> 1) * ts + (m & 1) * 256; ST128(w0, mo + o0); ST128(w1, mo + o1); }
                        else { const lds_ptr d = img + (((isv ? 4 : 0) + 2 * th + (m >> 1)) * 4 + g) * 1536 + (m & 1) * 256; *(AS3(u32x4)*)(d + o0) = w0; *(AS3(u32x4)*)(d + o1) = w1; }
                    }
                    if (kd == 0 && cw == 1) {
#pragma unroll
                        for (int m = 0; m < 4; ++m) {
                            float n2 = 0.f;
#pragma unroll
                            for (int i = 0; i < 4; ++i)
#pragma unroll
                                for (int r = 0; r < 4; ++r) n2 = fmaf(acc[m][i][r], acc[m][i][r], n2);
                            kn = (n2 == n2) ? fmaxf(kn, n2) : __builtin_inff();
                        }
                    }
                } else {
#pragma unroll
                    for (int m = 0; m < 4; ++m) {
                        const unsigned ok = WS_KV + (ht + (m >> 1)) * KVT + 1024, o8 = (m & 1) * 128 + l15 * 8, o16 = (m & 1) * 256 + (l15 ^ 8) * 16;
                        const lds_ptr d = img + ((2 * th + (m >> 1)) * 4 + g) * 1536 + 1024;
                        const u32x2 w1 = {cvtpk(acc[m][1][0], acc[m][1][1]), cvtpk(acc[m][1][2], acc[m][1][3])}, w2 = {cvtpk(acc[m][2][0], acc[m][2][1]), cvtpk(acc[m][2][2], acc[m][2][3])};
                        if (kd == 0) {
                            ST64((u32x2{cvtpk(acc[m][0][0] * QSCALE, acc[m][0][1] * QSCALE), cvtpk(acc[m][0][2] * QSCALE, acc[m][0][3] * QSCALE)}), WS_QA + (ht + (m >> 1)) * QFT + 1024 + o8);
                            ST128((u32x4{w1[0], w1[1], 0x3f803f80u, 0x00003f80u}), ok + (m & 1) * 256 + l15 * 16);
                            *(AS3(u32x2)*)(d + o8) = w2;
                            float n2 = 0.f;
#pragma unroll
                            for (int r = 0; r < 4; ++r) n2 = fmaf(acc[m][1][r], acc[m][1][r], n2);
                            kn = (n2 == n2) ? fmaxf(kn, n2) : __builtin_inff();
                        } else {
                            ST128((u32x4{cvtpk(acc[m][0][0], acc[m][0][1]), cvtpk(acc[m][0][2], acc[m][0][3]), 0x00003f80u, 0u}), ok + 1536 + o16);
                            *(AS3(u32x2)*)(d + o8) = w1;
                            *(AS3(u32x4)*)(d + 16 * 1536 + o16) = u32x4{w2[0], w2[1], 0x00003f80u, 0u};
                        }
                    }
                }
                if (cw == 3 && t3 && g == 0) {
#pragma unroll
                    for (int m = 0; m < 4; ++m) ST32(__float_as_uint(1.0f / (1.0f + __expf(-acc[m][3][0]))), WS_G + (rt * 128 + 64 * th + 16 * m + l15) * 4);
                }
                if (kd == 0 && (cw & 1)) {
                    kn = row_max16(kn);
                    if (l15 == 0) ST32(__float_as_uint(kn), WS_KMAX + ((4 * hj + g) * 64 + (cw == 3 ? 32 : 0) + rt * 2 + th) * 4);
                }
            }
            u32x4 wp0; u32x2 wp1;
            {
                const int fr = (r32 & 3) + 4 * (r32 >> 3);
                const lds_cptr wp = L + L_WPH + cw * 800 + (fr < 10 ? 10 * ((r32 >> 2) & 1) + fr : 0) * 2;
                unsigned wv[12];
#pragma unroll
                for (int j = 0; j < 8; ++j) wv[j] = *(const AS3(uint16_t)*)(wp + hh * 320 + j * 40);
#pragma unroll
                for (int j = 0; j < 4; ++j) wv[8 + j] = *(const AS3(uint16_t)*)(wp + 640 + j * 40);
                const unsigned m0 = fr < 10 ? 0xffffffffu : 0u, m1 = (fr < 10 && hh == 0) ? 0xffffffffu : 0u;
                wp0 = u32x4{(wv[0] | (wv[1] << 16)) & m0, (wv[2] | (wv[3] << 16)) & m0, (wv[4] | (wv[5] << 16)) & m0, (wv[6] | (wv[7] << 16)) & m0};
                wp1 = u32x2{(wv[8] | (wv[9] << 16)) & m1, (wv[10] | (wv[11] << 16)) & m1};
            }
            RAW_BAR();
#pragma unroll
            for (int t2 = 0; t2 < 2; ++t2) {
                const lds_cptr f = L + L_IMG + ((2 * th + t2) * 4 + cw) * 1536;
                unsigned ph[10];
                feature_map(wp0, wp1, *(const AS3(u32x4)*)(f + ka), *(const AS3(u32x2)*)(f + kb), ph);
                const lds_ptr d = (lds_ptr)lds + L_PH + (cw * 4 + 2 * th + t2) * 2560;
                if (kd) {
#pragma unroll
                    for (int u = 0; u < 5; ++u) *(AS3(u32x2)*)(d + r32 * 80 + hh * 40 + u * 8) = u32x2{ph[2 * u], ph[2 * u + 1]};
                } else {
                    *(AS3(u32x4)*)(d + l16) = u32x4{ph[0], ph[1], ph[2], ph[3]}; *(AS3(u32x4)*)(d + 1024 + l16) = u32x4{ph[4], ph[5], ph[6], ph[7]};
                    *(AS3(u32x2)*)(d + 2048 + lane * 8) = u32x2{ph[8], ph[9]};
                }
                if (t2 == 0) {
                    SBAR();
                    WAIT_VM(0);
                    BAR();
                    SBAR();
                }
            }
            if (kd) {
                RAW_BAR();
#pragma unroll
                for (int k2 = 0; k2 < 2; ++k2) {
                    if (k2 == 0 || wid < 4) {
                        const int u = wid + 8 * k2, uh = u / 3, un = u - 3 * uh;
                        f32x4 c0 = {0.f, 0.f, 0.f, 0.f}, c1 = {0.f, 0.f, 0.f, 0.f};
                        const unsigned vo = L_IMG + (16 + uh) * 1536;
                        const unsigned vb = 512 * (p4 >> 1) + 256 * (g >> 1) + 64 * ((2 * (g & 1)) ^ (p4 >> 1)) + 16 * q4 + 8 * (p4 & 1);
                        const unsigned vc = 1024 + 256 * (g >> 1) + 64 * ((2 * (g & 1)) ^ 2) + 16 * q4 + (p4 ? 8 : 0);
                        const unsigned pb = L_PH + (uh * 128 + 8 * g + q4) * 80 + un * 32 + p4 * 8;
#pragma unroll
                        for (int ks = 0; ks < 4; ++ks) {
                            const bf16x8 a0 = cat8(tr_read(L + vo + vb + ks * 6144), tr_read(L + vo + (vb ^ 64) + ks * 6144));
                            const bf16x8 a1 = cat8(tr_read(L + vo + vc + ks * 6144), tr_read(L + vo + (vc ^ 64) + ks * 6144));
                            const bf16x8 b = cat8(tr_read(L + pb + ks * 2560), tr_read(L + pb + ks * 2560 + 320));
                            c0 = MFMA16(a0, b, c0); c1 = MFMA16(a1, b, c1);
                        }
                        const int ph = 16 * un + l15;
                        const unsigned off = ph < 40 ? WS_KVP + ((4 * hj + uh) * 16 + rt) * 3840 + ph * 96 + 16 * g : OOB;
                        ST128(__builtin_bit_cast(u32x4, c0), off); ST128(__builtin_bit_cast(u32x4, c1), g < 2 ? off + 64 : OOB);
                    }
                }
            }
        }
        const int qp = wid & 3, kh = wid >> 2;
        const int own = 2 * qp + kh;
        const int blk = (lane >> 4) & 1, vj = blk ? 2 : p4 >> 1;
        const unsigned kb2 = 1024 + r32 * 16 + hh * 8;
        const unsigned va = 1536 + 512 * vj + 64 * ((2 * hh) ^ vj) + 16 * q4 + (blk ? (p4 ? 8 : 0) : 8 * (p4 & 1)), vb2 = va ^ 64;
        RAW_BAR();
        {
            const char* src = ws + WS_KV + ((size_t)h * 64 + kh * 32) * KVT + qp * 6144; const unsigned dst = lds0 + kh * SLOT + qp * 6144;
            glds_run<4, 0, 0, true>(src, l16, dst); glds_run<2, 4096, 4096, true>(src, l16, dst);
        }
        WAIT_VM(0);
        BAR();
        const lds_cptr lq = L + L_Q + 2 * qp * QFT;
        const u32x4 qa0 = *(const AS3(u32x4)*)(lq + ka), qb0 = *(const AS3(u32x4)*)(lq + QFT + ka);
        u32x2 qa1 = *(const AS3(u32x2)*)(lq + kb), qb1 = *(const AS3(u32x2)*)(lq + QFT + kb);
        float kmx = *(const AS3(float)*)(L + L_Q + 10240 + lane * 4);
        if (hh) { qa1 = u32x2{0u, 0u}; qb1 = u32x2{0u, 0u}; }
        const bf16x8 QA0 = __builtin_bit_cast(bf16x8, qa0), QB0 = __builtin_bit_cast(bf16x8, qb0);
        float nba, nbb;
        {
            kmx = row_max16(kmx);
            auto r = __builtin_amdgcn_permlane16_swap(__float_as_uint(kmx), __float_as_uint(kmx), false, false);
            kmx = fmaxf(__uint_as_float(r[0]), __uint_as_float(r[1]));
            const float kn = -1.01f * __builtin_amdgcn_sqrtf(xhalf_sum(kmx));
            nba = kn * __builtin_amdgcn_sqrtf(qnorm2(qa0, qa1)); nbb = kn * __builtin_amdgcn_sqrtf(qnorm2(qb0, qb1));
        }
        for (int attempt = 0;; ++attempt) {
            f32x16 oa, ob, sA, sB;
#pragma unroll
            for (int r = 0; r < 16; ++r) { oa[r] = 0.f; ob[r] = 0.f; sB[r] = 0.f; }
            const s16x4 QA1 = __builtin_bit_cast(s16x4, hh ? split3(nba) : qa1), QB1 = __builtin_bit_cast(s16x4, hh ? split3(nbb) : qb1);
            lds_cptr pka = L + kh * SLOT + ka, pkb = L + kh * SLOT + kb2, pv = L + kh * SLOT + va, pv2 = L + kh * SLOT + vb2;
            KFrag k0 = ld_k(pka, pkb, 0), k1 = ld_k(pka, pkb, KVT);
            VFrag v0 = ld_v(pv, 0), v1 = v0;
            sA = MFMA32(k0.k0, QA0, f32x16{}); sA = MFMA32K8(k0.k1, QA1, sA);
            for (int c = 0; c < 4; ++c) {
                const int dnext = (c & 1) ? -2 * SLOT : 2 * SLOT;
#define K2_PAIR(t, LOADK0, LOADV0, LOADK1) do { \
                bf16x8 vp = v1.v1; \
                v1 = ld_v(pv, ((t) + 1) * KVT); SBAR(); \
                attn_step(sA, sB, k0, vp, v0.v0, QB0, QB1, ob, oa); \
                LOADK0; SBAR(); \
                attn_step(sB, sA, k1, v0.v1, v0.v0, QA0, QA1, oa, ob); \
                vp = v0.v1; \
                LOADV0; SBAR(); \
                attn_step(sA, sB, k1, vp, v1.v0, QB0, QB1, ob, oa); \
                LOADK1; SBAR(); \
                attn_step(sB, sA, k0, v1.v1, v1.v0, QA0, QA1, oa, ob); } while (0)
#define PRIO(p) do { asm volatile("s_cmp_eq_u32 %0, 0\n\ts_cbranch_scc1 1f\n\ts_setprio " #p "\n1:" :: "s"(kh) : "scc"); SBAR(); } while (0)
                K2_PAIR(0, k0 = ld_k(pka, pkb, 2 * KVT), v0 = ld_v(pv, 2 * KVT), k1 = ld_k(pka, pkb, 3 * KVT));
                PRIO_A;
                K2_PAIR(2, k0 = ld_k(pka, pkb, 4 * KVT), v0 = ld_v(pv, 4 * KVT), k1 = ld_k(pka, pkb, 5 * KVT));
                PRIO_B;
                K2_PAIR(4, k0 = ld_k(pka, pkb, 6 * KVT), v0 = ld_v(pv, 6 * KVT), k1 = ld_k(pka, pkb, 7 * KVT));
#define TURN() do { asm volatile("s_cmp_eq_u32 %0, 3\n\ts_cbranch_scc1 1f\n\ts_waitcnt vmcnt(0) lgkmcnt(0)\n\ts_barrier\n1:" :: "s"(c) : "memory", "scc"); \
                    pka += dnext; pkb += dnext; pv += dnext; pv2 += dnext; k0 = ld_k(pka, pkb, 0); PRIO_C; } while (0)
                K2_PAIR(6, TURN(), v0 = ld_v(pv, 0), k1 = ld_k(pka, pkb, KVT));
            }
            ob = SMF(v1.v1, HI8(sB), ob);
            PRIO(0);
            {
                lds_ptr d = (lds_ptr)lds + L_X + (own ^ 1) * 3072 + l16;
#pragma unroll
                for (int a = 0; a < 3; ++a)
                    *(AS3(f32x4)*)(d + a * 1024) = kh ? f32x4{oa[4 * a], oa[4 * a + 1], oa[4 * a + 2], oa[4 * a + 3]} : f32x4{ob[4 * a], ob[4 * a + 1], ob[4 * a + 2], ob[4 * a + 3]};
            }
            RAW_BAR();
            float o[12];
#pragma unroll
            for (int a = 0; a < 3; ++a) {
                const f32x4 t = *(const AS3(f32x4)*)(L + L_X + own * 3072 + l16 + a * 1024);
#pragma unroll
                for (int r = 0; r < 4; ++r) o[4 * a + r] = t[r] + (kh ? ob[4 * a + r] : oa[4 * a + r]);
            }
            auto rr = __builtin_amdgcn_permlane32_swap(__float_as_uint(o[8]), __float_as_uint(o[8]), false, false);
            const float lsum = __uint_as_float(rr[1]);
            const float sa = *(const AS3(float)*)(L + L_GATE + (own * 32 + r32) * 4) * __builtin_amdgcn_rcpf(lsum);
            const unsigned ocb = WS_OC + ((qt * 8 + own) * 32 + r32) * 16;
            ST128((u32x4{cvtpk(o[0] * sa, o[1] * sa), cvtpk(o[2] * sa, o[3] * sa), cvtpk(o[4] * sa, o[5] * sa), cvtpk(o[6] * sa, o[7] * sa)}), ocb + (2 * h + hh) * (SEQ * 16));
            if (!hh) ST64((u32x2{cvtpk(o[8] * sa, o[9] * sa), cvtpk(o[10] * sa, o[11] * sa)}), ocb + (64 + (h >> 1)) * (SEQ * 16) + (h & 1) * 8);
            if (attempt == 0 && __any(!(lsum >= 5.4210109e-20f)))
                *(AS3(unsigned)*)((lds_ptr)lds + L_FLAG) = 1u;
            WAIT_VM(0);
            RAW_BAR();
            const unsigned redo = *(const AS3(unsigned)*)(L + L_FLAG);
            if (__builtin_amdgcn_readfirstlane(redo) == 0u) break;
            float ma = -__builtin_inff(), mb = -__builtin_inff();
            for (int t = 0; t < 64; ++t) {
                const u32x4 g0 = LD128(WS_KV + (h * 64 + t) * KVT + ka);
                const u32x2 g1 = LD64(WS_KV + (h * 64 + t) * KVT + kb2);
                f32x16 s = MFMA32(__builtin_bit_cast(bf16x8, g0), QA0, f32x16{}); s = MFMA32K8(__builtin_bit_cast(s16x4, g1), __builtin_bit_cast(s16x4, qa1), s);
                f32x16 u = MFMA32(__builtin_bit_cast(bf16x8, g0), QB0, f32x16{}); u = MFMA32K8(__builtin_bit_cast(s16x4, g1), __builtin_bit_cast(s16x4, qb1), u);
#pragma unroll
                for (int r = 0; r < 16; ++r) { ma = fmaxf(ma, s[r]); mb = fmaxf(mb, u[r]); }
            }
            nba = -xhalf_max(ma); nbb = -xhalf_max(mb);
            RAW_BAR();
            BAR();
        }
        {
            const int mt = wid & 3, kp = wid >> 2;
            const unsigned ofo = WS_OC + ((4 * kp + g) * SEQ + tt * 64 + 16 * mt + l15) * 16;
            const lds_cptr wa = L + g * 1280 + (20 * q4 + p4) * 16;
            f32x4 acc[5];
#pragma unroll
            for (int i = 0; i < 5; ++i) acc[i] = f32x4{0.f, 0.f, 0.f, 0.f};
            u32x4 of[10];
#define P3_MMA(br) do { if (kp) { _Pragma("unroll") for (int k = 0; k < 10; ++k) _Pragma("unroll") for (int i = 0; i < 5; ++i) \
                                      acc[i] = MFMA16(LDS_B128(wa + p3_off(br, 2 * k + 1) + i * 64), __builtin_bit_cast(bf16x8, of[k]), acc[i]); } \
                        else { _Pragma("unroll") for (int k = 0; k < 10; ++k) _Pragma("unroll") for (int i = 0; i < 5; ++i) \
                                      acc[i] = MFMA16(LDS_B128(wa + p3_off(br, 2 * k) + i * 64), __builtin_bit_cast(bf16x8, of[k]), acc[i]); } } while (0)
#pragma unroll
            for (int k = 0; k < 10; ++k) of[k] = LD128(ofo + (80 + 8 * k) * (SEQ * 16));
            BAR();
            P3_MMA(1);
            RAW_BAR();
            for (int ks = 7 + wid; ks < 20; ks += 8) P3_WKS(0, ks, ks < 17 ? L_RA + (ks - 7) * 5120 : L_RB + (ks - 17) * 5120);
            BAR();
#pragma unroll
            for (int k = 0; k < 10; ++k) of[k] = LD128(ofo + (8 * k) * (SEQ * 16));
            WAIT_VM(10);
            BAR();
            P3_MMA(0);
            if (kp) {
#pragma unroll
                for (int i = 0; i < 5; ++i) *(AS3(f32x4)*)((lds_ptr)lds + L_CX + (mt * 5 + i) * 1024 + l16) = acc[i];
            }
            RAW_BAR();
            if (!kp) {
                char* dst = (char*)out + (size_t)(tt * 64 + 16 * mt + l15) * 1280 + ct * 160 + g * 40;
#pragma unroll
                for (int i = 0; i < 5; ++i) {
                    const f32x4 v = acc[i] + *(const AS3(f32x4)*)(L + L_CX + (mt * 5 + i) * 1024 + l16);
                    *(u32x2*)(dst + i * 8) = u32x2{cvtpk(v[0], v[1]), cvtpk(v[2], v[3])};
                }
            }
        }
    }
    if (bid == 0 && tid == 512) publish(ws, WS_EPOCH, tag);
}

extern "C" void kernel_launch(void* const* d_in, const int* in_sizes, int n_in, void* d_out, int out_size, void* d_ws, size_t ws_size,
                              hipStream_t stream) {
    fused_kernel<<<256, 768, 0, stream>>>((const uint16_t*)d_in[0], (const uint16_t*)d_in[1], (const uint16_t*)d_in[4], (const uint16_t*)d_in[2],
                                          (const uint16_t*)d_in[6], (const uint16_t*)d_in[5], (const uint16_t*)d_in[7], (char*)d_ws, (uint16_t*)d_out);
}
```

```cpp
#include <hip/hip_runtime.h>
#include <hip/hip_bf16.h>
#include <stdint.h>

typedef __attribute__((ext_vector_type(8))) __bf16 bf16x8;
typedef __attribute__((ext_vector_type(16))) __bf16 bf16x16;
typedef __attribute__((ext_vector_type(4))) short s16x4;
typedef __attribute__((ext_vector_type(8))) short s16x8;
typedef __attribute__((ext_vector_type(4))) float f32x4;
typedef __attribute__((ext_vector_type(16))) float f32x16;
typedef __attribute__((ext_vector_type(4))) unsigned u32x4;
typedef __attribute__((ext_vector_type(2))) unsigned u32x2;
typedef __attribute__((ext_vector_type(2))) __bf16 bf16x2;
typedef __attribute__((ext_vector_type(2))) float f32x2;
typedef __attribute__((address_space(3))) const char* lds_cptr;
typedef __attribute__((address_space(3))) char* lds_ptr;
typedef unsigned long long u64;
#define AS3(T) __attribute__((address_space(3))) T

constexpr unsigned CODE_BYTES = 30000;
constexpr int SEQ = 2048, NH = 32;
constexpr unsigned OOB = 0x7ffffff0u;
constexpr float LOG2E = 1.4426950408889634f;
constexpr float QSCALE = 0.22360679774997896f * 1.4426950408889634f;

constexpr int KVT = 3072;
constexpr int QFT = 1280;
constexpr unsigned WS_KV = 0;
constexpr unsigned WS_QA = WS_KV + NH * 64 * KVT;
constexpr unsigned WS_PHQ = WS_QA + NH * 64 * QFT;
constexpr unsigned WS_WOP = WS_PHQ + NH * 64 * 2560;
constexpr unsigned WS_OC = WS_WOP + 160 * 640 * 16;
constexpr unsigned WS_KMAX = WS_OC + 160 * SEQ * 16;
constexpr unsigned WS_G = WS_KMAX + NH * 64 * 4;
constexpr unsigned WS_KVP = WS_G + SEQ * 4;
constexpr unsigned WS_FT = WS_KVP + NH * 16 * 3840;
constexpr unsigned WS_FH = WS_FT + 2048;
constexpr unsigned WS_FOB = WS_FH + 16384;
constexpr unsigned WS_FOA = WS_FOB + 2048;
constexpr unsigned WS_EPOCH = WS_FOA + 2048;
constexpr unsigned WS_END = WS_EPOCH + 128;

__device__ __forceinline__ unsigned cvtpk(float lo, float hi) {
    return __builtin_bit_cast(unsigned, __builtin_convertvector(f32x2{lo, hi}, bf16x2)); }
__device__ __forceinline__ float bflo(unsigned w) { return __uint_as_float(w << 16); }
__device__ __forceinline__ float bfhi(unsigned w) { return __uint_as_float(w & 0xffff0000u); }
#define GL1(P) "global_load_lds_dwordx4 %1, %2" P "\n\t"
#define GL2(P) GL1(P) "global_load_lds_dwordx4 %1, %2 offset:1024" P "\n\t"
#define GL3(P) GL2(P) "global_load_lds_dwordx4 %1, %2 offset:2048" P "\n\t"
#define GL4(P) GL3(P) "global_load_lds_dwordx4 %1, %2 offset:3072" P "\n\t"
#define GLASM(BODY) asm volatile("s_mov_b32 %0, m0\n\ts_add_u32 m0, %3, %4\n\ts_nop 0\n\t" BODY "s_mov_b32 m0, %0" \
                     : "=&s"(sv) : "v"(voff), "s"(t), "s"(lbase), "i"(LOFF) : "memory", "scc")
template <int N, int GOFF, int LOFF, bool SC1>
__device__ __forceinline__ void glds_run(const void* sbase, unsigned voff, unsigned lbase) {
    static_assert(N >= 1 && N <= 4, "");
    unsigned sv, tl, th;
    lbase = __builtin_amdgcn_readfirstlane(lbase);
    const unsigned lo = __builtin_amdgcn_readfirstlane((unsigned)(uintptr_t)sbase), hi = __builtin_amdgcn_readfirstlane((unsigned)((uintptr_t)sbase >> 32));
    asm volatile("s_add_u32 %0, %2, %4\n\ts_addc_u32 %1, %3, 0" : "=&s"(tl), "=s"(th) : "s"(lo), "s"(hi), "i"(GOFF) : "scc");
    const uint64_t t = ((uint64_t)th << 32) | tl;
    if constexpr (SC1) {
        if constexpr (N == 1) GLASM(GL1(" sc1")); else if constexpr (N == 2) GLASM(GL2(" sc1")); else if constexpr (N == 3) GLASM(GL3(" sc1")); else GLASM(GL4(" sc1"));
    } else {
        if constexpr (N == 1) GLASM(GL1("")); else if constexpr (N == 2) GLASM(GL2("")); else if constexpr (N == 3) GLASM(GL3("")); else GLASM(GL4(""));
    }
}
template <int LOFF>
__device__ __forceinline__ void glds_ptr(const void* p, unsigned lbase) {
    unsigned sv; lbase = __builtin_amdgcn_readfirstlane(lbase);
    asm volatile("s_mov_b32 %0, m0\n\ts_add_u32 m0, %2, %3\n\ts_nop 0\n\tglobal_load_lds_dwordx4 %1, off\n\ts_mov_b32 m0, %0"
                 : "=&s"(sv) : "v"(p), "s"(lbase), "i"(LOFF) : "memory", "scc"); }
__device__ __forceinline__ void touch(const void* p, unsigned lds_scratch) {
    unsigned sv;
    asm volatile("s_mov_b32 %0, m0\n\ts_mov_b32 m0, %2\n\ts_nop 0\n\tglobal_load_lds_dword %1, off sc1\n\ts_mov_b32 m0, %0" : "=&s"(sv) : "v"(p), "s"(lds_scratch) : "memory"); }
__device__ __forceinline__ s16x4 tr_read(lds_cptr p) { return __builtin_amdgcn_ds_read_tr16_b64_v4i16((AS3(s16x4)*)p); }
__device__ __forceinline__ bf16x8 cat8(s16x4 a, s16x4 b) {
    s16x8 r = {a[0], a[1], a[2], a[3], b[0], b[1], b[2], b[3]}; return __builtin_bit_cast(bf16x8, r); }
#define LDS_B128(p) (*(const AS3(bf16x8)*)(p))
#define LDS_B64(p) (*(const AS3(s16x4)*)(p))
#define MFMA16(a, b, c) __builtin_amdgcn_mfma_f32_16x16x32_bf16(a, b, c, 0, 0, 0)
#define MFMA32(a, b, c) __builtin_amdgcn_mfma_f32_32x32x16_bf16(a, b, c, 0, 0, 0)
#define MFMA32K8(a, b, c) __builtin_amdgcn_mfma_f32_32x32x8bf16_1k(a, b, c, 0, 0, 0)
#define WAIT_VM_(n) asm volatile("s_waitcnt vmcnt(" #n ")" ::: "memory")
#define WAIT_VM(n) WAIT_VM_(n)
#define RAW_BAR() asm volatile("s_waitcnt lgkmcnt(0)\n\ts_barrier" ::: "memory")
#define SBAR() __builtin_amdgcn_sched_barrier(0)
#define EXP2(v) __builtin_amdgcn_exp2f(v)
#define SMF(a, lo, acc) __builtin_amdgcn_smfmac_f32_32x32x32_bf16(a, __builtin_bit_cast(bf16x16, lo), acc, 0xDDDD, 0, 0)
#define LO8(s) __builtin_shufflevector(s, s, 0, 1, 2, 3, 4, 5, 6, 7)
#define HI8(s) __builtin_shufflevector(s, s, 8, 9, 10, 11, 12, 13, 14, 15)
#define ST128(v, off) __builtin_amdgcn_raw_buffer_store_b128(v, wsr, off, 0, 16)
#define ST64(v, off) __builtin_amdgcn_raw_buffer_store_b64(v, wsr, off, 0, 16)
#define ST32(v, off) __builtin_amdgcn_raw_buffer_store_b32(v, wsr, off, 0, 16)
__device__ __forceinline__ float xhalf_max(float v) {
    auto r = __builtin_amdgcn_permlane32_swap(__float_as_uint(v), __float_as_uint(v), false, false);
    return fmaxf(__uint_as_float(r[0]), __uint_as_float(r[1])); }
__device__ __forceinline__ float xhalf_min(float v) {
    auto r = __builtin_amdgcn_permlane32_swap(__float_as_uint(v), __float_as_uint(v), false, false);
    return fminf(__uint_as_float(r[0]), __uint_as_float(r[1])); }
__device__ __forceinline__ float xhalf_sum(float v) {
    auto r = __builtin_amdgcn_permlane32_swap(__float_as_uint(v), __float_as_uint(v), false, false);
    return __uint_as_float(r[0]) + __uint_as_float(r[1]); }
__device__ __forceinline__ float row_max16(float v) {
    v = fmaxf(v, __int_as_float(__builtin_amdgcn_update_dpp(0, __float_as_int(v), 0x128, 0xf, 0xf, false)));
    v = fmaxf(v, __int_as_float(__builtin_amdgcn_update_dpp(0, __float_as_int(v), 0x124, 0xf, 0xf, false)));
    v = fmaxf(v, __int_as_float(__builtin_amdgcn_update_dpp(0, __float_as_int(v), 0x122, 0xf, 0xf, false)));
    v = fmaxf(v, __int_as_float(__builtin_amdgcn_update_dpp(0, __float_as_int(v), 0x121, 0xf, 0xf, false)));
    return v; }
__device__ __forceinline__ void publish(char* ws, unsigned off, u64 tag) {
    __hip_atomic_store((u64*)(ws + off), tag, __ATOMIC_RELAXED, __HIP_MEMORY_SCOPE_AGENT); }
__device__ __forceinline__ void wait_flags(const char* ws, unsigned off, u64 tag) {
    for (int spin = 0; spin < (1 << 20); ++spin) {
        const u64 f = __hip_atomic_load((const u64*)(ws + off), __ATOMIC_RELAXED, __HIP_MEMORY_SCOPE_AGENT);
        if (__all(f == tag)) break;
        __builtin_amdgcn_s_sleep(2);
    }
    asm volatile("" ::: "memory");
}

struct KFrag { bf16x8 k0; s16x4 k1; };
struct VFrag { bf16x8 v0, v1; };
__device__ __forceinline__ KFrag ld_k(lds_cptr pa, lds_cptr pb, int off) { KFrag k; k.k0 = LDS_B128(pa + off); k.k1 = LDS_B64(pb + off); return k; }
__device__ __forceinline__ VFrag ld_v2(lds_cptr p, lds_cptr q, int off) {
    VFrag v; v.v0 = cat8(tr_read(p + off), tr_read(q + off)); v.v1 = cat8(tr_read(p + off + 256), tr_read(q + off + 256)); return v; }
#define ld_v(p, off) ld_v2(p, p##2, off)
__device__ __forceinline__ void attn_step(f32x16& sc, f32x16& sn, const KFrag& kn, const bf16x8& vprev1, const bf16x8& vc0,
                                          const bf16x8& qn0, const s16x4& qn1, f32x16& oprev, f32x16& oc) {
    oprev = SMF(vprev1, HI8(sn), oprev); SBAR();
#pragma unroll
    for (int r = 0; r < 4; ++r) sc[r] = EXP2(sc[r]);
    SBAR(); sn = MFMA32(kn.k0, qn0, f32x16{}); SBAR();
#pragma unroll
    for (int r = 4; r < 8; ++r) sc[r] = EXP2(sc[r]);
    SBAR(); sn = MFMA32K8(kn.k1, qn1, sn); SBAR();
#pragma unroll
    for (int r = 8; r < 12; ++r) sc[r] = EXP2(sc[r]);
    SBAR(); oc = SMF(vc0, LO8(sc), oc); SBAR();
#pragma unroll
    for (int r = 12; r < 16; ++r) sc[r] = EXP2(sc[r]);
    SBAR();
}
__device__ __forceinline__ void feature_map(const u32x4 wp0, const u32x2 wp1, const u32x4 f0, const u32x2 f1, unsigned (&ph)[10]) {
    f32x16 pq = MFMA32(__builtin_bit_cast(bf16x8, wp0), __builtin_bit_cast(bf16x8, f0), f32x16{});
    pq = MFMA32K8(__builtin_bit_cast(s16x4, wp1), __builtin_bit_cast(s16x4, f1), pq);
    float mx = fmaxf(pq[0], pq[1]), mn = fminf(pq[0], pq[1]);
#pragma unroll
    for (int u = 2; u < 10; u += 2) { mx = fmaxf(fmaxf(mx, pq[u]), pq[u + 1]); mn = fminf(fminf(mn, pq[u]), pq[u + 1]); }
    mx = xhalf_max(mx); mn = xhalf_min(mn);
    const f32x2 lp = {LOG2E, LOG2E}, cmx = {-mx * LOG2E, -mx * LOG2E}, cmn = {mn * LOG2E, mn * LOG2E};
    f32x2 ep[5], en[5], sp2 = {0.f, 0.f}, sn2 = {0.f, 0.f};
#pragma unroll
    for (int u = 0; u < 5; ++u) {
        const f32x2 p = {pq[2 * u], pq[2 * u + 1]};
        const f32x2 a = __builtin_elementwise_fma(p, lp, cmx), b = __builtin_elementwise_fma(p, -lp, cmn);
        ep[u] = f32x2{EXP2(a[0]), EXP2(a[1])}; en[u] = f32x2{EXP2(b[0]), EXP2(b[1])};
        sp2 += ep[u]; sn2 += en[u];
    }
    const float sp = __builtin_amdgcn_rcpf(xhalf_sum(sp2[0] + sp2[1])), sn = __builtin_amdgcn_rcpf(xhalf_sum(sn2[0] + sn2[1]));
#pragma unroll
    for (int u = 0; u < 5; ++u) {
        const f32x2 a = ep[u] * sp, b = en[u] * sn;
        ph[u] = cvtpk(a[0], a[1]); ph[5 + u] = cvtpk(b[0], b[1]);
    }
}
__device__ __forceinline__ u32x2 split3(float v) {
    const unsigned hi = cvtpk(v, 0.f) & 0xffffu; const float r1 = v - bflo(hi);
    const unsigned mid = cvtpk(r1, 0.f) & 0xffffu; const float r2 = r1 - bflo(mid);
    return u32x2{hi | (mid << 16), cvtpk(r2, 0.f) & 0xffffu};
}
__device__ __forceinline__ float qnorm2(const u32x4& a, const u32x2& b) {
    float n2 = 0.f;
#pragma unroll
    for (int j = 0; j < 4; ++j) { n2 = fmaf(bflo(a[j]), bflo(a[j]), n2); n2 = fmaf(bfhi(a[j]), bfhi(a[j]), n2); }
#pragma unroll
    for (int j = 0; j < 2; ++j) { n2 = fmaf(bflo(b[j]), bflo(b[j]), n2); n2 = fmaf(bfhi(b[j]), bfhi(b[j]), n2); }
    return xhalf_sum(n2);
}

#define LD128(off) __builtin_amdgcn_raw_buffer_load_b128(wsr, off, 0, 16)
#define LD64(off) __builtin_amdgcn_raw_buffer_load_b64(wsr, off, 0, 16)
#define LD32(off) __builtin_amdgcn_raw_buffer_load_b32(wsr, off, 0, 16)
#define BAR() asm volatile("s_barrier" ::: "memory")

__device__ __forceinline__ const char* w_source(int p, int lane, const char* g0, const char* g1, const char* g2) {
    int q = (p & 7) * 64 + lane; const int up = q >= 248; q -= up ? 248 : 0; q = q < 239 ? q : 239;
    const int row = __umul24(q, 2185) >> 16, ch = q - row * 30, sgm = __umul24(ch, 6554) >> 16;
    const uintptr_t b = sgm == 0 ? (uintptr_t)g0 : sgm == 1 ? (uintptr_t)g1 : (uintptr_t)g2;
    return (const char*)(b + (__umul24(16 * (p >> 3) + 8 * up + row, 3840) + (ch - 10 * sgm) * 16));
}

constexpr int P1_STAGE = 49152;
constexpr int L_WPH = 3 * P1_STAGE;
constexpr int L_GW = L_WPH + 3200;
constexpr int L_WO = L_GW + 1280;
constexpr int L_SCR = L_WO + 10240;
constexpr int L_FLAG = L_SCR + 256;
constexpr int LDS_BYTES = L_FLAG + 16;
constexpr int L_IMG = P1_STAGE;
constexpr int L_PH = L_IMG + 32 * 1536;
constexpr int L_Q = L_PH + 40960 + 256;
constexpr int SLOT = 8 * KVT;
constexpr int L_X = 0;
constexpr int L_MISC = L_WO;
constexpr int L_GATE = L_MISC + 3072;
constexpr int L_RA = 4 * SLOT, L_RB = SLOT, L_RC = 2 * SLOT, L_RD = 0, L_RE = L_RC + 6 * 5120;
constexpr int L_CX = L_RC;
static_assert(L_Q + 10240 + 256 <= L_WO && L_RA + 10 * 5120 <= L_WO && L_RE + 3 * 5120 <= L_RA && LDS_BYTES <= 163840, "");
__device__ __forceinline__ constexpr int p3_off(int br, int ks) {
    return br ? (ks < 10 ? L_RA + ks * 5120 : ks < 14 ? L_RB + (ks - 10) * 5120 : L_RC + (ks - 14) * 5120)
              : (ks < 4 ? L_RD + ks * 5120 : ks < 7 ? L_RE + (ks - 4) * 5120 : ks < 17 ? L_RA + (ks - 7) * 5120 : L_RB + (ks - 17) * 5120); }

__global__ __launch_bounds__(768, 3) void fused_kernel(const uint16_t* __restrict__ x, const uint16_t* __restrict__ wA, const uint16_t* __restrict__ wB,
                                                    const uint16_t* __restrict__ woA, const uint16_t* __restrict__ woB,
                                                    const uint16_t* __restrict__ wphi, const uint16_t* __restrict__ gatew,
                                                    char* __restrict__ ws, uint16_t* __restrict__ out) {
    __shared__ __attribute__((aligned(16))) char lds[LDS_BYTES];
    uint64_t pc0; asm volatile("s_getpc_b64 %0" : "=s"(pc0));
    const int bid = blockIdx.x, xcd = bid & 7, bi = bid >> 3;
    const int tid = threadIdx.x, lane = tid & 63;
    const int wid = __builtin_amdgcn_readfirstlane(tid >> 6);
    const unsigned lds0 = (unsigned)(uintptr_t)lds;
    const lds_cptr L = (lds_cptr)lds;
    const unsigned l16 = lane * 16;
    const int g = lane >> 4, l15 = lane & 15, q4 = l15 >> 2, p4 = lane & 3, r32 = lane & 31, hh = lane >> 5;
    const auto wsr = __builtin_amdgcn_make_buffer_rsrc(ws, 0, WS_END, 0x00020000);
    const int rt = (xcd & 1) * 8 + (bi & 7), kd = bi >> 4, hj = (xcd >> 1) * 2 + ((bi >> 3) & 1);
    const int h = xcd * 4 + (bi >> 3), qt = bi & 7;
    const int tt = xcd * 4 + (bi & 3), ct = bi >> 2;
    const char* xb = (const char*)x + (size_t)rt * 128 * 1280;
    const unsigned ka = lane * 16, kb = 1024 + r32 * 8;
    const char* wg0 = (const char*)wA + hj * 160 + (kd ? 2560 : 0);
    const char* wg1 = kd ? (const char*)wB + hj * 160 + 1280 : (const char*)wA + hj * 160 + 1280;
    const char* wg2 = (const char*)wB + hj * 160 + (kd ? 2560 : 0);
#define wsrc(p) w_source(p, lane, wg0, wg1, wg2)
#define P3_WKS(br, ks, dst) do { _Pragma("unroll") for (int j_ = 0; j_ < 5; ++j_) { const int q_ = j_ * 64 + lane, c_ = __umul24(q_, 52429) >> 22, n_ = q_ - c_ * 80; \
        glds_run<1, 0, 0, true>(ws + WS_WOP + ct * 1280 + ((br) * 80 + 4 * (ks)) * 10240, c_ * 10240 + n_ * 16, lds0 + (dst) + j_ * 1024); } } while (0)
    u64 tag = 0;
#define MAKE_TAG() do { u64 z = ((u64)(unsigned)__builtin_amdgcn_readfirstlane((unsigned)(tag >> 32)) << 32) | (unsigned)__builtin_amdgcn_readfirstlane((unsigned)tag); \
        z = z * 0x9E3779B97F4A7C15ull + 0x632BE59BD9B4E019ull; z ^= z >> 29; z *= 0xBF58476D1CE4E5B9ull; z ^= z >> 32; tag = z; } while (0)

    if (wid >= 8) {
        const int hw = wid - 8;
        __builtin_amdgcn_s_setprio(2);
        unsigned hbt = 0;
        if (tid == 512) *(AS3(unsigned)*)((lds_ptr)lds + L_FLAG + 4) = 0u;
#define HBAR() do { hbt += 4; asm volatile("s_waitcnt lgkmcnt(0)" ::: "memory"); \
        if (lane == 0) __hip_atomic_fetch_add((AS3(unsigned)*)((lds_ptr)lds + L_FLAG + 4), 1u, __ATOMIC_RELAXED, __HIP_MEMORY_SCOPE_WORKGROUP); \
        while (__builtin_amdgcn_readfirstlane(__hip_atomic_load((AS3(unsigned)*)((lds_ptr)lds + L_FLAG + 4), __ATOMIC_RELAXED, __HIP_MEMORY_SCOPE_WORKGROUP)) < hbt) __builtin_amdgcn_s_sleep(1); \
        asm volatile("" ::: "memory"); } while (0)
        const char* pw[4];
#pragma unroll
        for (int j = 0; j < 4; ++j) pw[j] = wsrc(16 + 4 * hw + j);
        auto wstage = [&](int slot) {
            const unsigned lw = lds0 + slot * P1_STAGE + 32768 + hw * 4096;
            glds_ptr<0>(pw[0], lw); glds_ptr<1024>(pw[1], lw); glds_ptr<2048>(pw[2], lw); glds_ptr<3072>(pw[3], lw);
#pragma unroll
            for (int j = 0; j < 4; ++j) pw[j] += 245760;
        };
        if (hj == 0 && kd == 0 && hw < 2 && hw * 1024 + l16 < 1280) glds_run<1, 0, L_GW, false>(gatew, hw * 1024 + l16, lds0 + hw * 1024);
        wstage(0);
        RAW_BAR();
        wstage(1);
        WAIT_VM(4);
        BAR();
        wstage(2);
        const char* tp = hw == 0 ? xb + ((bi >> 3) * 32 + r32) * 1280 + 3 * 128
                       : hw == 1 ? ((lane % 6) < 2 ? wg0 : (lane % 6) < 4 ? wg1 : wg2) + ((bi & 7) * 8 + (lane < 48 ? lane / 6 : 0) + 192) * 3840 + (lane & 1) * 156 : xb;
        const unsigned tstep = hw == 0 ? 128u : hw == 1 ? 245760u : 0u;
        touch(tp, lds0 + L_SCR); tp += tstep; touch(tp, lds0 + L_SCR); tp += tstep;
        for (int s = 0; s < 9; ++s) {
            if (s == 0) { WAIT_VM(6); } else if (s == 1) { WAIT_VM(7); } else if (s < 7) { WAIT_VM(6); } else if (s == 7) { WAIT_VM(10); } else { WAIT_VM(0); }
            BAR();
            if (s < 7) wstage(s % 3);
            if (s < 5) { touch(tp, lds0 + L_SCR); tp += tstep; }
            if (s == 5) { const unsigned o = (hw * 64 + lane) * 128; touch((const char*)pc0 + (o < CODE_BYTES ? o : 0u), lds0 + L_SCR); }
            if (s == 6) {
                const int cb = bid < 160 ? bid : 0, ch = cb < 80 ? cb : cb - 80;
                const int r0 = (ch < 64 ? (ch >> 1) * 20 + (ch & 1) * 4 : (ch - 64) * 40 + 16) + hw, dr = (ch < 64 ? 8 : 20) * 1280;
                const char* pr = (const char*)(cb < 80 ? woA : woB) + r0 * 1280; const unsigned lr = lds0 + hw * 1280;
                glds_run<1, 0, L_WO, false>(pr, l16, lr); glds_run<1, 0, L_WO + 5120, false>(pr + dr, l16, lr);
                if (lane < 16) { glds_run<1, 1024, L_WO + 1024, false>(pr, l16, lr); glds_run<1, 1024, L_WO + 6144, false>(pr + dr, l16, lr); }
                if (hw * 1024 + l16 < 3200) glds_run<1, 0, L_WPH, false>((const char*)wphi + hj * 3200, hw * 1024 + l16, lds0 + hw * 1024);
            }
            if (s == 7) tag = __hip_atomic_load((const u64*)(ws + WS_EPOCH), __ATOMIC_RELAXED, __HIP_MEMORY_SCOPE_AGENT);
            if (s == 8 && bid < 160) {
                const lds_cptr r = L + L_WO + q4 * 1280 + g * 32 + p4 * 8 + hw * 128;
#pragma unroll
                for (int j = 0; j < 3; ++j)
                    if (j < 2 || hw < 2) ST128(__builtin_bit_cast(u32x4, cat8(tr_read(r + j * 512), tr_read(r + j * 512 + 5120))), WS_WOP + (bid * 640 + (j * 4 + hw) * 64 + lane) * 16);
            }
        }
        MAKE_TAG();
        WAIT_VM(0);
        BAR();
        BAR();
        const unsigned fhoff = WS_FH + (((4 * hj + (lane & 3)) * 4 + kd) * 16 + rt) * 8;
        if (hw == 0 && lane < 5) publish(ws, lane < 4 ? fhoff : WS_FT + bid * 8, tag);
        if (kd) BAR();
        const char* kvbase = ws + WS_KV + (size_t)h * 64 * KVT;
        const char* kvw = kvbase + (size_t)(hw >> 1) * 4 * SLOT + (hw & 1) * 12288; const unsigned ldw = lds0 + (hw >> 1) * SLOT + (hw & 1) * 12288;
#define H_CHUNK(j) do { glds_run<4, (j) * SLOT, ((j) & 1) * 2 * SLOT, true>(kvw, l16, ldw); glds_run<4, (j) * SLOT + 4096, ((j) & 1) * 2 * SLOT + 4096, true>(kvw, l16, ldw); \
                        glds_run<4, (j) * SLOT + 8192, ((j) & 1) * 2 * SLOT + 8192, true>(kvw, l16, ldw); } while (0)
        if (hw == 0) wait_flags(ws, WS_FH + (h * 64 + r32) * 8, tag);
        BAR();
        {
            const char* pq = ws + WS_QA + ((size_t)h * 64 + qt * 8) * QFT;
            if (hw < 2) glds_run<3, 0, L_Q, true>(pq + hw * 3072, l16, lds0 + hw * 3072); else glds_run<2, 0, L_Q, true>(pq + 2048 + hw * 2048, l16, lds0 + 2048 + hw * 2048);
            if (hw == 3 && lane < 16) glds_run<1, 0, L_Q + 10240, true>(ws + WS_KMAX + h * 256, l16, lds0);
        }
        WAIT_VM(0);
        BAR();
        {
            if (kd) { if (hw == 0 && lane < 4) publish(ws, fhoff + 256, tag); }
            else {
#pragma unroll
                for (int j = 0; j < 10; ++j) ST128(*(const AS3(u32x4)*)(L + L_PH + hw * 10240 + j * 1024 + l16), WS_PHQ + ((4 * hj + hw) * 64 + rt * 4) * 2560 + j * 1024 + l16);
                WAIT_VM(0);
                if (lane == hw) publish(ws, fhoff + 256, tag);
            }
            H_CHUNK(1);
            wait_flags(ws, WS_FT + (64 * hw + lane) * 8, tag);
            WAIT_VM(0);
            BAR();
            {
                wait_flags(ws, WS_FH + (lane < 32 ? h * 64 + 32 + lane : 2 * qt + (lane & 1)) * 8, tag);
                H_CHUNK(2);
                const int e = 64 * hw + lane;
                f32x4 ks = {0.f, 0.f, 0.f, 0.f};
#pragma unroll
                for (int j = 0; j < 16; ++j) ks += __builtin_bit_cast(f32x4, LD128(WS_KVP + (h * 16 + j) * 3840 + (e < 240 ? e : 0) * 16));
                u32x4 pf[2][2]; u32x2 pg[2]; float gt[2];
#pragma unroll
                for (int t = 0; t < 2; ++t) {
                    const unsigned o = WS_PHQ + (h * 64 + qt * 8 + 2 * hw + t) * 2560;
                    pf[t][0] = LD128(o + l16); pf[t][1] = LD128(o + 1024 + l16); pg[t] = LD64(o + 2048 + lane * 8);
                    gt[t] = __uint_as_float(LD32(WS_G + (qt * 256 + (2 * hw + t) * 32 + r32) * 4));
                }
                if (e < 240) {
                    const int ph = e / 6, d = 4 * (e - ph * 6), ph_h = ph / 20, u = ph - ph_h * 20;
                    const unsigned w0 = cvtpk(ks[0], ks[1]), w1 = cvtpk(ks[2], ks[3]);
                    lds_ptr dd = (lds_ptr)lds + L_MISC + d * 96 + ph_h * 48 + u * 2;
                    *(AS3(uint16_t)*)(dd) = (uint16_t)(w0 & 0xffffu); *(AS3(uint16_t)*)(dd + 96) = (uint16_t)(w0 >> 16);
                    *(AS3(uint16_t)*)(dd + 192) = (uint16_t)(w1 & 0xffffu); *(AS3(uint16_t)*)(dd + 288) = (uint16_t)(w1 >> 16);
                }
                *(AS3(float)*)((lds_ptr)lds + L_GATE + (64 * hw + lane) * 4) = hh ? gt[1] : gt[0];
                WAIT_VM(0);
                RAW_BAR();
                H_CHUNK(3);
                const lds_cptr a = L + L_MISC + r32 * 96 + hh * 48;
                const bf16x8 a0 = LDS_B128(a), a1 = LDS_B128(a + 16); const s16x4 a2 = LDS_B64(a + 32);
#pragma unroll
                for (int t = 0; t < 2; ++t) {
                    f32x16 lo = MFMA32(a0, __builtin_bit_cast(bf16x8, pf[t][0]), f32x16{});
                    lo = MFMA32(a1, __builtin_bit_cast(bf16x8, pf[t][1]), lo);
                    lo = MFMA32K8(a2, __builtin_bit_cast(s16x4, pg[t]), lo);
                    auto rb = __builtin_amdgcn_permlane32_swap(__float_as_uint(lo[8]), __float_as_uint(lo[8]), false, false);
                    const float sb = (1.0f - gt[t]) / (__uint_as_float(rb[1]) + 1e-6f);
                    const unsigned oc = WS_OC + ((qt * 8 + 2 * hw + t) * 32 + r32) * 16;
                    ST128((u32x4{cvtpk(lo[0] * sb, lo[1] * sb), cvtpk(lo[2] * sb, lo[3] * sb), cvtpk(lo[4] * sb, lo[5] * sb), cvtpk(lo[6] * sb, lo[7] * sb)}), oc + (80 + 2 * h + hh) * (SEQ * 16));
                    if (!hh) ST64((u32x2{cvtpk(lo[8] * sb, lo[9] * sb), cvtpk(lo[10] * sb, lo[11] * sb)}), oc + (80 + 64 + (h >> 1)) * (SEQ * 16) + (h & 1) * 8);
                }
            }
            WAIT_VM(0);
            HBAR();
            if (tid == 512) { publish(ws, WS_FOB + (qt * 32 + h) * 8, tag); *(AS3(unsigned)*)((lds_ptr)lds + L_FLAG) = 0u; }
            for (int ks = hw; ks < 10; ks += 4) P3_WKS(1, ks, L_RA + ks * 5120);
            RAW_BAR();
            P3_WKS(1, 10 + hw, L_RB + hw * 5120);
            { const int ln = hw * 64 + lane; touch(ws + WS_WOP + bi * 25600 + (ln < 200 ? ln : 0) * 128, lds0 + L_SCR); }
            if (hw == 0) wait_flags(ws, WS_FOB + (xcd * 32 + r32) * 8, tag);
            WAIT_VM(0);
            RAW_BAR();
            BAR();
            const unsigned redo = *(const AS3(unsigned)*)(L + L_FLAG);
            if (__builtin_amdgcn_readfirstlane(redo) != 0u) {
                RAW_BAR();
                H_CHUNK(0); WAIT_VM(0); BAR();
                H_CHUNK(1); WAIT_VM(0); BAR();
                H_CHUNK(2); WAIT_VM(0); BAR();
                H_CHUNK(3);
                if (tid == 512) *(AS3(unsigned)*)((lds_ptr)lds + L_FLAG) = 0u;
                WAIT_VM(0); RAW_BAR();
                P3_WKS(1, 10 + hw, L_RB + hw * 5120); WAIT_VM(0);
                BAR(); BAR();
            }
        }
        if (tid == 512) publish(ws, WS_FOA + (qt * 32 + h) * 8, tag);
        for (int ks = 14 + hw; ks < 20; ks += 4) P3_WKS(1, ks, L_RC + (ks - 14) * 5120);
        WAIT_VM(0);
        BAR();
        for (int ks = hw; ks < 7; ks += 4) P3_WKS(0, ks, ks < 4 ? L_RD + ks * 5120 : L_RE + (ks - 4) * 5120);
        BAR();
        if (hw == 3) wait_flags(ws, WS_FOA + (xcd * 32 + r32) * 8, tag);
        WAIT_VM(0);
        BAR();
        BAR();
        BAR();
    } else {
        const int th = wid >> 2, cw = wid & 3;
        {
            unsigned vx[2];
#pragma unroll
            for (int j = 0; j < 2; ++j) { const int row = 8 * (wid * 2 + j) + (lane >> 3); vx[j] = row * 1280 + (((lane ^ row) & 7) << 4); }
            const unsigned lx = lds0 + wid * 2048;
            const char* pw0 = wsrc(2 * wid); const char* pw1 = wsrc(2 * wid + 1);
#define P1_X(s, slot) do { glds_run<1, 0, 0, false>(xb + (s) * 128, vx[0], lx + (slot) * P1_STAGE); glds_run<1, 0, 1024, false>(xb + (s) * 128, vx[1], lx + (slot) * P1_STAGE); \
                           glds_ptr<16384>(pw0, lx + (slot) * P1_STAGE); glds_ptr<17408>(pw1, lx + (slot) * P1_STAGE); pw0 += 245760; pw1 += 245760; } while (0)
            P1_X(0, 0);
            BAR();
            P1_X(1, 1);
            const bool t3 = cw < 3 || (hj == 0 && kd == 0);
            const int xrow = 64 * th + l15;
            lds_cptr xa0 = L + xrow * 128 + ((g ^ (xrow & 7)) << 4), xa1 = L + ((xrow * 128 + ((g ^ (xrow & 7)) << 4)) ^ 64);
            const unsigned wa0 = 16384 + 8192 * (g >> 1) + 480 * (8 * (g & 1) + q4) + 128 * (g & 1) + 40 * p4 + (cw < 3 ? 160 * cw : 32);
            const unsigned wst = cw < 3 ? 8 : 160;
            lds_cptr wa[4];
#pragma unroll
            for (int i = 0; i < 3; ++i) wa[i] = L + wa0 + i * wst;
            wa[3] = cw < 3 ? L + wa0 + 24 : L + L_GW + g * 16;
            f32x4 acc[4][4];
#pragma unroll
            for (int m = 0; m < 4; ++m)
#pragma unroll
                for (int i = 0; i < 4; ++i) acc[m][i] = f32x4{0.f, 0.f, 0.f, 0.f};
            bf16x8 xA[4], wA_[4], xB[4], wB_[4];
#define P1_LOAD(X, W, ks, NT, G3) do { \
        _Pragma("unroll") for (int i = 0; i < 3; ++i) W[i] = cat8(tr_read(wa[i] + (ks) * 16384), tr_read(wa[i] + (ks) * 16384 + 1920)); \
        if (NT == 4) { if (G3) W[3] = LDS_B128(wa[3] + (ks) * 64); else W[3] = cat8(tr_read(wa[3] + (ks) * 16384), tr_read(wa[3] + (ks) * 16384 + 1920)); } \
        _Pragma("unroll") for (int m = 0; m < 4; ++m) X[m] = LDS_B128(((ks) ? xa1 : xa0) + m * 2048); } while (0)
#define P1_HALF(X, W, XN, WN, ks, NT, G3, MID1, MID2) do { \
        _Pragma("unroll") for (int i = 0; i < NT; ++i) { \
            s16x4 lo_, hi_; \
            acc[0][i] = MFMA16(W[i], X[0], acc[0][i]); SBAR(); \
            if (i == 3 && G3) WN[3] = LDS_B128(wa[3] + (ks) * 64); else lo_ = tr_read(wa[i] + (ks) * 16384); SBAR(); \
            acc[1][i] = MFMA16(W[i], X[1], acc[1][i]); SBAR(); \
            if (!(i == 3 && G3)) { hi_ = tr_read(wa[i] + (ks) * 16384 + 1920); WN[i] = cat8(lo_, hi_); } SBAR(); \
            acc[2][i] = MFMA16(W[i], X[2], acc[2][i]); SBAR(); \
            XN[i] = LDS_B128(((ks) ? xa1 : xa0) + i * 2048); SBAR(); \
            acc[3][i] = MFMA16(W[i], X[3], acc[3][i]); SBAR(); \
            if (NT == 3 && i == 2) XN[3] = LDS_B128(((ks) ? xa1 : xa0) + 3 * 2048); \
            if (i == 0) { MID1; } if (i == 1) { MID2; } SBAR(); \
        } } while (0)
#define P1_DX(lim, st, sl) do { unsigned sv_; const char* xs_ = xb + (st) * 128; \
                  asm volatile("s_cmp_gt_u32 %1, " #lim "\n\ts_cbranch_scc1 1f\n\ts_mov_b32 %0, m0\n\ts_mov_b32 m0, %5\n\ts_nop 0\n\tglobal_load_lds_dwordx4 %2, %4\n\t" \
                               "s_add_u32 m0, %5, 1024\n\ts_nop 0\n\tglobal_load_lds_dwordx4 %3, %4\n\ts_mov_b32 m0, %0\n1:" \
                               : "=&s"(sv_) : "s"(s), "v"(vx[0]), "v"(vx[1]), "s"(xs_), "s"(lx + (sl) * P1_STAGE) : "memory", "scc"); } while (0)
#define P1_DW(lim, sl) do { unsigned sv_; \
                  asm volatile("s_cmp_gt_u32 %3, " #lim "\n\ts_cbranch_scc1 1f\n\ts_mov_b32 %0, m0\n\ts_add_u32 m0, %4, 16384\n\ts_nop 0\n\tglobal_load_lds_dwordx4 %1, off\n\t" \
                               "s_add_u32 m0, %4, 17408\n\tv_lshl_add_u64 %1, %1, 0, %5\n\tglobal_load_lds_dwordx4 %2, off\n\ts_mov_b32 m0, %0\n\tv_lshl_add_u64 %2, %2, 0, %5\n1:" \
                               : "=&s"(sv_), "+v"(pw0), "+v"(pw1) : "s"(s), "s"(lx + (sl) * P1_STAGE), "s"(245760ull) : "memory", "scc"); } while (0)
#define P1_LOOP(NT, G3) do { \
            P1_LOAD(xA, wA_, 0, NT, G3); \
            int slot = 0; \
            _Pragma("nounroll") for (int s = 0; s < 10; ++s) { \
                P1_HALF(xA, wA_, xB, wB_, 1, NT, G3, P1_DX(0, 2, 2), P1_DW(0, 2)); \
                  \
                asm volatile("s_cmp_gt_u32 %0, 7\n\ts_cbranch_scc1 1f\n\ts_waitcnt vmcnt(4) lgkmcnt(0)\n\ts_barrier\n\ts_branch 2f\n" \
                             "1:\n\ts_cmp_eq_u32 %0, 9\n\ts_cbranch_scc1 2f\n\ts_waitcnt vmcnt(0) lgkmcnt(0)\n\ts_barrier\n2:" :: "s"(s) : "memory", "scc"); \
                const int d = slot == 2 ? -2 * P1_STAGE : P1_STAGE; \
                xa0 += d; xa1 += d; wa[0] += d; wa[1] += d; wa[2] += d; wa[3] += G3 ? 128 : d; SBAR(); \
                P1_HALF(xB, wB_, xA, wA_, 0, NT, G3, P1_DX(6, s + 3, slot), P1_DW(6, slot)); \
                slot = slot == 2 ? 0 : slot + 1; \
            } } while (0)
            WAIT_VM(4);
            BAR();
            if (cw < 3) P1_LOOP(4, false); else if (t3) P1_LOOP(4, true); else P1_LOOP(3, false);
            const lds_ptr img = (lds_ptr)lds + L_IMG;
            {
                const int ht = (4 * hj + g) * 64 + rt * 4 + 2 * th;
                float kn = 0.f;
                if (cw < 3) {
                    const bool isv = kd ? cw != 1 : false, toA = kd ? cw == 0 : cw < 2;
                    if (kd == 0 && cw == 0) {
#pragma unroll
                        for (int m = 0; m < 4; ++m)
#pragma unroll
                            for (int i = 0; i < 4; ++i) acc[m][i] *= QSCALE;
                    }
                    const unsigned ts = (kd == 0 && cw == 0) ? QFT : KVT, tb = ((kd == 0 && cw == 0) ? WS_QA : WS_KV + (kd ? 1536 : 0)) + ht * ts;
                    const unsigned o0 = l15 * 16, o1 = 512 + (isv ? l15 ^ 4 : l15) * 16;
#pragma unroll
                    for (int m = 0; m < 4; ++m) {
                        const u32x4 w0 = {cvtpk(acc[m][0][0], acc[m][0][1]), cvtpk(acc[m][0][2], acc[m][0][3]), cvtpk(acc[m][1][0], acc[m][1][1]), cvtpk(acc[m][1][2], acc[m][1][3])};
                        const u32x4 w1 = {cvtpk(acc[m][2][0], acc[m][2][1]), cvtpk(acc[m][2][2], acc[m][2][3]), cvtpk(acc[m][3][0], acc[m][3][1]), cvtpk(acc[m][3][2], acc[m][3][3])};
                        if (toA) { const unsigned mo = tb + (m >> 1) * ts + (m & 1) * 256; ST128(w0, mo + o0); ST128(w1, mo + o1); }
                        else { const lds_ptr d = img + (((isv ? 4 : 0) + 2 * th + (m >> 1)) * 4 + g) * 1536 + (m & 1) * 256; *(AS3(u32x4)*)(d + o0) = w0; *(AS3(u32x4)*)(d + o1) = w1; }
                    }
                    if (kd == 0 && cw == 1) {
#pragma unroll
                        for (int m = 0; m < 4; ++m) {
                            float n2 = 0.f;
#pragma unroll
                            for (int i = 0; i < 4; ++i)
#pragma unroll
                                for (int r = 0; r < 4; ++r) n2 = fmaf(acc[m][i][r], acc[m][i][r], n2);
                            kn = (n2 == n2) ? fmaxf(kn, n2) : __builtin_inff();
                        }
                    }
                } else {
#pragma unroll
                    for (int m = 0; m < 4; ++m) {
                        const unsigned ok = WS_KV + (ht + (m >> 1)) * KVT + 1024, o8 = (m & 1) * 128 + l15 * 8, o16 = (m & 1) * 256 + (l15 ^ 8) * 16;
                        const lds_ptr d = img + ((2 * th + (m >> 1)) * 4 + g) * 1536 + 1024;
                        const u32x2 w1 = {cvtpk(acc[m][1][0], acc[m][1][1]), cvtpk(acc[m][1][2], acc[m][1][3])}, w2 = {cvtpk(acc[m][2][0], acc[m][2][1]), cvtpk(acc[m][2][2], acc[m][2][3])};
                        if (kd == 0) {
                            ST64((u32x2{cvtpk(acc[m][0][0] * QSCALE, acc[m][0][1] * QSCALE), cvtpk(acc[m][0][2] * QSCALE, acc[m][0][3] * QSCALE)}), WS_QA + (ht + (m >> 1)) * QFT + 1024 + o8);
                            ST128((u32x4{w1[0], w1[1], 0x3f803f80u, 0x00003f80u}), ok + (m & 1) * 256 + l15 * 16);
                            *(AS3(u32x2)*)(d + o8) = w2;
                            float n2 = 0.f;
#pragma unroll
                            for (int r = 0; r < 4; ++r) n2 = fmaf(acc[m][1][r], acc[m][1][r], n2);
                            kn = (n2 == n2) ? fmaxf(kn, n2) : __builtin_inff();
                        } else {
                            ST128((u32x4{cvtpk(acc[m][0][0], acc[m][0][1]), cvtpk(acc[m][0][2], acc[m][0][3]), 0x00003f80u, 0u}), ok + 1536 + o16);
                            *(AS3(u32x2)*)(d + o8) = w1;
                            *(AS3(u32x4)*)(d + 16 * 1536 + o16) = u32x4{w2[0], w2[1], 0x00003f80u, 0u};
                        }
                    }
                }
                if (cw == 3 && t3 && g == 0) {
#pragma unroll
                    for (int m = 0; m < 4; ++m) ST32(__float_as_uint(1.0f / (1.0f + __expf(-acc[m][3][0]))), WS_G + (rt * 128 + 64 * th + 16 * m + l15) * 4);
                }
                if (kd == 0 && (cw & 1)) {
                    kn = row_max16(kn);
                    if (l15 == 0) ST32(__float_as_uint(kn), WS_KMAX + ((4 * hj + g) * 64 + (cw == 3 ? 32 : 0) + rt * 2 + th) * 4);
                }
            }
            u32x4 wp0; u32x2 wp1;
            {
                const int fr = (r32 & 3) + 4 * (r32 >> 3);
                const lds_cptr wp = L + L_WPH + cw * 800 + (fr < 10 ? 10 * ((r32 >> 2) & 1) + fr : 0) * 2;
                unsigned wv[12];
#pragma unroll
                for (int j = 0; j < 8; ++j) wv[j] = *(const AS3(uint16_t)*)(wp + hh * 320 + j * 40);
#pragma unroll
                for (int j = 0; j < 4; ++j) wv[8 + j] = *(const AS3(uint16_t)*)(wp + 640 + j * 40);
                const unsigned m0 = fr < 10 ? 0xffffffffu : 0u, m1 = (fr < 10 && hh == 0) ? 0xffffffffu : 0u;
                wp0 = u32x4{(wv[0] | (wv[1] << 16)) & m0, (wv[2] | (wv[3] << 16)) & m0, (wv[4] | (wv[5] << 16)) & m0, (wv[6] | (wv[7] << 16)) & m0};
                wp1 = u32x2{(wv[8] | (wv[9] << 16)) & m1, (wv[10] | (wv[11] << 16)) & m1};
            }
            RAW_BAR();
#pragma unroll
            for (int t2 = 0; t2 < 2; ++t2) {
                const lds_cptr f = L + L_IMG + ((2 * th + t2) * 4 + cw) * 1536;
                unsigned ph[10];
                feature_map(wp0, wp1, *(const AS3(u32x4)*)(f + ka), *(const AS3(u32x2)*)(f + kb), ph);
                const lds_ptr d = (lds_ptr)lds + L_PH + (cw * 4 + 2 * th + t2) * 2560;
                if (kd) {
#pragma unroll
                    for (int u = 0; u < 5; ++u) *(AS3(u32x2)*)(d + r32 * 80 + hh * 40 + u * 8) = u32x2{ph[2 * u], ph[2 * u + 1]};
                } else {
                    *(AS3(u32x4)*)(d + l16) = u32x4{ph[0], ph[1], ph[2], ph[3]}; *(AS3(u32x4)*)(d + 1024 + l16) = u32x4{ph[4], ph[5], ph[6], ph[7]};
                    *(AS3(u32x2)*)(d + 2048 + lane * 8) = u32x2{ph[8], ph[9]};
                }
                if (t2 == 0) {
                    SBAR();
                    WAIT_VM(0);
                    BAR();
                    SBAR();
                }
            }
            if (kd) {
                RAW_BAR();
#pragma unroll
                for (int k2 = 0; k2 < 2; ++k2) {
                    if (k2 == 0 || wid < 4) {
                        const int u = wid + 8 * k2, uh = u / 3, un = u - 3 * uh;
                        f32x4 c0 = {0.f, 0.f, 0.f, 0.f}, c1 = {0.f, 0.f, 0.f, 0.f};
                        const unsigned vo = L_IMG + (16 + uh) * 1536;
                        const unsigned vb = 512 * (p4 >> 1) + 256 * (g >> 1) + 64 * ((2 * (g & 1)) ^ (p4 >> 1)) + 16 * q4 + 8 * (p4 & 1);
                        const unsigned vc = 1024 + 256 * (g >> 1) + 64 * ((2 * (g & 1)) ^ 2) + 16 * q4 + (p4 ? 8 : 0);
                        const unsigned pb = L_PH + (uh * 128 + 8 * g + q4) * 80 + un * 32 + p4 * 8;
#pragma unroll
                        for (int ks = 0; ks < 4; ++ks) {
                            const bf16x8 a0 = cat8(tr_read(L + vo + vb + ks * 6144), tr_read(L + vo + (vb ^ 64) + ks * 6144));
                            const bf16x8 a1 = cat8(tr_read(L + vo + vc + ks * 6144), tr_read(L + vo + (vc ^ 64) + ks * 6144));
                            const bf16x8 b = cat8(tr_read(L + pb + ks * 2560), tr_read(L + pb + ks * 2560 + 320));
                            c0 = MFMA16(a0, b, c0); c1 = MFMA16(a1, b, c1);
                        }
                        const int ph = 16 * un + l15;
                        const unsigned off = ph < 40 ? WS_KVP + ((4 * hj + uh) * 16 + rt) * 3840 + ph * 96 + 16 * g : OOB;
                        ST128(__builtin_bit_cast(u32x4, c0), off); ST128(__builtin_bit_cast(u32x4, c1), g < 2 ? off + 64 : OOB);
                    }
                }
            }
        }
        const int qp = wid & 3, kh = wid >> 2;
        const int own = 2 * qp + kh;
        const int blk = (lane >> 4) & 1, vj = blk ? 2 : p4 >> 1;
        const unsigned kb2 = 1024 + r32 * 16 + hh * 8;
        const unsigned va = 1536 + 512 * vj + 64 * ((2 * hh) ^ vj) + 16 * q4 + (blk ? (p4 ? 8 : 0) : 8 * (p4 & 1)), vb2 = va ^ 64;
        RAW_BAR();
        {
            const char* src = ws + WS_KV + ((size_t)h * 64 + kh * 32) * KVT + qp * 6144; const unsigned dst = lds0 + kh * SLOT + qp * 6144;
            glds_run<4, 0, 0, true>(src, l16, dst); glds_run<2, 4096, 4096, true>(src, l16, dst);
        }
        WAIT_VM(0);
        BAR();
        const lds_cptr lq = L + L_Q + 2 * qp * QFT;
        const u32x4 qa0 = *(const AS3(u32x4)*)(lq + ka), qb0 = *(const AS3(u32x4)*)(lq + QFT + ka);
        u32x2 qa1 = *(const AS3(u32x2)*)(lq + kb), qb1 = *(const AS3(u32x2)*)(lq + QFT + kb);
        float kmx = *(const AS3(float)*)(L + L_Q + 10240 + lane * 4);
        if (hh) { qa1 = u32x2{0u, 0u}; qb1 = u32x2{0u, 0u}; }
        const bf16x8 QA0 = __builtin_bit_cast(bf16x8, qa0), QB0 = __builtin_bit_cast(bf16x8, qb0);
        float nba, nbb;
        {
            kmx = row_max16(kmx);
            auto r = __builtin_amdgcn_permlane16_swap(__float_as_uint(kmx), __float_as_uint(kmx), false, false);
            kmx = fmaxf(__uint_as_float(r[0]), __uint_as_float(r[1]));
            const float kn = -1.01f * __builtin_amdgcn_sqrtf(xhalf_sum(kmx));
            nba = kn * __builtin_amdgcn_sqrtf(qnorm2(qa0, qa1)); nbb = kn * __builtin_amdgcn_sqrtf(qnorm2(qb0, qb1));
        }
        for (int attempt = 0;; ++attempt) {
            f32x16 oa, ob, sA, sB;
#pragma unroll
            for (int r = 0; r < 16; ++r) { oa[r] = 0.f; ob[r] = 0.f; sB[r] = 0.f; }
            const s16x4 QA1 = __builtin_bit_cast(s16x4, hh ? split3(nba) : qa1), QB1 = __builtin_bit_cast(s16x4, hh ? split3(nbb) : qb1);
            lds_cptr pka = L + kh * SLOT + ka, pkb = L + kh * SLOT + kb2, pv = L + kh * SLOT + va, pv2 = L + kh * SLOT + vb2;
            KFrag k0 = ld_k(pka, pkb, 0), k1 = ld_k(pka, pkb, KVT);
            VFrag v0 = ld_v(pv, 0), v1 = v0;
            sA = MFMA32(k0.k0, QA0, f32x16{}); sA = MFMA32K8(k0.k1, QA1, sA);
            for (int c = 0; c < 4; ++c) {
                const int dnext = (c & 1) ? -2 * SLOT : 2 * SLOT;
#define K2_PAIR(t, LOADK0, LOADV0, LOADK1) do { \
                bf16x8 vp = v1.v1; \
                v1 = ld_v(pv, ((t) + 1) * KVT); SBAR(); \
                attn_step(sA, sB, k0, vp, v0.v0, QB0, QB1, ob, oa); \
                LOADK0; SBAR(); \
                attn_step(sB, sA, k1, v0.v1, v0.v0, QA0, QA1, oa, ob); \
                vp = v0.v1; \
                LOADV0; SBAR(); \
                attn_step(sA, sB, k1, vp, v1.v0, QB0, QB1, ob, oa); \
                LOADK1; SBAR(); \
                attn_step(sB, sA, k0, v1.v1, v1.v0, QA0, QA1, oa, ob); } while (0)
#define PRIO(p) do { asm volatile("s_cmp_eq_u32 %0, 0\n\ts_cbranch_scc1 1f\n\ts_setprio " #p "\n1:" :: "s"(kh) : "scc"); SBAR(); } while (0)
                K2_PAIR(0, k0 = ld_k(pka, pkb, 2 * KVT), v0 = ld_v(pv, 2 * KVT), k1 = ld_k(pka, pkb, 3 * KVT));
                K2_PAIR(2, k0 = ld_k(pka, pkb, 4 * KVT), v0 = ld_v(pv, 4 * KVT), k1 = ld_k(pka, pkb, 5 * KVT));
                PRIO(0);
                K2_PAIR(4, k0 = ld_k(pka, pkb, 6 * KVT), v0 = ld_v(pv, 6 * KVT), k1 = ld_k(pka, pkb, 7 * KVT));
#define TURN() do { asm volatile("s_cmp_eq_u32 %0, 3\n\ts_cbranch_scc1 1f\n\ts_waitcnt vmcnt(0) lgkmcnt(0)\n\ts_barrier\n1:" :: "s"(c) : "memory", "scc"); \
                    pka += dnext; pkb += dnext; pv += dnext; pv2 += dnext; k0 = ld_k(pka, pkb, 0); PRIO(1); } while (0)
                K2_PAIR(6, TURN(), v0 = ld_v(pv, 0), k1 = ld_k(pka, pkb, KVT));
            }
            ob = SMF(v1.v1, HI8(sB), ob);
            PRIO(0);
            {
                lds_ptr d = (lds_ptr)lds + L_X + (own ^ 1) * 3072 + l16;
#pragma unroll
                for (int a = 0; a < 3; ++a)
                    *(AS3(f32x4)*)(d + a * 1024) = kh ? f32x4{oa[4 * a], oa[4 * a + 1], oa[4 * a + 2], oa[4 * a + 3]} : f32x4{ob[4 * a], ob[4 * a + 1], ob[4 * a + 2], ob[4 * a + 3]};
            }
            RAW_BAR();
            float o[12];
#pragma unroll
            for (int a = 0; a < 3; ++a) {
                const f32x4 t = *(const AS3(f32x4)*)(L + L_X + own * 3072 + l16 + a * 1024);
#pragma unroll
                for (int r = 0; r < 4; ++r) o[4 * a + r] = t[r] + (kh ? ob[4 * a + r] : oa[4 * a + r]);
            }
            auto rr = __builtin_amdgcn_permlane32_swap(__float_as_uint(o[8]), __float_as_uint(o[8]), false, false);
            const float lsum = __uint_as_float(rr[1]);
            const float sa = *(const AS3(float)*)(L + L_GATE + (own * 32 + r32) * 4) * __builtin_amdgcn_rcpf(lsum);
            const unsigned ocb = WS_OC + ((qt * 8 + own) * 32 + r32) * 16;
            ST128((u32x4{cvtpk(o[0] * sa, o[1] * sa), cvtpk(o[2] * sa, o[3] * sa), cvtpk(o[4] * sa, o[5] * sa), cvtpk(o[6] * sa, o[7] * sa)}), ocb + (2 * h + hh) * (SEQ * 16));
            if (!hh) ST64((u32x2{cvtpk(o[8] * sa, o[9] * sa), cvtpk(o[10] * sa, o[11] * sa)}), ocb + (64 + (h >> 1)) * (SEQ * 16) + (h & 1) * 8);
            if (attempt == 0 && __any(!(lsum >= 5.4210109e-20f)))
                *(AS3(unsigned)*)((lds_ptr)lds + L_FLAG) = 1u;
            WAIT_VM(0);
            RAW_BAR();
            const unsigned redo = *(const AS3(unsigned)*)(L + L_FLAG);
            if (__builtin_amdgcn_readfirstlane(redo) == 0u) break;
            float ma = -__builtin_inff(), mb = -__builtin_inff();
            for (int t = 0; t < 64; ++t) {
                const u32x4 g0 = LD128(WS_KV + (h * 64 + t) * KVT + ka);
                const u32x2 g1 = LD64(WS_KV + (h * 64 + t) * KVT + kb2);
                f32x16 s = MFMA32(__builtin_bit_cast(bf16x8, g0), QA0, f32x16{}); s = MFMA32K8(__builtin_bit_cast(s16x4, g1), __builtin_bit_cast(s16x4, qa1), s);
                f32x16 u = MFMA32(__builtin_bit_cast(bf16x8, g0), QB0, f32x16{}); u = MFMA32K8(__builtin_bit_cast(s16x4, g1), __builtin_bit_cast(s16x4, qb1), u);
#pragma unroll
                for (int r = 0; r < 16; ++r) { ma = fmaxf(ma, s[r]); mb = fmaxf(mb, u[r]); }
            }
            nba = -xhalf_max(ma); nbb = -xhalf_max(mb);
            RAW_BAR();
            BAR();
        }
        {
            const int mt = wid & 3, kp = wid >> 2;
            const unsigned ofo = WS_OC + ((4 * kp + g) * SEQ + tt * 64 + 16 * mt + l15) * 16;
            const lds_cptr wa = L + g * 1280 + (20 * q4 + p4) * 16;
            f32x4 acc[5];
#pragma unroll
            for (int i = 0; i < 5; ++i) acc[i] = f32x4{0.f, 0.f, 0.f, 0.f};
            u32x4 of[10];
#define P3_MMA(br) do { if (kp) { _Pragma("unroll") for (int k = 0; k < 10; ++k) _Pragma("unroll") for (int i = 0; i < 5; ++i) \
                                      acc[i] = MFMA16(LDS_B128(wa + p3_off(br, 2 * k + 1) + i * 64), __builtin_bit_cast(bf16x8, of[k]), acc[i]); } \
                        else { _Pragma("unroll") for (int k = 0; k < 10; ++k) _Pragma("unroll") for (int i = 0; i < 5; ++i) \
                                      acc[i] = MFMA16(LDS_B128(wa + p3_off(br, 2 * k) + i * 64), __builtin_bit_cast(bf16x8, of[k]), acc[i]); } } while (0)
#pragma unroll
            for (int k = 0; k < 10; ++k) of[k] = LD128(ofo + (80 + 8 * k) * (SEQ * 16));
            BAR();
            P3_MMA(1);
            RAW_BAR();
            for (int ks = 7 + wid; ks < 20; ks += 8) P3_WKS(0, ks, ks < 17 ? L_RA + (ks - 7) * 5120 : L_RB + (ks - 17) * 5120);
            BAR();
#pragma unroll
            for (int k = 0; k < 10; ++k) of[k] = LD128(ofo + (8 * k) * (SEQ * 16));
            WAIT_VM(10);
            BAR();
            P3_MMA(0);
            if (kp) {
#pragma unroll
                for (int i = 0; i < 5; ++i) *(AS3(f32x4)*)((lds_ptr)lds + L_CX + (mt * 5 + i) * 1024 + l16) = acc[i];
            }
            RAW_BAR();
            if (!kp) {
                char* dst = (char*)out + (size_t)(tt * 64 + 16 * mt + l15) * 1280 + ct * 160 + g * 40;
#pragma unroll
                for (int i = 0; i < 5; ++i) {
                    const f32x4 v = acc[i] + *(const AS3(f32x4)*)(L + L_CX + (mt * 5 + i) * 1024 + l16);
                    *(u32x2*)(dst + i * 8) = u32x2{cvtpk(v[0], v[1]), cvtpk(v[2], v[3])};
                }
            }
        }
    }
    if (bid == 0 && tid == 512) publish(ws, WS_EPOCH, tag);
}

extern "C" void kernel_launch(void* const* d_in, const int* in_sizes, int n_in, void* d_out, int out_size, void* d_ws, size_t ws_size,
                              hipStream_t stream) {
    fused_kernel<<<256, 768, 0, stream>>>((const uint16_t*)d_in[0], (const uint16_t*)d_in[1], (const uint16_t*)d_in[4], (const uint16_t*)d_in[2],
                                          (const uint16_t*)d_in[6], (const uint16_t*)d_in[5], (const uint16_t*)d_in[7], (char*)d_ws, (uint16_t*)d_out);
}
```

```cpp
#include <hip/hip_runtime.h>
#include <hip/hip_bf16.h>
#include <stdint.h>

typedef __attribute__((ext_vector_type(8))) __bf16 bf16x8;
typedef __attribute__((ext_vector_type(16))) __bf16 bf16x16;
typedef __attribute__((ext_vector_type(4))) short s16x4;
typedef __attribute__((ext_vector_type(8))) short s16x8;
typedef __attribute__((ext_vector_type(4))) float f32x4;
typedef __attribute__((ext_vector_type(16))) float f32x16;
typedef __attribute__((ext_vector_type(4))) unsigned u32x4;
typedef __attribute__((ext_vector_type(2))) unsigned u32x2;
typedef __attribute__((ext_vector_type(2))) __bf16 bf16x2;
typedef __attribute__((ext_vector_type(2))) float f32x2;
typedef __attribute__((address_space(3))) const char* lds_cptr;
typedef __attribute__((address_space(3))) char* lds_ptr;
typedef unsigned long long u64;
#define AS3(T) __attribute__((address_space(3))) T

constexpr unsigned CODE_BYTES = 30000;
#define ROTF xcd
constexpr int SEQ = 2048, NH = 32;
constexpr unsigned OOB = 0x7ffffff0u;
constexpr float LOG2E = 1.4426950408889634f;
constexpr float QSCALE = 0.22360679774997896f * 1.4426950408889634f;

constexpr int KVT = 3072;
constexpr int QFT = 1280;
constexpr unsigned WS_KV = 0;
constexpr unsigned WS_QA = WS_KV + NH * 64 * KVT;
constexpr unsigned WS_PHQ = WS_QA + NH * 64 * QFT;
constexpr unsigned WS_WOP = WS_PHQ + NH * 64 * 2560;
constexpr unsigned WS_OC = WS_WOP + 160 * 640 * 16;
constexpr unsigned WS_KMAX = WS_OC + 160 * SEQ * 16;
constexpr unsigned WS_G = WS_KMAX + NH * 64 * 4;
constexpr unsigned WS_KVP = WS_G + SEQ * 4;
constexpr unsigned WS_FT = WS_KVP + NH * 16 * 3840;
constexpr unsigned WS_FH = WS_FT + 2048;
constexpr unsigned WS_FOB = WS_FH + 16384;
constexpr unsigned WS_FOA = WS_FOB + 2048;
constexpr unsigned WS_EPOCH = WS_FOA + 2048;
constexpr unsigned WS_END = WS_EPOCH + 128;

__device__ __forceinline__ unsigned cvtpk(float lo, float hi) {
    return __builtin_bit_cast(unsigned, __builtin_convertvector(f32x2{lo, hi}, bf16x2)); }
__device__ __forceinline__ float bflo(unsigned w) { return __uint_as_float(w << 16); }
__device__ __forceinline__ float bfhi(unsigned w) { return __uint_as_float(w & 0xffff0000u); }
#define GL1(P) "global_load_lds_dwordx4 %1, %2" P "\n\t"
#define GL2(P) GL1(P) "global_load_lds_dwordx4 %1, %2 offset:1024" P "\n\t"
#define GL3(P) GL2(P) "global_load_lds_dwordx4 %1, %2 offset:2048" P "\n\t"
#define GL4(P) GL3(P) "global_load_lds_dwordx4 %1, %2 offset:3072" P "\n\t"
#define GLASM(BODY) asm volatile("s_mov_b32 %0, m0\n\ts_add_u32 m0, %3, %4\n\ts_nop 0\n\t" BODY "s_mov_b32 m0, %0" \
                     : "=&s"(sv) : "v"(voff), "s"(t), "s"(lbase), "i"(LOFF) : "memory", "scc")
template <int N, int GOFF, int LOFF, bool SC1>
__device__ __forceinline__ void glds_run(const void* sbase, unsigned voff, unsigned lbase) {
    static_assert(N >= 1 && N <= 4, "");
    unsigned sv, tl, th;
    lbase = __builtin_amdgcn_readfirstlane(lbase);
    const unsigned lo = __builtin_amdgcn_readfirstlane((unsigned)(uintptr_t)sbase), hi = __builtin_amdgcn_readfirstlane((unsigned)((uintptr_t)sbase >> 32));
    asm volatile("s_add_u32 %0, %2, %4\n\ts_addc_u32 %1, %3, 0" : "=&s"(tl), "=s"(th) : "s"(lo), "s"(hi), "i"(GOFF) : "scc");
    const uint64_t t = ((uint64_t)th << 32) | tl;
    if constexpr (SC1) {
        if constexpr (N == 1) GLASM(GL1(" sc1")); else if constexpr (N == 2) GLASM(GL2(" sc1")); else if constexpr (N == 3) GLASM(GL3(" sc1")); else GLASM(GL4(" sc1"));
    } else {
        if constexpr (N == 1) GLASM(GL1("")); else if constexpr (N == 2) GLASM(GL2("")); else if constexpr (N == 3) GLASM(GL3("")); else GLASM(GL4(""));
    }
}
template <int LOFF>
__device__ __forceinline__ void glds_ptr(const void* p, unsigned lbase) {
    unsigned sv; lbase = __builtin_amdgcn_readfirstlane(lbase);
    asm volatile("s_mov_b32 %0, m0\n\ts_add_u32 m0, %2, %3\n\ts_nop 0\n\tglobal_load_lds_dwordx4 %1, off\n\ts_mov_b32 m0, %0"
                 : "=&s"(sv) : "v"(p), "s"(lbase), "i"(LOFF) : "memory", "scc"); }
__device__ __forceinline__ void touch(const void* p, unsigned lds_scratch) {
    unsigned sv;
    asm volatile("s_mov_b32 %0, m0\n\ts_mov_b32 m0, %2\n\ts_nop 0\n\tglobal_load_lds_dword %1, off sc1\n\ts_mov_b32 m0, %0" : "=&s"(sv) : "v"(p), "s"(lds_scratch) : "memory"); }
__device__ __forceinline__ s16x4 tr_read(lds_cptr p) { return __builtin_amdgcn_ds_read_tr16_b64_v4i16((AS3(s16x4)*)p); }
__device__ __forceinline__ bf16x8 cat8(s16x4 a, s16x4 b) {
    s16x8 r = {a[0], a[1], a[2], a[3], b[0], b[1], b[2], b[3]}; return __builtin_bit_cast(bf16x8, r); }
#define LDS_B128(p) (*(const AS3(bf16x8)*)(p))
#define LDS_B64(p) (*(const AS3(s16x4)*)(p))
#define MFMA16(a, b, c) __builtin_amdgcn_mfma_f32_16x16x32_bf16(a, b, c, 0, 0, 0)
#define MFMA32(a, b, c) __builtin_amdgcn_mfma_f32_32x32x16_bf16(a, b, c, 0, 0, 0)
#define MFMA32K8(a, b, c) __builtin_amdgcn_mfma_f32_32x32x8bf16_1k(a, b, c, 0, 0, 0)
#define WAIT_VM_(n) asm volatile("s_waitcnt vmcnt(" #n ")" ::: "memory")
#define WAIT_VM(n) WAIT_VM_(n)
#define RAW_BAR() asm volatile("s_waitcnt lgkmcnt(0)\n\ts_barrier" ::: "memory")
#define SBAR() __builtin_amdgcn_sched_barrier(0)
#define EXP2(v) __builtin_amdgcn_exp2f(v)
#define SMF(a, lo, acc) __builtin_amdgcn_smfmac_f32_32x32x32_bf16(a, __builtin_bit_cast(bf16x16, lo), acc, 0xDDDD, 0, 0)
#define LO8(s) __builtin_shufflevector(s, s, 0, 1, 2, 3, 4, 5, 6, 7)
#define HI8(s) __builtin_shufflevector(s, s, 8, 9, 10, 11, 12, 13, 14, 15)
#define ST128(v, off) __builtin_amdgcn_raw_buffer_store_b128(v, wsr, off, 0, 16)
#define ST64(v, off) __builtin_amdgcn_raw_buffer_store_b64(v, wsr, off, 0, 16)
#define ST32(v, off) __builtin_amdgcn_raw_buffer_store_b32(v, wsr, off, 0, 16)
__device__ __forceinline__ float xhalf_max(float v) {
    auto r = __builtin_amdgcn_permlane32_swap(__float_as_uint(v), __float_as_uint(v), false, false);
    return fmaxf(__uint_as_float(r[0]), __uint_as_float(r[1])); }
__device__ __forceinline__ float xhalf_min(float v) {
    auto r = __builtin_amdgcn_permlane32_swap(__float_as_uint(v), __float_as_uint(v), false, false);
    return fminf(__uint_as_float(r[0]), __uint_as_float(r[1])); }
__device__ __forceinline__ float xhalf_sum(float v) {
    auto r = __builtin_amdgcn_permlane32_swap(__float_as_uint(v), __float_as_uint(v), false, false);
    return __uint_as_float(r[0]) + __uint_as_float(r[1]); }
__device__ __forceinline__ float row_max16(float v) {
    v = fmaxf(v, __int_as_float(__builtin_amdgcn_update_dpp(0, __float_as_int(v), 0x128, 0xf, 0xf, false)));
    v = fmaxf(v, __int_as_float(__builtin_amdgcn_update_dpp(0, __float_as_int(v), 0x124, 0xf, 0xf, false)));
    v = fmaxf(v, __int_as_float(__builtin_amdgcn_update_dpp(0, __float_as_int(v), 0x122, 0xf, 0xf, false)));
    v = fmaxf(v, __int_as_float(__builtin_amdgcn_update_dpp(0, __float_as_int(v), 0x121, 0xf, 0xf, false)));
    return v; }
__device__ __forceinline__ void publish(char* ws, unsigned off, u64 tag) {
    __hip_atomic_store((u64*)(ws + off), tag, __ATOMIC_RELAXED, __HIP_MEMORY_SCOPE_AGENT); }
__device__ __forceinline__ void wait_flags(const char* ws, unsigned off, u64 tag) {
    for (int spin = 0; spin < (1 << 20); ++spin) {
        const u64 f = __hip_atomic_load((const u64*)(ws + off), __ATOMIC_RELAXED, __HIP_MEMORY_SCOPE_AGENT);
        if (__all(f == tag)) break;
        __builtin_amdgcn_s_sleep(2);
    }
    asm volatile("" ::: "memory");
}

struct KFrag { bf16x8 k0; s16x4 k1; };
struct VFrag { bf16x8 v0, v1; };
__device__ __forceinline__ KFrag ld_k(lds_cptr pa, lds_cptr pb, int off) { KFrag k; k.k0 = LDS_B128(pa + off); k.k1 = LDS_B64(pb + off); return k; }
__device__ __forceinline__ VFrag ld_v2(lds_cptr p, lds_cptr q, int off) {
    VFrag v; v.v0 = cat8(tr_read(p + off), tr_read(q + off)); v.v1 = cat8(tr_read(p + off + 256), tr_read(q + off + 256)); return v; }
#define ld_v(p, off) ld_v2(p, p##2, off)
__device__ __forceinline__ void attn_step(f32x16& sc, f32x16& sn, const KFrag& kn, const bf16x8& vprev1, const bf16x8& vc0,
                                          const bf16x8& qn0, const s16x4& qn1, f32x16& oprev, f32x16& oc) {
    oprev = SMF(vprev1, HI8(sn), oprev); SBAR();
#pragma unroll
    for (int r = 0; r < 4; ++r) sc[r] = EXP2(sc[r]);
    SBAR(); sn = MFMA32(kn.k0, qn0, f32x16{}); SBAR();
#pragma unroll
    for (int r = 4; r < 8; ++r) sc[r] = EXP2(sc[r]);
    SBAR(); sn = MFMA32K8(kn.k1, qn1, sn); SBAR();
#pragma unroll
    for (int r = 8; r < 12; ++r) sc[r] = EXP2(sc[r]);
    SBAR(); oc = SMF(vc0, LO8(sc), oc); SBAR();
#pragma unroll
    for (int r = 12; r < 16; ++r) sc[r] = EXP2(sc[r]);
    SBAR();
}
__device__ __forceinline__ void feature_map(const u32x4 wp0, const u32x2 wp1, const u32x4 f0, const u32x2 f1, unsigned (&ph)[10]) {
    f32x16 pq = MFMA32(__builtin_bit_cast(bf16x8, wp0), __builtin_bit_cast(bf16x8, f0), f32x16{});
    pq = MFMA32K8(__builtin_bit_cast(s16x4, wp1), __builtin_bit_cast(s16x4, f1), pq);
    float mx = fmaxf(pq[0], pq[1]), mn = fminf(pq[0], pq[1]);
#pragma unroll
    for (int u = 2; u < 10; u += 2) { mx = fmaxf(fmaxf(mx, pq[u]), pq[u + 1]); mn = fminf(fminf(mn, pq[u]), pq[u + 1]); }
    mx = xhalf_max(mx); mn = xhalf_min(mn);
    const f32x2 lp = {LOG2E, LOG2E}, cmx = {-mx * LOG2E, -mx * LOG2E}, cmn = {mn * LOG2E, mn * LOG2E};
    f32x2 ep[5], en[5], sp2 = {0.f, 0.f}, sn2 = {0.f, 0.f};
#pragma unroll
    for (int u = 0; u < 5; ++u) {
        const f32x2 p = {pq[2 * u], pq[2 * u + 1]};
        const f32x2 a = __builtin_elementwise_fma(p, lp, cmx), b = __builtin_elementwise_fma(p, -lp, cmn);
        ep[u] = f32x2{EXP2(a[0]), EXP2(a[1])}; en[u] = f32x2{EXP2(b[0]), EXP2(b[1])};
        sp2 += ep[u]; sn2 += en[u];
    }
    const float sp = __builtin_amdgcn_rcpf(xhalf_sum(sp2[0] + sp2[1])), sn = __builtin_amdgcn_rcpf(xhalf_sum(sn2[0] + sn2[1]));
#pragma unroll
    for (int u = 0; u < 5; ++u) {
        const f32x2 a = ep[u] * sp, b = en[u] * sn;
        ph[u] = cvtpk(a[0], a[1]); ph[5 + u] = cvtpk(b[0], b[1]);
    }
}
__device__ __forceinline__ u32x2 split3(float v) {
    const unsigned hi = cvtpk(v, 0.f) & 0xffffu; const float r1 = v - bflo(hi);
    const unsigned mid = cvtpk(r1, 0.f) & 0xffffu; const float r2 = r1 - bflo(mid);
    return u32x2{hi | (mid << 16), cvtpk(r2, 0.f) & 0xffffu};
}
__device__ __forceinline__ float qnorm2(const u32x4& a, const u32x2& b) {
    float n2 = 0.f;
#pragma unroll
    for (int j = 0; j < 4; ++j) { n2 = fmaf(bflo(a[j]), bflo(a[j]), n2); n2 = fmaf(bfhi(a[j]), bfhi(a[j]), n2); }
#pragma unroll
    for (int j = 0; j < 2; ++j) { n2 = fmaf(bflo(b[j]), bflo(b[j]), n2); n2 = fmaf(bfhi(b[j]), bfhi(b[j]), n2); }
    return xhalf_sum(n2);
}

#define LD128(off) __builtin_amdgcn_raw_buffer_load_b128(wsr, off, 0, 16)
#define LD64(off) __builtin_amdgcn_raw_buffer_load_b64(wsr, off, 0, 16)
#define LD32(off) __builtin_amdgcn_raw_buffer_load_b32(wsr, off, 0, 16)
#define BAR() asm volatile("s_barrier" ::: "memory")

__device__ __forceinline__ const char* w_source(int p, int lane, const char* g0, const char* g1, const char* g2) {
    int q = (p & 7) * 64 + lane; const int up = q >= 248; q -= up ? 248 : 0; q = q < 239 ? q : 239;
    const int row = __umul24(q, 2185) >> 16, ch = q - row * 30, sgm = __umul24(ch, 6554) >> 16;
    const uintptr_t b = sgm == 0 ? (uintptr_t)g0 : sgm == 1 ? (uintptr_t)g1 : (uintptr_t)g2;
    return (const char*)(b + (__umul24(16 * (p >> 3) + 8 * up + row, 3840) + (ch - 10 * sgm) * 16));
}

constexpr int P1_STAGE = 49152;
constexpr int L_WPH = 3 * P1_STAGE;
constexpr int L_GW = L_WPH + 3200;
constexpr int L_WO = L_GW + 1280;
constexpr int L_SCR = L_WO + 10240;
constexpr int L_FLAG = L_SCR + 256;
constexpr int LDS_BYTES = L_FLAG + 16;
constexpr int L_IMG = P1_STAGE;
constexpr int L_PH = L_IMG + 32 * 1536;
constexpr int L_Q = L_PH + 40960 + 256;
constexpr int SLOT = 8 * KVT;
constexpr int L_X = 0;
constexpr int L_MISC = L_WO;
constexpr int L_GATE = L_MISC + 3072;
constexpr int L_RA = 4 * SLOT, L_RB = SLOT, L_RC = 2 * SLOT, L_RD = 0, L_RE = L_RC + 6 * 5120;
constexpr int L_CX = L_RC;
static_assert(L_Q + 10240 + 256 <= L_WO && L_RA + 10 * 5120 <= L_WO && L_RE + 3 * 5120 <= L_RA && LDS_BYTES <= 163840, "");
__device__ __forceinline__ constexpr int p3_off(int br, int ks) {
    return br ? (ks < 10 ? L_RA + ks * 5120 : ks < 14 ? L_RB + (ks - 10) * 5120 : L_RC + (ks - 14) * 5120)
              : (ks < 4 ? L_RD + ks * 5120 : ks < 7 ? L_RE + (ks - 4) * 5120 : ks < 17 ? L_RA + (ks - 7) * 5120 : L_RB + (ks - 17) * 5120); }

__global__ __launch_bounds__(768, 3) void fused_kernel(const uint16_t* __restrict__ x, const uint16_t* __restrict__ wA, const uint16_t* __restrict__ wB,
                                                    const uint16_t* __restrict__ woA, const uint16_t* __restrict__ woB,
                                                    const uint16_t* __restrict__ wphi, const uint16_t* __restrict__ gatew,
                                                    char* __restrict__ ws, uint16_t* __restrict__ out) {
    __shared__ __attribute__((aligned(16))) char lds[LDS_BYTES];
    uint64_t pc0; asm volatile("s_getpc_b64 %0" : "=s"(pc0));
    const int bid = blockIdx.x, xcd = bid & 7, bi = bid >> 3;
    const int tid = threadIdx.x, lane = tid & 63;
    const int wid = __builtin_amdgcn_readfirstlane(tid >> 6);
    const unsigned lds0 = (unsigned)(uintptr_t)lds;
    const lds_cptr L = (lds_cptr)lds;
    const unsigned l16 = lane * 16;
    const int g = lane >> 4, l15 = lane & 15, q4 = l15 >> 2, p4 = lane & 3, r32 = lane & 31, hh = lane >> 5;
    const auto wsr = __builtin_amdgcn_make_buffer_rsrc(ws, 0, WS_END, 0x00020000);
    const int rt = (xcd & 1) * 8 + (bi & 7), kd = bi >> 4, hj = (xcd >> 1) * 2 + ((bi >> 3) & 1);
    const int h = xcd * 4 + (bi >> 3), qt = bi & 7;
    const int tt = xcd * 4 + (bi & 3), ct = bi >> 2;
    const char* xb = (const char*)x + (size_t)rt * 128 * 1280;
    const unsigned ka = lane * 16, kb = 1024 + r32 * 8;
    const char* wg0 = (const char*)wA + hj * 160 + (kd ? 2560 : 0);
    const char* wg1 = kd ? (const char*)wB + hj * 160 + 1280 : (const char*)wA + hj * 160 + 1280;
    const char* wg2 = (const char*)wB + hj * 160 + (kd ? 2560 : 0);
    const int rot = ROTF;
#define KC(st) ((st) + rot >= 10 ? (st) + rot - 10 : (st) + rot)
#define WSTEP(st) (KC(st) == 9 ? -9ll * 245760 : 245760ll)
#define wsrc(p) (w_source(p, lane, wg0, wg1, wg2) + rot * 245760)
#define P3_WKS(br, ks, dst) do { _Pragma("unroll") for (int j_ = 0; j_ < 5; ++j_) { const int q_ = j_ * 64 + lane, c_ = __umul24(q_, 52429) >> 22, n_ = q_ - c_ * 80; \
        glds_run<1, 0, 0, true>(ws + WS_WOP + ct * 1280 + ((br) * 80 + 4 * (ks)) * 10240, c_ * 10240 + n_ * 16, lds0 + (dst) + j_ * 1024); } } while (0)
    u64 tag = 0;
#define MAKE_TAG() do { u64 z = ((u64)(unsigned)__builtin_amdgcn_readfirstlane((unsigned)(tag >> 32)) << 32) | (unsigned)__builtin_amdgcn_readfirstlane((unsigned)tag); \
        z = z * 0x9E3779B97F4A7C15ull + 0x632BE59BD9B4E019ull; z ^= z >> 29; z *= 0xBF58476D1CE4E5B9ull; z ^= z >> 32; tag = z; } while (0)

    if (wid >= 8) {
        const int hw = wid - 8;
        __builtin_amdgcn_s_setprio(2);
        unsigned hbt = 0;
        if (tid == 512) *(AS3(unsigned)*)((lds_ptr)lds + L_FLAG + 4) = 0u;
#define HBAR() do { hbt += 4; asm volatile("s_waitcnt lgkmcnt(0)" ::: "memory"); \
        if (lane == 0) __hip_atomic_fetch_add((AS3(unsigned)*)((lds_ptr)lds + L_FLAG + 4), 1u, __ATOMIC_RELAXED, __HIP_MEMORY_SCOPE_WORKGROUP); \
        while (__builtin_amdgcn_readfirstlane(__hip_atomic_load((AS3(unsigned)*)((lds_ptr)lds + L_FLAG + 4), __ATOMIC_RELAXED, __HIP_MEMORY_SCOPE_WORKGROUP)) < hbt) __builtin_amdgcn_s_sleep(1); \
        asm volatile("" ::: "memory"); } while (0)
        const char* pw[4];
#pragma unroll
        for (int j = 0; j < 4; ++j) pw[j] = wsrc(16 + 4 * hw + j);
        auto wstage = [&](int st) {
            const unsigned lw = lds0 + (st % 3) * P1_STAGE + 32768 + hw * 4096;
            glds_ptr<0>(pw[0], lw); glds_ptr<1024>(pw[1], lw); glds_ptr<2048>(pw[2], lw); glds_ptr<3072>(pw[3], lw);
#pragma unroll
            for (int j = 0; j < 4; ++j) pw[j] += WSTEP(st);
        };
        if (hj == 0 && kd == 0 && hw < 2 && hw * 1024 + l16 < 1280) glds_run<1, 0, L_GW, false>(gatew, hw * 1024 + l16, lds0 + hw * 1024);
        wstage(0);
        RAW_BAR();
        wstage(1);
        WAIT_VM(4);
        BAR();
        wstage(2);
        const char* tp = hw == 0 ? xb + ((bi >> 3) * 32 + r32) * 1280
                       : hw == 1 ? ((lane % 6) < 2 ? wg0 : (lane % 6) < 4 ? wg1 : wg2) + ((bi & 7) * 8 + (lane < 48 ? lane / 6 : 0)) * 3840 + (lane & 1) * 156 : xb;
        const unsigned tstep = hw == 0 ? 128u : hw == 1 ? 245760u : 0u;
        touch(tp + KC(3) * tstep, lds0 + L_SCR); touch(tp + KC(4) * tstep, lds0 + L_SCR);
        for (int s = 0; s < 9; ++s) {
            if (s == 0) { WAIT_VM(6); } else if (s == 1) { WAIT_VM(7); } else if (s < 7) { WAIT_VM(6); } else if (s == 7) { WAIT_VM(10); } else { WAIT_VM(0); }
            BAR();
            if (s < 7) wstage(s + 3);
            if (s < 5) touch(tp + KC(s + 5) * tstep, lds0 + L_SCR);
            if (s == 5) { const unsigned o = (hw * 64 + lane) * 128; touch((const char*)pc0 + (o < CODE_BYTES ? o : 0u), lds0 + L_SCR); }
            if (s == 6) {
                const int cb = bid < 160 ? bid : 0, ch = cb < 80 ? cb : cb - 80;
                const int r0 = (ch < 64 ? (ch >> 1) * 20 + (ch & 1) * 4 : (ch - 64) * 40 + 16) + hw, dr = (ch < 64 ? 8 : 20) * 1280;
                const char* pr = (const char*)(cb < 80 ? woA : woB) + r0 * 1280; const unsigned lr = lds0 + hw * 1280;
                glds_run<1, 0, L_WO, false>(pr, l16, lr); glds_run<1, 0, L_WO + 5120, false>(pr + dr, l16, lr);
                if (lane < 16) { glds_run<1, 1024, L_WO + 1024, false>(pr, l16, lr); glds_run<1, 1024, L_WO + 6144, false>(pr + dr, l16, lr); }
                if (hw * 1024 + l16 < 3200) glds_run<1, 0, L_WPH, false>((const char*)wphi + hj * 3200, hw * 1024 + l16, lds0 + hw * 1024);
            }
            if (s == 7) tag = __hip_atomic_load((const u64*)(ws + WS_EPOCH), __ATOMIC_RELAXED, __HIP_MEMORY_SCOPE_AGENT);
            if (s == 8 && bid < 160) {
                const lds_cptr r = L + L_WO + q4 * 1280 + g * 32 + p4 * 8 + hw * 128;
#pragma unroll
                for (int j = 0; j < 3; ++j)
                    if (j < 2 || hw < 2) ST128(__builtin_bit_cast(u32x4, cat8(tr_read(r + j * 512), tr_read(r + j * 512 + 5120))), WS_WOP + (bid * 640 + (j * 4 + hw) * 64 + lane) * 16);
            }
        }
        MAKE_TAG();
        WAIT_VM(0);
        BAR();
        BAR();
        const unsigned fhoff = WS_FH + (((4 * hj + (lane & 3)) * 4 + kd) * 16 + rt) * 8;
        if (hw == 0 && lane < 5) publish(ws, lane < 4 ? fhoff : WS_FT + bid * 8, tag);
        if (kd) BAR();
        const char* kvbase = ws + WS_KV + (size_t)h * 64 * KVT;
        const char* kvw = kvbase + (size_t)(hw >> 1) * 4 * SLOT + (hw & 1) * 12288; const unsigned ldw = lds0 + (hw >> 1) * SLOT + (hw & 1) * 12288;
#define H_CHUNK(j) do { glds_run<4, (j) * SLOT, ((j) & 1) * 2 * SLOT, true>(kvw, l16, ldw); glds_run<4, (j) * SLOT + 4096, ((j) & 1) * 2 * SLOT + 4096, true>(kvw, l16, ldw); \
                        glds_run<4, (j) * SLOT + 8192, ((j) & 1) * 2 * SLOT + 8192, true>(kvw, l16, ldw); } while (0)
        if (hw == 0) wait_flags(ws, WS_FH + (h * 64 + r32) * 8, tag);
        BAR();
        {
            const char* pq = ws + WS_QA + ((size_t)h * 64 + qt * 8) * QFT;
            if (hw < 2) glds_run<3, 0, L_Q, true>(pq + hw * 3072, l16, lds0 + hw * 3072); else glds_run<2, 0, L_Q, true>(pq + 2048 + hw * 2048, l16, lds0 + 2048 + hw * 2048);
            if (hw == 3 && lane < 16) glds_run<1, 0, L_Q + 10240, true>(ws + WS_KMAX + h * 256, l16, lds0);
        }
        WAIT_VM(0);
        BAR();
        {
            if (kd) { if (hw == 0 && lane < 4) publish(ws, fhoff + 256, tag); }
            else {
#pragma unroll
                for (int j = 0; j < 10; ++j) ST128(*(const AS3(u32x4)*)(L + L_PH + hw * 10240 + j * 1024 + l16), WS_PHQ + ((4 * hj + hw) * 64 + rt * 4) * 2560 + j * 1024 + l16);
                WAIT_VM(0);
                if (lane == hw) publish(ws, fhoff + 256, tag);
            }
            H_CHUNK(1);
            wait_flags(ws, WS_FT + (64 * hw + lane) * 8, tag);
            WAIT_VM(0);
            BAR();
            {
                wait_flags(ws, WS_FH + (lane < 32 ? h * 64 + 32 + lane : 2 * qt + (lane & 1)) * 8, tag);
                H_CHUNK(2);
                const int e = 64 * hw + lane;
                f32x4 ks = {0.f, 0.f, 0.f, 0.f};
#pragma unroll
                for (int j = 0; j < 16; ++j) ks += __builtin_bit_cast(f32x4, LD128(WS_KVP + (h * 16 + j) * 3840 + (e < 240 ? e : 0) * 16));
                u32x4 pf[2][2]; u32x2 pg[2]; float gt[2];
#pragma unroll
                for (int t = 0; t < 2; ++t) {
                    const unsigned o = WS_PHQ + (h * 64 + qt * 8 + 2 * hw + t) * 2560;
                    pf[t][0] = LD128(o + l16); pf[t][1] = LD128(o + 1024 + l16); pg[t] = LD64(o + 2048 + lane * 8);
                    gt[t] = __uint_as_float(LD32(WS_G + (qt * 256 + (2 * hw + t) * 32 + r32) * 4));
                }
                if (e < 240) {
                    const int ph = e / 6, d = 4 * (e - ph * 6), ph_h = ph / 20, u = ph - ph_h * 20;
                    const unsigned w0 = cvtpk(ks[0], ks[1]), w1 = cvtpk(ks[2], ks[3]);
                    lds_ptr dd = (lds_ptr)lds + L_MISC + d * 96 + ph_h * 48 + u * 2;
                    *(AS3(uint16_t)*)(dd) = (uint16_t)(w0 & 0xffffu); *(AS3(uint16_t)*)(dd + 96) = (uint16_t)(w0 >> 16);
                    *(AS3(uint16_t)*)(dd + 192) = (uint16_t)(w1 & 0xffffu); *(AS3(uint16_t)*)(dd + 288) = (uint16_t)(w1 >> 16);
                }
                *(AS3(float)*)((lds_ptr)lds + L_GATE + (64 * hw + lane) * 4) = hh ? gt[1] : gt[0];
                WAIT_VM(0);
                RAW_BAR();
                H_CHUNK(3);
                const lds_cptr a = L + L_MISC + r32 * 96 + hh * 48;
                const bf16x8 a0 = LDS_B128(a), a1 = LDS_B128(a + 16); const s16x4 a2 = LDS_B64(a + 32);
#pragma unroll
                for (int t = 0; t < 2; ++t) {
                    f32x16 lo = MFMA32(a0, __builtin_bit_cast(bf16x8, pf[t][0]), f32x16{});
                    lo = MFMA32(a1, __builtin_bit_cast(bf16x8, pf[t][1]), lo);
                    lo = MFMA32K8(a2, __builtin_bit_cast(s16x4, pg[t]), lo);
                    auto rb = __builtin_amdgcn_permlane32_swap(__float_as_uint(lo[8]), __float_as_uint(lo[8]), false, false);
                    const float sb = (1.0f - gt[t]) / (__uint_as_float(rb[1]) + 1e-6f);
                    const unsigned oc = WS_OC + ((qt * 8 + 2 * hw + t) * 32 + r32) * 16;
                    ST128((u32x4{cvtpk(lo[0] * sb, lo[1] * sb), cvtpk(lo[2] * sb, lo[3] * sb), cvtpk(lo[4] * sb, lo[5] * sb), cvtpk(lo[6] * sb, lo[7] * sb)}), oc + (80 + 2 * h + hh) * (SEQ * 16));
                    if (!hh) ST64((u32x2{cvtpk(lo[8] * sb, lo[9] * sb), cvtpk(lo[10] * sb, lo[11] * sb)}), oc + (80 + 64 + (h >> 1)) * (SEQ * 16) + (h & 1) * 8);
                }
            }
            WAIT_VM(0);
            HBAR();
            if (tid == 512) { publish(ws, WS_FOB + (qt * 32 + h) * 8, tag); *(AS3(unsigned)*)((lds_ptr)lds + L_FLAG) = 0u; }
            for (int ks = hw; ks < 10; ks += 4) P3_WKS(1, ks, L_RA + ks * 5120);
            RAW_BAR();
            P3_WKS(1, 10 + hw, L_RB + hw * 5120);
            { const int ln = hw * 64 + lane; touch(ws + WS_WOP + bi * 25600 + (ln < 200 ? ln : 0) * 128, lds0 + L_SCR); }
            if (hw == 0) wait_flags(ws, WS_FOB + (xcd * 32 + r32) * 8, tag);
            WAIT_VM(0);
            RAW_BAR();
            BAR();
            const unsigned redo = *(const AS3(unsigned)*)(L + L_FLAG);
            if (__builtin_amdgcn_readfirstlane(redo) != 0u) {
                RAW_BAR();
                H_CHUNK(0); WAIT_VM(0); BAR();
                H_CHUNK(1); WAIT_VM(0); BAR();
                H_CHUNK(2); WAIT_VM(0); BAR();
                H_CHUNK(3);
                if (tid == 512) *(AS3(unsigned)*)((lds_ptr)lds + L_FLAG) = 0u;
                WAIT_VM(0); RAW_BAR();
                P3_WKS(1, 10 + hw, L_RB + hw * 5120); WAIT_VM(0);
                BAR(); BAR();
            }
        }
        if (tid == 512) publish(ws, WS_FOA + (qt * 32 + h) * 8, tag);
        for (int ks = 14 + hw; ks < 20; ks += 4) P3_WKS(1, ks, L_RC + (ks - 14) * 5120);
        WAIT_VM(0);
        BAR();
        for (int ks = hw; ks < 7; ks += 4) P3_WKS(0, ks, ks < 4 ? L_RD + ks * 5120 : L_RE + (ks - 4) * 5120);
        BAR();
        if (hw == 3) wait_flags(ws, WS_FOA + (xcd * 32 + r32) * 8, tag);
        WAIT_VM(0);
        BAR();
        BAR();
        BAR();
    } else {
        const int th = wid >> 2, cw = wid & 3;
        {
            unsigned vx[2];
#pragma unroll
            for (int j = 0; j < 2; ++j) { const int row = 8 * (wid * 2 + j) + (lane >> 3); vx[j] = row * 1280 + (((lane ^ row) & 7) << 4); }
            const unsigned lx = lds0 + wid * 2048;
            const char* pw0 = wsrc(2 * wid); const char* pw1 = wsrc(2 * wid + 1);
#define P1_X(s, slot) do { glds_run<1, 0, 0, false>(xb + KC(s) * 128, vx[0], lx + (slot) * P1_STAGE); glds_run<1, 0, 1024, false>(xb + KC(s) * 128, vx[1], lx + (slot) * P1_STAGE); \
                           glds_ptr<16384>(pw0, lx + (slot) * P1_STAGE); glds_ptr<17408>(pw1, lx + (slot) * P1_STAGE); pw0 += WSTEP(s); pw1 += WSTEP(s); } while (0)
            P1_X(0, 0);
            BAR();
            P1_X(1, 1);
            const bool t3 = cw < 3 || (hj == 0 && kd == 0);
            const int xrow = 64 * th + l15;
            lds_cptr xa0 = L + xrow * 128 + ((g ^ (xrow & 7)) << 4), xa1 = L + ((xrow * 128 + ((g ^ (xrow & 7)) << 4)) ^ 64);
            const unsigned wa0 = 16384 + 8192 * (g >> 1) + 480 * (8 * (g & 1) + q4) + 128 * (g & 1) + 40 * p4 + (cw < 3 ? 160 * cw : 32);
            const unsigned wst = cw < 3 ? 8 : 160;
            lds_cptr wa[4];
#pragma unroll
            for (int i = 0; i < 3; ++i) wa[i] = L + wa0 + i * wst;
            wa[3] = cw < 3 ? L + wa0 + 24 : L + L_GW + g * 16 + rot * 128;
            f32x4 acc[4][4];
#pragma unroll
            for (int m = 0; m < 4; ++m)
#pragma unroll
                for (int i = 0; i < 4; ++i) acc[m][i] = f32x4{0.f, 0.f, 0.f, 0.f};
            bf16x8 xA[4], wA_[4], xB[4], wB_[4];
#define P1_LOAD(X, W, ks, NT, G3) do { \
        _Pragma("unroll") for (int i = 0; i < 3; ++i) W[i] = cat8(tr_read(wa[i] + (ks) * 16384), tr_read(wa[i] + (ks) * 16384 + 1920)); \
        if (NT == 4) { if (G3) W[3] = LDS_B128(wa[3] + (ks) * 64); else W[3] = cat8(tr_read(wa[3] + (ks) * 16384), tr_read(wa[3] + (ks) * 16384 + 1920)); } \
        _Pragma("unroll") for (int m = 0; m < 4; ++m) X[m] = LDS_B128(((ks) ? xa1 : xa0) + m * 2048); } while (0)
#define P1_HALF(X, W, XN, WN, ks, NT, G3, MID1, MID2) do { \
        _Pragma("unroll") for (int i = 0; i < NT; ++i) { \
            s16x4 lo_, hi_; \
            acc[0][i] = MFMA16(W[i], X[0], acc[0][i]); SBAR(); \
            if (i == 3 && G3) WN[3] = LDS_B128(wa[3] + (ks) * 64); else lo_ = tr_read(wa[i] + (ks) * 16384); SBAR(); \
            acc[1][i] = MFMA16(W[i], X[1], acc[1][i]); SBAR(); \
            if (!(i == 3 && G3)) { hi_ = tr_read(wa[i] + (ks) * 16384 + 1920); WN[i] = cat8(lo_, hi_); } SBAR(); \
            acc[2][i] = MFMA16(W[i], X[2], acc[2][i]); SBAR(); \
            XN[i] = LDS_B128(((ks) ? xa1 : xa0) + i * 2048); SBAR(); \
            acc[3][i] = MFMA16(W[i], X[3], acc[3][i]); SBAR(); \
            if (NT == 3 && i == 2) XN[3] = LDS_B128(((ks) ? xa1 : xa0) + 3 * 2048); \
            if (i == 0) { MID1; } if (i == 1) { MID2; } SBAR(); \
        } } while (0)
#define P1_DX(lim, st, sl) do { unsigned sv_; const char* xs_ = xb + KC(st) * 128; \
                  asm volatile("s_cmp_gt_u32 %1, " #lim "\n\ts_cbranch_scc1 1f\n\ts_mov_b32 %0, m0\n\ts_mov_b32 m0, %5\n\ts_nop 0\n\tglobal_load_lds_dwordx4 %2, %4\n\t" \
                               "s_add_u32 m0, %5, 1024\n\ts_nop 0\n\tglobal_load_lds_dwordx4 %3, %4\n\ts_mov_b32 m0, %0\n1:" \
                               : "=&s"(sv_) : "s"(s), "v"(vx[0]), "v"(vx[1]), "s"(xs_), "s"(lx + (sl) * P1_STAGE) : "memory", "scc"); } while (0)
#define P1_DW(lim, st, sl) do { unsigned sv_; \
                  asm volatile("s_cmp_gt_u32 %3, " #lim "\n\ts_cbranch_scc1 1f\n\ts_mov_b32 %0, m0\n\ts_add_u32 m0, %4, 16384\n\ts_nop 0\n\tglobal_load_lds_dwordx4 %1, off\n\t" \
                               "s_add_u32 m0, %4, 17408\n\tv_lshl_add_u64 %1, %1, 0, %5\n\tglobal_load_lds_dwordx4 %2, off\n\ts_mov_b32 m0, %0\n\tv_lshl_add_u64 %2, %2, 0, %5\n1:" \
                               : "=&s"(sv_), "+v"(pw0), "+v"(pw1) : "s"(s), "s"(lx + (sl) * P1_STAGE), "s"(WSTEP(st)) : "memory", "scc"); } while (0)
#define P1_LOOP(NT, G3) do { \
            P1_LOAD(xA, wA_, 0, NT, G3); \
            int slot = 0; \
            _Pragma("nounroll") for (int s = 0; s < 10; ++s) { \
                P1_HALF(xA, wA_, xB, wB_, 1, NT, G3, P1_DX(0, 2, 2), P1_DW(0, 2, 2)); \
                  \
                asm volatile("s_cmp_gt_u32 %0, 7\n\ts_cbranch_scc1 1f\n\ts_waitcnt vmcnt(4) lgkmcnt(0)\n\ts_barrier\n\ts_branch 2f\n" \
                             "1:\n\ts_cmp_eq_u32 %0, 9\n\ts_cbranch_scc1 2f\n\ts_waitcnt vmcnt(0) lgkmcnt(0)\n\ts_barrier\n2:" :: "s"(s) : "memory", "scc"); \
                const int d = slot == 2 ? -2 * P1_STAGE : P1_STAGE; \
                xa0 += d; xa1 += d; wa[0] += d; wa[1] += d; wa[2] += d; wa[3] += G3 ? (KC(s) == 9 ? -9 * 128 : 128) : d; SBAR(); \
                P1_HALF(xB, wB_, xA, wA_, 0, NT, G3, P1_DX(6, s + 3, slot), P1_DW(6, s + 3, slot)); \
                slot = slot == 2 ? 0 : slot + 1; \
            } } while (0)
            WAIT_VM(4);
            BAR();
            if (cw < 3) P1_LOOP(4, false); else if (t3) P1_LOOP(4, true); else P1_LOOP(3, false);
            const lds_ptr img = (lds_ptr)lds + L_IMG;
            {
                const int ht = (4 * hj + g) * 64 + rt * 4 + 2 * th;
                float kn = 0.f;
                if (cw < 3) {
                    const bool isv = kd ? cw != 1 : false, toA = kd ? cw == 0 : cw < 2;
                    if (kd == 0 && cw == 0) {
#pragma unroll
                        for (int m = 0; m < 4; ++m)
#pragma unroll
                            for (int i = 0; i < 4; ++i) acc[m][i] *= QSCALE;
                    }
                    const unsigned ts = (kd == 0 && cw == 0) ? QFT : KVT, tb = ((kd == 0 && cw == 0) ? WS_QA : WS_KV + (kd ? 1536 : 0)) + ht * ts;
                    const unsigned o0 = l15 * 16, o1 = 512 + (isv ? l15 ^ 4 : l15) * 16;
#pragma unroll
                    for (int m = 0; m < 4; ++m) {
                        const u32x4 w0 = {cvtpk(acc[m][0][0], acc[m][0][1]), cvtpk(acc[m][0][2], acc[m][0][3]), cvtpk(acc[m][1][0], acc[m][1][1]), cvtpk(acc[m][1][2], acc[m][1][3])};
                        const u32x4 w1 = {cvtpk(acc[m][2][0], acc[m][2][1]), cvtpk(acc[m][2][2], acc[m][2][3]), cvtpk(acc[m][3][0], acc[m][3][1]), cvtpk(acc[m][3][2], acc[m][3][3])};
                        if (toA) { const unsigned mo = tb + (m >> 1) * ts + (m & 1) * 256; ST128(w0, mo + o0); ST128(w1, mo + o1); }
                        else { const lds_ptr d = img + (((isv ? 4 : 0) + 2 * th + (m >> 1)) * 4 + g) * 1536 + (m & 1) * 256; *(AS3(u32x4)*)(d + o0) = w0; *(AS3(u32x4)*)(d + o1) = w1; }
                    }
                    if (kd == 0 && cw == 1) {
#pragma unroll
                        for (int m = 0; m < 4; ++m) {
                            float n2 = 0.f;
#pragma unroll
                            for (int i = 0; i < 4; ++i)
#pragma unroll
                                for (int r = 0; r < 4; ++r) n2 = fmaf(acc[m][i][r], acc[m][i][r], n2);
                            kn = (n2 == n2) ? fmaxf(kn, n2) : __builtin_inff();
                        }
                    }
                } else {
#pragma unroll
                    for (int m = 0; m < 4; ++m) {
                        const unsigned ok = WS_KV + (ht + (m >> 1)) * KVT + 1024, o8 = (m & 1) * 128 + l15 * 8, o16 = (m & 1) * 256 + (l15 ^ 8) * 16;
                        const lds_ptr d = img + ((2 * th + (m >> 1)) * 4 + g) * 1536 + 1024;
                        const u32x2 w1 = {cvtpk(acc[m][1][0], acc[m][1][1]), cvtpk(acc[m][1][2], acc[m][1][3])}, w2 = {cvtpk(acc[m][2][0], acc[m][2][1]), cvtpk(acc[m][2][2], acc[m][2][3])};
                        if (kd == 0) {
                            ST64((u32x2{cvtpk(acc[m][0][0] * QSCALE, acc[m][0][1] * QSCALE), cvtpk(acc[m][0][2] * QSCALE, acc[m][0][3] * QSCALE)}), WS_QA + (ht + (m >> 1)) * QFT + 1024 + o8);
                            ST128((u32x4{w1[0], w1[1], 0x3f803f80u, 0x00003f80u}), ok + (m & 1) * 256 + l15 * 16);
                            *(AS3(u32x2)*)(d + o8) = w2;
                            float n2 = 0.f;
#pragma unroll
                            for (int r = 0; r < 4; ++r) n2 = fmaf(acc[m][1][r], acc[m][1][r], n2);
                            kn = (n2 == n2) ? fmaxf(kn, n2) : __builtin_inff();
                        } else {
                            ST128((u32x4{cvtpk(acc[m][0][0], acc[m][0][1]), cvtpk(acc[m][0][2], acc[m][0][3]), 0x00003f80u, 0u}), ok + 1536 + o16);
                            *(AS3(u32x2)*)(d + o8) = w1;
                            *(AS3(u32x4)*)(d + 16 * 1536 + o16) = u32x4{w2[0], w2[1], 0x00003f80u, 0u};
                        }
                    }
                }
                if (cw == 3 && t3 && g == 0) {
#pragma unroll
                    for (int m = 0; m < 4; ++m) ST32(__float_as_uint(1.0f / (1.0f + __expf(-acc[m][3][0]))), WS_G + (rt * 128 + 64 * th + 16 * m + l15) * 4);
                }
                if (kd == 0 && (cw & 1)) {
                    kn = row_max16(kn);
                    if (l15 == 0) ST32(__float_as_uint(kn), WS_KMAX + ((4 * hj + g) * 64 + (cw == 3 ? 32 : 0) + rt * 2 + th) * 4);
                }
            }
            u32x4 wp0; u32x2 wp1;
            {
                const int fr = (r32 & 3) + 4 * (r32 >> 3);
                const lds_cptr wp = L + L_WPH + cw * 800 + (fr < 10 ? 10 * ((r32 >> 2) & 1) + fr : 0) * 2;
                unsigned wv[12];
#pragma unroll
                for (int j = 0; j < 8; ++j) wv[j] = *(const AS3(uint16_t)*)(wp + hh * 320 + j * 40);
#pragma unroll
                for (int j = 0; j < 4; ++j) wv[8 + j] = *(const AS3(uint16_t)*)(wp + 640 + j * 40);
                const unsigned m0 = fr < 10 ? 0xffffffffu : 0u, m1 = (fr < 10 && hh == 0) ? 0xffffffffu : 0u;
                wp0 = u32x4{(wv[0] | (wv[1] << 16)) & m0, (wv[2] | (wv[3] << 16)) & m0, (wv[4] | (wv[5] << 16)) & m0, (wv[6] | (wv[7] << 16)) & m0};
                wp1 = u32x2{(wv[8] | (wv[9] << 16)) & m1, (wv[10] | (wv[11] << 16)) & m1};
            }
            RAW_BAR();
#pragma unroll
            for (int t2 = 0; t2 < 2; ++t2) {
                const lds_cptr f = L + L_IMG + ((2 * th + t2) * 4 + cw) * 1536;
                unsigned ph[10];
                feature_map(wp0, wp1, *(const AS3(u32x4)*)(f + ka), *(const AS3(u32x2)*)(f + kb), ph);
                const lds_ptr d = (lds_ptr)lds + L_PH + (cw * 4 + 2 * th + t2) * 2560;
                if (kd) {
#pragma unroll
                    for (int u = 0; u < 5; ++u) *(AS3(u32x2)*)(d + r32 * 80 + hh * 40 + u * 8) = u32x2{ph[2 * u], ph[2 * u + 1]};
                } else {
                    *(AS3(u32x4)*)(d + l16) = u32x4{ph[0], ph[1], ph[2], ph[3]}; *(AS3(u32x4)*)(d + 1024 + l16) = u32x4{ph[4], ph[5], ph[6], ph[7]};
                    *(AS3(u32x2)*)(d + 2048 + lane * 8) = u32x2{ph[8], ph[9]};
                }
                if (t2 == 0) {
                    SBAR();
                    WAIT_VM(0);
                    BAR();
                    SBAR();
                }
            }
            if (kd) {
                RAW_BAR();
#pragma unroll
                for (int k2 = 0; k2 < 2; ++k2) {
                    if (k2 == 0 || wid < 4) {
                        const int u = wid + 8 * k2, uh = u / 3, un = u - 3 * uh;
                        f32x4 c0 = {0.f, 0.f, 0.f, 0.f}, c1 = {0.f, 0.f, 0.f, 0.f};
                        const unsigned vo = L_IMG + (16 + uh) * 1536;
                        const unsigned vb = 512 * (p4 >> 1) + 256 * (g >> 1) + 64 * ((2 * (g & 1)) ^ (p4 >> 1)) + 16 * q4 + 8 * (p4 & 1);
                        const unsigned vc = 1024 + 256 * (g >> 1) + 64 * ((2 * (g & 1)) ^ 2) + 16 * q4 + (p4 ? 8 : 0);
                        const unsigned pb = L_PH + (uh * 128 + 8 * g + q4) * 80 + un * 32 + p4 * 8;
#pragma unroll
                        for (int ks = 0; ks < 4; ++ks) {
                            const bf16x8 a0 = cat8(tr_read(L + vo + vb + ks * 6144), tr_read(L + vo + (vb ^ 64) + ks * 6144));
                            const bf16x8 a1 = cat8(tr_read(L + vo + vc + ks * 6144), tr_read(L + vo + (vc ^ 64) + ks * 6144));
                            const bf16x8 b = cat8(tr_read(L + pb + ks * 2560), tr_read(L + pb + ks * 2560 + 320));
                            c0 = MFMA16(a0, b, c0); c1 = MFMA16(a1, b, c1);
                        }
                        const int ph = 16 * un + l15;
                        const unsigned off = ph < 40 ? WS_KVP + ((4 * hj + uh) * 16 + rt) * 3840 + ph * 96 + 16 * g : OOB;
                        ST128(__builtin_bit_cast(u32x4, c0), off); ST128(__builtin_bit_cast(u32x4, c1), g < 2 ? off + 64 : OOB);
                    }
                }
            }
        }
        const int qp = wid & 3, kh = wid >> 2;
        const int own = 2 * qp + kh;
        const int blk = (lane >> 4) & 1, vj = blk ? 2 : p4 >> 1;
        const unsigned kb2 = 1024 + r32 * 16 + hh * 8;
        const unsigned va = 1536 + 512 * vj + 64 * ((2 * hh) ^ vj) + 16 * q4 + (blk ? (p4 ? 8 : 0) : 8 * (p4 & 1)), vb2 = va ^ 64;
        RAW_BAR();
        {
            const char* src = ws + WS_KV + ((size_t)h * 64 + kh * 32) * KVT + qp * 6144; const unsigned dst = lds0 + kh * SLOT + qp * 6144;
            glds_run<4, 0, 0, true>(src, l16, dst); glds_run<2, 4096, 4096, true>(src, l16, dst);
        }
        WAIT_VM(0);
        BAR();
        const lds_cptr lq = L + L_Q + 2 * qp * QFT;
        const u32x4 qa0 = *(const AS3(u32x4)*)(lq + ka), qb0 = *(const AS3(u32x4)*)(lq + QFT + ka);
        u32x2 qa1 = *(const AS3(u32x2)*)(lq + kb), qb1 = *(const AS3(u32x2)*)(lq + QFT + kb);
        float kmx = *(const AS3(float)*)(L + L_Q + 10240 + lane * 4);
        if (hh) { qa1 = u32x2{0u, 0u}; qb1 = u32x2{0u, 0u}; }
        const bf16x8 QA0 = __builtin_bit_cast(bf16x8, qa0), QB0 = __builtin_bit_cast(bf16x8, qb0);
        float nba, nbb;
        {
            kmx = row_max16(kmx);
            auto r = __builtin_amdgcn_permlane16_swap(__float_as_uint(kmx), __float_as_uint(kmx), false, false);
            kmx = fmaxf(__uint_as_float(r[0]), __uint_as_float(r[1]));
            const float kn = -1.01f * __builtin_amdgcn_sqrtf(xhalf_sum(kmx));
            nba = kn * __builtin_amdgcn_sqrtf(qnorm2(qa0, qa1)); nbb = kn * __builtin_amdgcn_sqrtf(qnorm2(qb0, qb1));
        }
        for (int attempt = 0;; ++attempt) {
            f32x16 oa, ob, sA, sB;
#pragma unroll
            for (int r = 0; r < 16; ++r) { oa[r] = 0.f; ob[r] = 0.f; sB[r] = 0.f; }
            const s16x4 QA1 = __builtin_bit_cast(s16x4, hh ? split3(nba) : qa1), QB1 = __builtin_bit_cast(s16x4, hh ? split3(nbb) : qb1);
            lds_cptr pka = L + kh * SLOT + ka, pkb = L + kh * SLOT + kb2, pv = L + kh * SLOT + va, pv2 = L + kh * SLOT + vb2;
            KFrag k0 = ld_k(pka, pkb, 0), k1 = ld_k(pka, pkb, KVT);
            VFrag v0 = ld_v(pv, 0), v1 = v0;
            sA = MFMA32(k0.k0, QA0, f32x16{}); sA = MFMA32K8(k0.k1, QA1, sA);
            for (int c = 0; c < 4; ++c) {
                const int dnext = (c & 1) ? -2 * SLOT : 2 * SLOT;
#define K2_PAIR(t, LOADK0, LOADV0, LOADK1) do { \
                bf16x8 vp = v1.v1; \
                v1 = ld_v(pv, ((t) + 1) * KVT); SBAR(); \
                attn_step(sA, sB, k0, vp, v0.v0, QB0, QB1, ob, oa); \
                LOADK0; SBAR(); \
                attn_step(sB, sA, k1, v0.v1, v0.v0, QA0, QA1, oa, ob); \
                vp = v0.v1; \
                LOADV0; SBAR(); \
                attn_step(sA, sB, k1, vp, v1.v0, QB0, QB1, ob, oa); \
                LOADK1; SBAR(); \
                attn_step(sB, sA, k0, v1.v1, v1.v0, QA0, QA1, oa, ob); } while (0)
#define PRIO(p) do { asm volatile("s_cmp_eq_u32 %0, 0\n\ts_cbranch_scc1 1f\n\ts_setprio " #p "\n1:" :: "s"(kh) : "scc"); SBAR(); } while (0)
                K2_PAIR(0, k0 = ld_k(pka, pkb, 2 * KVT), v0 = ld_v(pv, 2 * KVT), k1 = ld_k(pka, pkb, 3 * KVT));
                K2_PAIR(2, k0 = ld_k(pka, pkb, 4 * KVT), v0 = ld_v(pv, 4 * KVT), k1 = ld_k(pka, pkb, 5 * KVT));
                PRIO(0);
                K2_PAIR(4, k0 = ld_k(pka, pkb, 6 * KVT), v0 = ld_v(pv, 6 * KVT), k1 = ld_k(pka, pkb, 7 * KVT));
#define TURN() do { asm volatile("s_cmp_eq_u32 %0, 3\n\ts_cbranch_scc1 1f\n\ts_waitcnt vmcnt(0) lgkmcnt(0)\n\ts_barrier\n1:" :: "s"(c) : "memory", "scc"); \
                    pka += dnext; pkb += dnext; pv += dnext; pv2 += dnext; k0 = ld_k(pka, pkb, 0); PRIO(1); } while (0)
                K2_PAIR(6, TURN(), v0 = ld_v(pv, 0), k1 = ld_k(pka, pkb, KVT));
            }
            ob = SMF(v1.v1, HI8(sB), ob);
            PRIO(0);
            {
                lds_ptr d = (lds_ptr)lds + L_X + (own ^ 1) * 3072 + l16;
#pragma unroll
                for (int a = 0; a < 3; ++a)
                    *(AS3(f32x4)*)(d + a * 1024) = kh ? f32x4{oa[4 * a], oa[4 * a + 1], oa[4 * a + 2], oa[4 * a + 3]} : f32x4{ob[4 * a], ob[4 * a + 1], ob[4 * a + 2], ob[4 * a + 3]};
            }
            RAW_BAR();
            float o[12];
#pragma unroll
            for (int a = 0; a < 3; ++a) {
                const f32x4 t = *(const AS3(f32x4)*)(L + L_X + own * 3072 + l16 + a * 1024);
#pragma unroll
                for (int r = 0; r < 4; ++r) o[4 * a + r] = t[r] + (kh ? ob[4 * a + r] : oa[4 * a + r]);
            }
            auto rr = __builtin_amdgcn_permlane32_swap(__float_as_uint(o[8]), __float_as_uint(o[8]), false, false);
            const float lsum = __uint_as_float(rr[1]);
            const float sa = *(const AS3(float)*)(L + L_GATE + (own * 32 + r32) * 4) * __builtin_amdgcn_rcpf(lsum);
            const unsigned ocb = WS_OC + ((qt * 8 + own) * 32 + r32) * 16;
            ST128((u32x4{cvtpk(o[0] * sa, o[1] * sa), cvtpk(o[2] * sa, o[3] * sa), cvtpk(o[4] * sa, o[5] * sa), cvtpk(o[6] * sa, o[7] * sa)}), ocb + (2 * h + hh) * (SEQ * 16));
            if (!hh) ST64((u32x2{cvtpk(o[8] * sa, o[9] * sa), cvtpk(o[10] * sa, o[11] * sa)}), ocb + (64 + (h >> 1)) * (SEQ * 16) + (h & 1) * 8);
            if (attempt == 0 && __any(!(lsum >= 5.4210109e-20f)))
                *(AS3(unsigned)*)((lds_ptr)lds + L_FLAG) = 1u;
            WAIT_VM(0);
            RAW_BAR();
            const unsigned redo = *(const AS3(unsigned)*)(L + L_FLAG);
            if (__builtin_amdgcn_readfirstlane(redo) == 0u) break;
            float ma = -__builtin_inff(), mb = -__builtin_inff();
            for (int t = 0; t < 64; ++t) {
                const u32x4 g0 = LD128(WS_KV + (h * 64 + t) * KVT + ka);
                const u32x2 g1 = LD64(WS_KV + (h * 64 + t) * KVT + kb2);
                f32x16 s = MFMA32(__builtin_bit_cast(bf16x8, g0), QA0, f32x16{}); s = MFMA32K8(__builtin_bit_cast(s16x4, g1), __builtin_bit_cast(s16x4, qa1), s);
                f32x16 u = MFMA32(__builtin_bit_cast(bf16x8, g0), QB0, f32x16{}); u = MFMA32K8(__builtin_bit_cast(s16x4, g1), __builtin_bit_cast(s16x4, qb1), u);
#pragma unroll
                for (int r = 0; r < 16; ++r) { ma = fmaxf(ma, s[r]); mb = fmaxf(mb, u[r]); }
            }
            nba = -xhalf_max(ma); nbb = -xhalf_max(mb);
            RAW_BAR();
            BAR();
        }
        {
            const int mt = wid & 3, kp = wid >> 2;
            const unsigned ofo = WS_OC + ((4 * kp + g) * SEQ + tt * 64 + 16 * mt + l15) * 16;
            const lds_cptr wa = L + g * 1280 + (20 * q4 + p4) * 16;
            f32x4 acc[5];
#pragma unroll
            for (int i = 0; i < 5; ++i) acc[i] = f32x4{0.f, 0.f, 0.f, 0.f};
            u32x4 of[10];
#define P3_MMA(br) do { if (kp) { _Pragma("unroll") for (int k = 0; k < 10; ++k) _Pragma("unroll") for (int i = 0; i < 5; ++i) \
                                      acc[i] = MFMA16(LDS_B128(wa + p3_off(br, 2 * k + 1) + i * 64), __builtin_bit_cast(bf16x8, of[k]), acc[i]); } \
                        else { _Pragma("unroll") for (int k = 0; k < 10; ++k) _Pragma("unroll") for (int i = 0; i < 5; ++i) \
                                      acc[i] = MFMA16(LDS_B128(wa + p3_off(br, 2 * k) + i * 64), __builtin_bit_cast(bf16x8, of[k]), acc[i]); } } while (0)
#pragma unroll
            for (int k = 0; k < 10; ++k) of[k] = LD128(ofo + (80 + 8 * k) * (SEQ * 16));
            BAR();
            P3_MMA(1);
            RAW_BAR();
            for (int ks = 7 + wid; ks < 20; ks += 8) P3_WKS(0, ks, ks < 17 ? L_RA + (ks - 7) * 5120 : L_RB + (ks - 17) * 5120);
            BAR();
#pragma unroll
            for (int k = 0; k < 10; ++k) of[k] = LD128(ofo + (8 * k) * (SEQ * 16));
            WAIT_VM(10);
            BAR();
            P3_MMA(0);
            if (kp) {
#pragma unroll
                for (int i = 0; i < 5; ++i) *(AS3(f32x4)*)((lds_ptr)lds + L_CX + (mt * 5 + i) * 1024 + l16) = acc[i];
            }
            RAW_BAR();
            if (!kp) {
                char* dst = (char*)out + (size_t)(tt * 64 + 16 * mt + l15) * 1280 + ct * 160 + g * 40;
#pragma unroll
                for (int i = 0; i < 5; ++i) {
                    const f32x4 v = acc[i] + *(const AS3(f32x4)*)(L + L_CX + (mt * 5 + i) * 1024 + l16);
                    *(u32x2*)(dst + i * 8) = u32x2{cvtpk(v[0], v[1]), cvtpk(v[2], v[3])};
                }
            }
        }
    }
    if (bid == 0 && tid == 512) publish(ws, WS_EPOCH, tag);
}

extern "C" void kernel_launch(void* const* d_in, const int* in_sizes, int n_in, void* d_out, int out_size, void* d_ws, size_t ws_size,
                              hipStream_t stream) {
    fused_kernel<<<256, 768, 0, stream>>>((const uint16_t*)d_in[0], (const uint16_t*)d_in[1], (const uint16_t*)d_in[4], (const uint16_t*)d_in[2],
                                          (const uint16_t*)d_in[6], (const uint16_t*)d_in[5], (const uint16_t*)d_in[7], (char*)d_ws, (uint16_t*)d_out);
}
```

```cpp
#include <hip/hip_runtime.h>
#include <hip/hip_bf16.h>
#include <stdint.h>

typedef __attribute__((ext_vector_type(8))) __bf16 bf16x8;
typedef __attribute__((ext_vector_type(16))) __bf16 bf16x16;
typedef __attribute__((ext_vector_type(4))) short s16x4;
typedef __attribute__((ext_vector_type(8))) short s16x8;
typedef __attribute__((ext_vector_type(4))) float f32x4;
typedef __attribute__((ext_vector_type(16))) float f32x16;
typedef __attribute__((ext_vector_type(4))) unsigned u32x4;
typedef __attribute__((ext_vector_type(2))) unsigned u32x2;
typedef __attribute__((ext_vector_type(2))) __bf16 bf16x2;
typedef __attribute__((ext_vector_type(2))) float f32x2;
typedef __attribute__((address_space(3))) const char* lds_cptr;
typedef __attribute__((address_space(3))) char* lds_ptr;
typedef unsigned long long u64;
#define AS3(T) __attribute__((address_space(3))) T

constexpr unsigned CODE_BYTES = 30000;
constexpr int SEQ = 2048, NH = 32;
constexpr unsigned OOB = 0x7ffffff0u;
constexpr float LOG2E = 1.4426950408889634f;
constexpr float QSCALE = 0.22360679774997896f * 1.4426950408889634f;

constexpr int KVT = 3072;
constexpr int QFT = 1280;
constexpr unsigned WS_KV = 0;
constexpr unsigned WS_QA = WS_KV + NH * 64 * KVT;
constexpr unsigned WS_PHQ = WS_QA + NH * 64 * QFT;
constexpr unsigned WS_WOP = WS_PHQ + NH * 64 * 2560;
constexpr unsigned WS_OC = WS_WOP + 160 * 640 * 16;
constexpr unsigned WS_KMAX = WS_OC + 160 * SEQ * 16;
constexpr unsigned WS_G = WS_KMAX + NH * 64 * 4;
constexpr unsigned WS_KVP = WS_G + SEQ * 4;
constexpr unsigned WS_FT = WS_KVP + NH * 16 * 3840;
constexpr unsigned WS_FH = WS_FT + 2048;
constexpr unsigned WS_FOB = WS_FH + 16384;
constexpr unsigned WS_FOA = WS_FOB + 2048;
constexpr unsigned WS_EPOCH = WS_FOA + 2048;
constexpr unsigned WS_END = WS_EPOCH + 128;

__device__ __forceinline__ unsigned cvtpk(float lo, float hi) {
    return __builtin_bit_cast(unsigned, __builtin_convertvector(f32x2{lo, hi}, bf16x2)); }
__device__ __forceinline__ float bflo(unsigned w) { return __uint_as_float(w << 16); }
__device__ __forceinline__ float bfhi(unsigned w) { return __uint_as_float(w & 0xffff0000u); }
#define GL1(P) "global_load_lds_dwordx4 %1, %2" P "\n\t"
#define GL2(P) GL1(P) "global_load_lds_dwordx4 %1, %2 offset:1024" P "\n\t"
#define GL3(P) GL2(P) "global_load_lds_dwordx4 %1, %2 offset:2048" P "\n\t"
#define GL4(P) GL3(P) "global_load_lds_dwordx4 %1, %2 offset:3072" P "\n\t"
#define GLASM(BODY) asm volatile("s_mov_b32 %0, m0\n\ts_add_u32 m0, %3, %4\n\ts_nop 0\n\t" BODY "s_mov_b32 m0, %0" \
                     : "=&s"(sv) : "v"(voff), "s"(t), "s"(lbase), "i"(LOFF) : "memory", "scc")
template <int N, int GOFF, int LOFF, bool SC1>
__device__ __forceinline__ void glds_run(const void* sbase, unsigned voff, unsigned lbase) {
    static_assert(N >= 1 && N <= 4, "");
    unsigned sv, tl, th;
    lbase = __builtin_amdgcn_readfirstlane(lbase);
    const unsigned lo = __builtin_amdgcn_readfirstlane((unsigned)(uintptr_t)sbase), hi = __builtin_amdgcn_readfirstlane((unsigned)((uintptr_t)sbase >> 32));
    asm volatile("s_add_u32 %0, %2, %4\n\ts_addc_u32 %1, %3, 0" : "=&s"(tl), "=s"(th) : "s"(lo), "s"(hi), "i"(GOFF) : "scc");
    const uint64_t t = ((uint64_t)th << 32) | tl;
    if constexpr (SC1) {
        if constexpr (N == 1) GLASM(GL1(" sc1")); else if constexpr (N == 2) GLASM(GL2(" sc1")); else if constexpr (N == 3) GLASM(GL3(" sc1")); else GLASM(GL4(" sc1"));
    } else {
        if constexpr (N == 1) GLASM(GL1("")); else if constexpr (N == 2) GLASM(GL2("")); else if constexpr (N == 3) GLASM(GL3("")); else GLASM(GL4(""));
    }
}
template <int LOFF>
__device__ __forceinline__ void glds_ptr(const void* p, unsigned lbase) {
    unsigned sv; lbase = __builtin_amdgcn_readfirstlane(lbase);
    asm volatile("s_mov_b32 %0, m0\n\ts_add_u32 m0, %2, %3\n\ts_nop 0\n\tglobal_load_lds_dwordx4 %1, off\n\ts_mov_b32 m0, %0"
                 : "=&s"(sv) : "v"(p), "s"(lbase), "i"(LOFF) : "memory", "scc"); }
__device__ __forceinline__ void touch(const void* p, unsigned lds_scratch) {
    unsigned sv;
    asm volatile("s_mov_b32 %0, m0\n\ts_mov_b32 m0, %2\n\ts_nop 0\n\tglobal_load_lds_dword %1, off sc1\n\ts_mov_b32 m0, %0" : "=&s"(sv) : "v"(p), "s"(lds_scratch) : "memory"); }
__device__ __forceinline__ s16x4 tr_read(lds_cptr p) { return __builtin_amdgcn_ds_read_tr16_b64_v4i16((AS3(s16x4)*)p); }
__device__ __forceinline__ bf16x8 cat8(s16x4 a, s16x4 b) {
    s16x8 r = {a[0], a[1], a[2], a[3], b[0], b[1], b[2], b[3]}; return __builtin_bit_cast(bf16x8, r); }
#define LDS_B128(p) (*(const AS3(bf16x8)*)(p))
#define LDS_B64(p) (*(const AS3(s16x4)*)(p))
#define MFMA16(a, b, c) __builtin_amdgcn_mfma_f32_16x16x32_bf16(a, b, c, 0, 0, 0)
#define MFMA32(a, b, c) __builtin_amdgcn_mfma_f32_32x32x16_bf16(a, b, c, 0, 0, 0)
#define MFMA32K8(a, b, c) __builtin_amdgcn_mfma_f32_32x32x8bf16_1k(a, b, c, 0, 0, 0)
#define WAIT_VM_(n) asm volatile("s_waitcnt vmcnt(" #n ")" ::: "memory")
#define WAIT_VM(n) WAIT_VM_(n)
#define RAW_BAR() asm volatile("s_waitcnt lgkmcnt(0)\n\ts_barrier" ::: "memory")
#define SBAR() __builtin_amdgcn_sched_barrier(0)
#define EXP2(v) __builtin_amdgcn_exp2f(v)
#define SMF(a, lo, acc) __builtin_amdgcn_smfmac_f32_32x32x32_bf16(a, __builtin_bit_cast(bf16x16, lo), acc, 0xDDDD, 0, 0)
#define LO8(s) __builtin_shufflevector(s, s, 0, 1, 2, 3, 4, 5, 6, 7)
#define HI8(s) __builtin_shufflevector(s, s, 8, 9, 10, 11, 12, 13, 14, 15)
#define ST128(v, off) __builtin_amdgcn_raw_buffer_store_b128(v, wsr, off, 0, 16)
#define ST64(v, off) __builtin_amdgcn_raw_buffer_store_b64(v, wsr, off, 0, 16)
#define ST32(v, off) __builtin_amdgcn_raw_buffer_store_b32(v, wsr, off, 0, 16)
__device__ __forceinline__ float xhalf_max(float v) {
    auto r = __builtin_amdgcn_permlane32_swap(__float_as_uint(v), __float_as_uint(v), false, false);
    return fmaxf(__uint_as_float(r[0]), __uint_as_float(r[1])); }
__device__ __forceinline__ float xhalf_min(float v) {
    auto r = __builtin_amdgcn_permlane32_swap(__float_as_uint(v), __float_as_uint(v), false, false);
    return fminf(__uint_as_float(r[0]), __uint_as_float(r[1])); }
__device__ __forceinline__ float xhalf_sum(float v) {
    auto r = __builtin_amdgcn_permlane32_swap(__float_as_uint(v), __float_as_uint(v), false, false);
    return __uint_as_float(r[0]) + __uint_as_float(r[1]); }
__device__ __forceinline__ float row_max16(float v) {
    v = fmaxf(v, __int_as_float(__builtin_amdgcn_update_dpp(0, __float_as_int(v), 0x128, 0xf, 0xf, false)));
    v = fmaxf(v, __int_as_float(__builtin_amdgcn_update_dpp(0, __float_as_int(v), 0x124, 0xf, 0xf, false)));
    v = fmaxf(v, __int_as_float(__builtin_amdgcn_update_dpp(0, __float_as_int(v), 0x122, 0xf, 0xf, false)));
    v = fmaxf(v, __int_as_float(__builtin_amdgcn_update_dpp(0, __float_as_int(v), 0x121, 0xf, 0xf, false)));
    return v; }
__device__ __forceinline__ void publish(char* ws, unsigned off, u64 tag) {
    __hip_atomic_store((u64*)(ws + off), tag, __ATOMIC_RELAXED, __HIP_MEMORY_SCOPE_AGENT); }
__device__ __forceinline__ void wait_flags(const char* ws, unsigned off, u64 tag) {
    for (int spin = 0; spin < (1 << 20); ++spin) {
        const u64 f = __hip_atomic_load((const u64*)(ws + off), __ATOMIC_RELAXED, __HIP_MEMORY_SCOPE_AGENT);
        if (__all(f == tag)) break;
        __builtin_amdgcn_s_sleep(2);
    }
    asm volatile("" ::: "memory");
}

struct KFrag { bf16x8 k0; s16x4 k1; };
struct VFrag { bf16x8 v0, v1; };
__device__ __forceinline__ KFrag ld_k(lds_cptr pa, lds_cptr pb, int off) { KFrag k; k.k0 = LDS_B128(pa + off); k.k1 = LDS_B64(pb + off); return k; }
__device__ __forceinline__ VFrag ld_v2(lds_cptr p, lds_cptr q, int off) {
    VFrag v; v.v0 = cat8(tr_read(p + off), tr_read(q + off)); v.v1 = cat8(tr_read(p + off + 256), tr_read(q + off + 256)); return v; }
#define ld_v(p, off) ld_v2(p, p##2, off)
__device__ __forceinline__ void attn_step(f32x16& sc, f32x16& sn, const KFrag& kn, const bf16x8& vprev1, const bf16x8& vc0,
                                          const bf16x8& qn0, const s16x4& qn1, f32x16& oprev, f32x16& oc) {
    oprev = SMF(vprev1, HI8(sn), oprev); SBAR();
#pragma unroll
    for (int r = 0; r < 4; ++r) sc[r] = EXP2(sc[r]);
    SBAR(); sn = MFMA32(kn.k0, qn0, f32x16{}); SBAR();
#pragma unroll
    for (int r = 4; r < 8; ++r) sc[r] = EXP2(sc[r]);
    SBAR(); sn = MFMA32K8(kn.k1, qn1, sn); SBAR();
#pragma unroll
    for (int r = 8; r < 12; ++r) sc[r] = EXP2(sc[r]);
    SBAR(); oc = SMF(vc0, LO8(sc), oc); SBAR();
#pragma unroll
    for (int r = 12; r < 16; ++r) sc[r] = EXP2(sc[r]);
    SBAR();
}
__device__ __forceinline__ void feature_map(const u32x4 wp0, const u32x2 wp1, const u32x4 f0, const u32x2 f1, unsigned (&ph)[10]) {
    f32x16 pq = MFMA32(__builtin_bit_cast(bf16x8, wp0), __builtin_bit_cast(bf16x8, f0), f32x16{});
    pq = MFMA32K8(__builtin_bit_cast(s16x4, wp1), __builtin_bit_cast(s16x4, f1), pq);
    float mx = fmaxf(pq[0], pq[1]), mn = fminf(pq[0], pq[1]);
#pragma unroll
    for (int u = 2; u < 10; u += 2) { mx = fmaxf(fmaxf(mx, pq[u]), pq[u + 1]); mn = fminf(fminf(mn, pq[u]), pq[u + 1]); }
    mx = xhalf_max(mx); mn = xhalf_min(mn);
    const f32x2 lp = {LOG2E, LOG2E}, cmx = {-mx * LOG2E, -mx * LOG2E}, cmn = {mn * LOG2E, mn * LOG2E};
    f32x2 ep[5], en[5], sp2 = {0.f, 0.f}, sn2 = {0.f, 0.f};
#pragma unroll
    for (int u = 0; u < 5; ++u) {
        const f32x2 p = {pq[2 * u], pq[2 * u + 1]};
        const f32x2 a = __builtin_elementwise_fma(p, lp, cmx), b = __builtin_elementwise_fma(p, -lp, cmn);
        ep[u] = f32x2{EXP2(a[0]), EXP2(a[1])}; en[u] = f32x2{EXP2(b[0]), EXP2(b[1])};
        sp2 += ep[u]; sn2 += en[u];
    }
    const float sp = __builtin_amdgcn_rcpf(xhalf_sum(sp2[0] + sp2[1])), sn = __builtin_amdgcn_rcpf(xhalf_sum(sn2[0] + sn2[1]));
#pragma unroll
    for (int u = 0; u < 5; ++u) {
        const f32x2 a = ep[u] * sp, b = en[u] * sn;
        ph[u] = cvtpk(a[0], a[1]); ph[5 + u] = cvtpk(b[0], b[1]);
    }
}
__device__ __forceinline__ u32x2 split3(float v) {
    const unsigned hi = cvtpk(v, 0.f) & 0xffffu; const float r1 = v - bflo(hi);
    const unsigned mid = cvtpk(r1, 0.f) & 0xffffu; const float r2 = r1 - bflo(mid);
    return u32x2{hi | (mid << 16), cvtpk(r2, 0.f) & 0xffffu};
}
__device__ __forceinline__ float qnorm2(const u32x4& a, const u32x2& b) {
    float n2 = 0.f;
#pragma unroll
    for (int j = 0; j < 4; ++j) { n2 = fmaf(bflo(a[j]), bflo(a[j]), n2); n2 = fmaf(bfhi(a[j]), bfhi(a[j]), n2); }
#pragma unroll
    for (int j = 0; j < 2; ++j) { n2 = fmaf(bflo(b[j]), bflo(b[j]), n2); n2 = fmaf(bfhi(b[j]), bfhi(b[j]), n2); }
    return xhalf_sum(n2);
}

#define LD128(off) __builtin_amdgcn_raw_buffer_load_b128(wsr, off, 0, 16)
#define LD64(off) __builtin_amdgcn_raw_buffer_load_b64(wsr, off, 0, 16)
#define LD32(off) __builtin_amdgcn_raw_buffer_load_b32(wsr, off, 0, 16)
#define BAR() asm volatile("s_barrier" ::: "memory")

__device__ __forceinline__ const char* w_source(int p, int lane, const char* g0, const char* g1, const char* g2) {
    int q = (p & 7) * 64 + lane; const int up = q >= 248; q -= up ? 248 : 0; q = q < 239 ? q : 239;
    const int row = __umul24(q, 2185) >> 16, ch = q - row * 30, sgm = __umul24(ch, 6554) >> 16;
    const uintptr_t b = sgm == 0 ? (uintptr_t)g0 : sgm == 1 ? (uintptr_t)g1 : (uintptr_t)g2;
    return (const char*)(b + (__umul24(16 * (p >> 3) + 8 * up + row, 3840) + (ch - 10 * sgm) * 16));
}

constexpr int P1_STAGE = 49152;
constexpr int L_WPH = 3 * P1_STAGE;
constexpr int L_GW = L_WPH + 3200;
constexpr int L_WO = L_GW + 1280;
constexpr int L_SCR = L_WO + 10240;
constexpr int L_FLAG = L_SCR + 256;
constexpr int LDS_BYTES = L_FLAG + 16;
constexpr int L_IMG = P1_STAGE;
constexpr int L_PH = L_IMG + 32 * 1536;
constexpr int L_Q = L_PH + 40960 + 256;
constexpr int SLOT = 8 * KVT;
constexpr int L_X = 0;
constexpr int L_MISC = L_WO;
constexpr int L_GATE = L_MISC + 3072;
constexpr int L_RA = 4 * SLOT, L_RB = SLOT, L_RC = 2 * SLOT, L_RD = 0, L_RE = L_RC + 6 * 5120;
constexpr int L_CX = L_RC;
static_assert(L_Q + 10240 + 256 <= L_WO && L_RA + 10 * 5120 <= L_WO && L_RE + 3 * 5120 <= L_RA && LDS_BYTES <= 163840, "");
__device__ __forceinline__ constexpr int p3_off(int br, int ks) {
    return br ? (ks < 10 ? L_RA + ks * 5120 : ks < 14 ? L_RB + (ks - 10) * 5120 : L_RC + (ks - 14) * 5120)
              : (ks < 4 ? L_RD + ks * 5120 : ks < 7 ? L_RE + (ks - 4) * 5120 : ks < 17 ? L_RA + (ks - 7) * 5120 : L_RB + (ks - 17) * 5120); }

__global__ __launch_bounds__(768, 3) void fused_kernel(const uint16_t* __restrict__ x, const uint16_t* __restrict__ wA, const uint16_t* __restrict__ wB,
                                                    const uint16_t* __restrict__ woA, const uint16_t* __restrict__ woB,
                                                    const uint16_t* __restrict__ wphi, const uint16_t* __restrict__ gatew,
                                                    char* __restrict__ ws, uint16_t* __restrict__ out) {
    __shared__ __attribute__((aligned(16))) char lds[LDS_BYTES];
    uint64_t pc0; asm volatile("s_getpc_b64 %0" : "=s"(pc0));
    const int bid = blockIdx.x, xcd = bid & 7, bi = bid >> 3;
    const int tid = threadIdx.x, lane = tid & 63;
    const int wid = __builtin_amdgcn_readfirstlane(tid >> 6);
    const unsigned lds0 = (unsigned)(uintptr_t)lds;
    const lds_cptr L = (lds_cptr)lds;
    const unsigned l16 = lane * 16;
    const int g = lane >> 4, l15 = lane & 15, q4 = l15 >> 2, p4 = lane & 3, r32 = lane & 31, hh = lane >> 5;
    const auto wsr = __builtin_amdgcn_make_buffer_rsrc(ws, 0, WS_END, 0x00020000);
    const int rt = (xcd & 1) * 8 + (bi & 7), kd = bi >> 4, hj = (xcd >> 1) * 2 + ((bi >> 3) & 1);
    const int h = xcd * 4 + (bi >> 3), qt = bi & 7;
    const int tt = xcd * 4 + (bi & 3), ct = bi >> 2;
    const char* xb = (const char*)x + (size_t)rt * 128 * 1280;
    const unsigned ka = lane * 16, kb = 1024 + r32 * 8;
    const char* wg0 = (const char*)wA + hj * 160 + (kd ? 2560 : 0);
    const char* wg1 = kd ? (const char*)wB + hj * 160 + 1280 : (const char*)wA + hj * 160 + 1280;
    const char* wg2 = (const char*)wB + hj * 160 + (kd ? 2560 : 0);
#define wsrc(p) w_source(p, lane, wg0, wg1, wg2)
#define P3_WKS(br, ks, dst) do { _Pragma("unroll") for (int j_ = 0; j_ < 5; ++j_) { const int q_ = j_ * 64 + lane, c_ = __umul24(q_, 52429) >> 22, n_ = q_ - c_ * 80; \
        glds_run<1, 0, 0, true>(ws + WS_WOP + ct * 1280 + ((br) * 80 + 4 * (ks)) * 10240, c_ * 10240 + n_ * 16, lds0 + (dst) + j_ * 1024); } } while (0)
    u64 tag = 0;
#define MAKE_TAG() do { u64 z = ((u64)(unsigned)__builtin_amdgcn_readfirstlane((unsigned)(tag >> 32)) << 32) | (unsigned)__builtin_amdgcn_readfirstlane((unsigned)tag); \
        z = z * 0x9E3779B97F4A7C15ull + 0x632BE59BD9B4E019ull; z ^= z >> 29; z *= 0xBF58476D1CE4E5B9ull; z ^= z >> 32; tag = z; } while (0)

    if (wid >= 8) {
        const int hw = wid - 8;
        __builtin_amdgcn_s_setprio(2);
        unsigned hbt = 0;
        if (tid == 512) *(AS3(unsigned)*)((lds_ptr)lds + L_FLAG + 4) = 0u;
#define HBAR() do { hbt += 4; asm volatile("s_waitcnt lgkmcnt(0)" ::: "memory"); \
        if (lane == 0) __hip_atomic_fetch_add((AS3(unsigned)*)((lds_ptr)lds + L_FLAG + 4), 1u, __ATOMIC_RELAXED, __HIP_MEMORY_SCOPE_WORKGROUP); \
        while (__builtin_amdgcn_readfirstlane(__hip_atomic_load((AS3(unsigned)*)((lds_ptr)lds + L_FLAG + 4), __ATOMIC_RELAXED, __HIP_MEMORY_SCOPE_WORKGROUP)) < hbt) __builtin_amdgcn_s_sleep(1); \
        asm volatile("" ::: "memory"); } while (0)
        const char* pw[4];
#pragma unroll
        for (int j = 0; j < 4; ++j) pw[j] = wsrc(16 + 4 * hw + j);
        auto wstage = [&](int slot) {
            const unsigned lw = lds0 + slot * P1_STAGE + 32768 + hw * 4096;
            glds_ptr<0>(pw[0], lw); glds_ptr<1024>(pw[1], lw); glds_ptr<2048>(pw[2], lw); glds_ptr<3072>(pw[3], lw);
#pragma unroll
            for (int j = 0; j < 4; ++j) pw[j] += 245760;
        };
        if (hj == 0 && kd == 0 && hw < 2 && hw * 1024 + l16 < 1280) glds_run<1, 0, L_GW, false>(gatew, hw * 1024 + l16, lds0 + hw * 1024);
        wstage(0);
        RAW_BAR();
        wstage(1);
        WAIT_VM(4);
        BAR();
        wstage(2);
        const char* tp = hw == 0 ? xb + ((bi >> 3) * 32 + r32) * 1280 + 3 * 128
                       : hw == 1 ? ((lane % 6) < 2 ? wg0 : (lane % 6) < 4 ? wg1 : wg2) + ((bi & 7) * 8 + (lane < 48 ? lane / 6 : 0) + 192) * 3840 + (lane & 1) * 156 : xb;
        const unsigned tstep = hw == 0 ? 128u : hw == 1 ? 245760u : 0u;
        touch(tp, lds0 + L_SCR); tp += tstep; touch(tp, lds0 + L_SCR); tp += tstep;
        for (int s = 0; s < 9; ++s) {
            if (s == 0) { WAIT_VM(6); } else if (s == 1) { WAIT_VM(7); } else if (s < 7) { WAIT_VM(6); } else if (s == 7) { WAIT_VM(10); } else { WAIT_VM(0); }
            BAR();
            if (s < 7) wstage(s % 3);
            if (s < 5) { touch(tp, lds0 + L_SCR); tp += tstep; }
            if (s == 5) { const unsigned o = (hw * 64 + lane) * 128; touch((const char*)pc0 + (o < CODE_BYTES ? o : 0u), lds0 + L_SCR); }
            if (s == 6) {
                const int cb = bid < 160 ? bid : 0, ch = cb < 80 ? cb : cb - 80;
                const int r0 = (ch < 64 ? (ch >> 1) * 20 + (ch & 1) * 4 : (ch - 64) * 40 + 16) + hw, dr = (ch < 64 ? 8 : 20) * 1280;
                const char* pr = (const char*)(cb < 80 ? woA : woB) + r0 * 1280; const unsigned lr = lds0 + hw * 1280;
                glds_run<1, 0, L_WO, false>(pr, l16, lr); glds_run<1, 0, L_WO + 5120, false>(pr + dr, l16, lr);
                if (lane < 16) { glds_run<1, 1024, L_WO + 1024, false>(pr, l16, lr); glds_run<1, 1024, L_WO + 6144, false>(pr + dr, l16, lr); }
                if (hw * 1024 + l16 < 3200) glds_run<1, 0, L_WPH, false>((const char*)wphi + hj * 3200, hw * 1024 + l16, lds0 + hw * 1024);
            }
            if (s == 7) tag = __hip_atomic_load((const u64*)(ws + WS_EPOCH), __ATOMIC_RELAXED, __HIP_MEMORY_SCOPE_AGENT);
            if (s == 8 && bid < 160) {
                const lds_cptr r = L + L_WO + q4 * 1280 + g * 32 + p4 * 8 + hw * 128;
#pragma unroll
                for (int j = 0; j < 3; ++j)
                    if (j < 2 || hw < 2) ST128(__builtin_bit_cast(u32x4, cat8(tr_read(r + j * 512), tr_read(r + j * 512 + 5120))), WS_WOP + (bid * 640 + (j * 4 + hw) * 64 + lane) * 16);
            }
        }
        MAKE_TAG();
        WAIT_VM(0);
        BAR();
        BAR();
        const unsigned fhoff = WS_FH + (((4 * hj + (lane & 3)) * 4 + kd) * 16 + rt) * 8;
        if (hw == 0 && lane < 5) publish(ws, lane < 4 ? fhoff : WS_FT + bid * 8, tag);
        if (kd) BAR();
        const char* kvbase = ws + WS_KV + (size_t)h * 64 * KVT;
        const char* kvw = kvbase + (size_t)(hw >> 1) * 4 * SLOT + (hw & 1) * 12288; const unsigned ldw = lds0 + (hw >> 1) * SLOT + (hw & 1) * 12288;
#define H_CHUNK(j) do { glds_run<4, (j) * SLOT, ((j) & 1) * 2 * SLOT, true>(kvw, l16, ldw); glds_run<4, (j) * SLOT + 4096, ((j) & 1) * 2 * SLOT + 4096, true>(kvw, l16, ldw); \
                        glds_run<4, (j) * SLOT + 8192, ((j) & 1) * 2 * SLOT + 8192, true>(kvw, l16, ldw); } while (0)
        if (hw == 0) wait_flags(ws, WS_FH + (h * 64 + r32) * 8, tag);
        BAR();
        {
            const char* pq = ws + WS_QA + ((size_t)h * 64 + qt * 8) * QFT;
            if (hw < 2) glds_run<3, 0, L_Q, true>(pq + hw * 3072, l16, lds0 + hw * 3072); else glds_run<2, 0, L_Q, true>(pq + 2048 + hw * 2048, l16, lds0 + 2048 + hw * 2048);
            if (hw == 3 && lane < 16) glds_run<1, 0, L_Q + 10240, true>(ws + WS_KMAX + h * 256, l16, lds0);
        }
        WAIT_VM(0);
        BAR();
        {
            if (kd) { if (hw == 0 && lane < 4) publish(ws, fhoff + 256, tag); }
            else {
#pragma unroll
                for (int j = 0; j < 10; ++j) ST128(*(const AS3(u32x4)*)(L + L_PH + hw * 10240 + j * 1024 + l16), WS_PHQ + ((4 * hj + hw) * 64 + rt * 4) * 2560 + j * 1024 + l16);
                WAIT_VM(0);
                if (lane == hw) publish(ws, fhoff + 256, tag);
            }
            H_CHUNK(1);
            wait_flags(ws, WS_FT + (64 * hw + lane) * 8, tag);
            WAIT_VM(0);
            BAR();
            {
                wait_flags(ws, WS_FH + (lane < 32 ? h * 64 + 32 + lane : 2 * qt + (lane & 1)) * 8, tag);
                H_CHUNK(2);
                const int e = 64 * hw + lane;
                f32x4 ks = {0.f, 0.f, 0.f, 0.f};
#pragma unroll
                for (int j = 0; j < 16; ++j) ks += __builtin_bit_cast(f32x4, LD128(WS_KVP + (h * 16 + j) * 3840 + (e < 240 ? e : 0) * 16));
                u32x4 pf[2][2]; u32x2 pg[2]; float gt[2];
#pragma unroll
                for (int t = 0; t < 2; ++t) {
                    const unsigned o = WS_PHQ + (h * 64 + qt * 8 + 2 * hw + t) * 2560;
                    pf[t][0] = LD128(o + l16); pf[t][1] = LD128(o + 1024 + l16); pg[t] = LD64(o + 2048 + lane * 8);
                    gt[t] = __uint_as_float(LD32(WS_G + (qt * 256 + (2 * hw + t) * 32 + r32) * 4));
                }
                if (e < 240) {
                    const int ph = e / 6, d = 4 * (e - ph * 6), ph_h = ph / 20, u = ph - ph_h * 20;
                    const unsigned w0 = cvtpk(ks[0], ks[1]), w1 = cvtpk(ks[2], ks[3]);
                    lds_ptr dd = (lds_ptr)lds + L_MISC + d * 96 + ph_h * 48 + u * 2;
                    *(AS3(uint16_t)*)(dd) = (uint16_t)(w0 & 0xffffu); *(AS3(uint16_t)*)(dd + 96) = (uint16_t)(w0 >> 16);
                    *(AS3(uint16_t)*)(dd + 192) = (uint16_t)(w1 & 0xffffu); *(AS3(uint16_t)*)(dd + 288) = (uint16_t)(w1 >> 16);
                }
                *(AS3(float)*)((lds_ptr)lds + L_GATE + (64 * hw + lane) * 4) = hh ? gt[1] : gt[0];
                WAIT_VM(0);
                RAW_BAR();
                H_CHUNK(3);
                const lds_cptr a = L + L_MISC + r32 * 96 + hh * 48;
                const bf16x8 a0 = LDS_B128(a), a1 = LDS_B128(a + 16); const s16x4 a2 = LDS_B64(a + 32);
#pragma unroll
                for (int t = 0; t < 2; ++t) {
                    f32x16 lo = MFMA32(a0, __builtin_bit_cast(bf16x8, pf[t][0]), f32x16{});
                    lo = MFMA32(a1, __builtin_bit_cast(bf16x8, pf[t][1]), lo);
                    lo = MFMA32K8(a2, __builtin_bit_cast(s16x4, pg[t]), lo);
                    auto rb = __builtin_amdgcn_permlane32_swap(__float_as_uint(lo[8]), __float_as_uint(lo[8]), false, false);
                    const float sb = (1.0f - gt[t]) / (__uint_as_float(rb[1]) + 1e-6f);
                    const unsigned oc = WS_OC + ((qt * 8 + 2 * hw + t) * 32 + r32) * 16;
                    ST128((u32x4{cvtpk(lo[0] * sb, lo[1] * sb), cvtpk(lo[2] * sb, lo[3] * sb), cvtpk(lo[4] * sb, lo[5] * sb), cvtpk(lo[6] * sb, lo[7] * sb)}), oc + (80 + 2 * h + hh) * (SEQ * 16));
                    if (!hh) ST64((u32x2{cvtpk(lo[8] * sb, lo[9] * sb), cvtpk(lo[10] * sb, lo[11] * sb)}), oc + (80 + 64 + (h >> 1)) * (SEQ * 16) + (h & 1) * 8);
                }
            }
            WAIT_VM(0);
            HBAR();
            if (tid == 512) { publish(ws, WS_FOB + (qt * 32 + h) * 8, tag); *(AS3(unsigned)*)((lds_ptr)lds + L_FLAG) = 0u; }
            for (int ks = hw; ks < 10; ks += 4) P3_WKS(1, ks, L_RA + ks * 5120);
            RAW_BAR();
            P3_WKS(1, 10 + hw, L_RB + hw * 5120);
            { const int ln = hw * 64 + lane; touch(ws + WS_WOP + bi * 25600 + (ln < 200 ? ln : 0) * 128, lds0 + L_SCR); }
            if (hw == 0) wait_flags(ws, WS_FOB + (xcd * 32 + r32) * 8, tag);
            WAIT_VM(0);
            RAW_BAR();
            BAR();
            const unsigned redo = *(const AS3(unsigned)*)(L + L_FLAG);
            if (__builtin_amdgcn_readfirstlane(redo) != 0u) {
                RAW_BAR();
                H_CHUNK(0); WAIT_VM(0); BAR();
                H_CHUNK(1); WAIT_VM(0); BAR();
                H_CHUNK(2); WAIT_VM(0); BAR();
                H_CHUNK(3);
                if (tid == 512) *(AS3(unsigned)*)((lds_ptr)lds + L_FLAG) = 0u;
                WAIT_VM(0); RAW_BAR();
                P3_WKS(1, 10 + hw, L_RB + hw * 5120); WAIT_VM(0);
                BAR(); BAR();
            }
        }
        if (tid == 512) publish(ws, WS_FOA + (qt * 32 + h) * 8, tag);
        for (int ks = 14 + hw; ks < 20; ks += 4) P3_WKS(1, ks, L_RC + (ks - 14) * 5120);
        WAIT_VM(0);
        BAR();
        for (int ks = hw; ks < 7; ks += 4) P3_WKS(0, ks, ks < 4 ? L_RD + ks * 5120 : L_RE + (ks - 4) * 5120);
        BAR();
        if (hw == 3) wait_flags(ws, WS_FOA + (xcd * 32 + r32) * 8, tag);
        WAIT_VM(0);
        BAR();
        BAR();
        BAR();
    } else {
        const int th = wid >> 2, cw = wid & 3;
        {
            unsigned vx[2];
#pragma unroll
            for (int j = 0; j < 2; ++j) { const int row = 8 * (wid * 2 + j) + (lane >> 3); vx[j] = row * 1280 + (((lane ^ row) & 7) << 4); }
            const unsigned lx = lds0 + wid * 2048;
            const char* pw0 = wsrc(2 * wid); const char* pw1 = wsrc(2 * wid + 1);
#define P1_X(s, slot) do { glds_run<1, 0, 0, false>(xb + (s) * 128, vx[0], lx + (slot) * P1_STAGE); glds_run<1, 0, 1024, false>(xb + (s) * 128, vx[1], lx + (slot) * P1_STAGE); \
                           glds_ptr<16384>(pw0, lx + (slot) * P1_STAGE); glds_ptr<17408>(pw1, lx + (slot) * P1_STAGE); pw0 += 245760; pw1 += 245760; } while (0)
            P1_X(0, 0);
            BAR();
            P1_X(1, 1);
            const bool t3 = cw < 3 || (hj == 0 && kd == 0);
            const int xrow = 64 * th + l15;
            lds_cptr xa0 = L + xrow * 128 + ((g ^ (xrow & 7)) << 4), xa1 = L + ((xrow * 128 + ((g ^ (xrow & 7)) << 4)) ^ 64);
            const unsigned wa0 = 16384 + 8192 * (g >> 1) + 480 * (8 * (g & 1) + q4) + 128 * (g & 1) + 40 * p4 + (cw < 3 ? 160 * cw : 32);
            const unsigned wst = cw < 3 ? 8 : 160;
            lds_cptr wa[4];
#pragma unroll
            for (int i = 0; i < 3; ++i) wa[i] = L + wa0 + i * wst;
            wa[3] = cw < 3 ? L + wa0 + 24 : L + L_GW + g * 16;
            f32x4 acc[4][4];
#pragma unroll
            for (int m = 0; m < 4; ++m)
#pragma unroll
                for (int i = 0; i < 4; ++i) acc[m][i] = f32x4{0.f, 0.f, 0.f, 0.f};
            bf16x8 xA[4], wA_[4], xB[4], wB_[4];
#define P1_LOAD(X, W, ks, NT, G3) do { \
        _Pragma("unroll") for (int i = 0; i < 3; ++i) W[i] = cat8(tr_read(wa[i] + (ks) * 16384), tr_read(wa[i] + (ks) * 16384 + 1920)); \
        if (NT == 4) { if (G3) W[3] = LDS_B128(wa[3] + (ks) * 64); else W[3] = cat8(tr_read(wa[3] + (ks) * 16384), tr_read(wa[3] + (ks) * 16384 + 1920)); } \
        _Pragma("unroll") for (int m = 0; m < 4; ++m) X[m] = LDS_B128(((ks) ? xa1 : xa0) + m * 2048); } while (0)
#define P1_HALF(X, W, XN, WN, ks, NT, G3, MID1, MID2) do { \
        _Pragma("unroll") for (int i = 0; i < NT; ++i) { \
            s16x4 lo_, hi_; \
            acc[0][i] = MFMA16(W[i], X[0], acc[0][i]); SBAR(); \
            if (i == 3 && G3) WN[3] = LDS_B128(wa[3] + (ks) * 64); else lo_ = tr_read(wa[i] + (ks) * 16384); SBAR(); \
            acc[1][i] = MFMA16(W[i], X[1], acc[1][i]); SBAR(); \
            if (!(i == 3 && G3)) { hi_ = tr_read(wa[i] + (ks) * 16384 + 1920); WN[i] = cat8(lo_, hi_); } SBAR(); \
            acc[2][i] = MFMA16(W[i], X[2], acc[2][i]); SBAR(); \
            XN[i] = LDS_B128(((ks) ? xa1 : xa0) + i * 2048); SBAR(); \
            acc[3][i] = MFMA16(W[i], X[3], acc[3][i]); SBAR(); \
            if (NT == 3 && i == 2) XN[3] = LDS_B128(((ks) ? xa1 : xa0) + 3 * 2048); \
            if (i == 0) { MID1; } if (i == 1) { MID2; } SBAR(); \
        } } while (0)
#define P1_DX(lim, st, sl) do { unsigned sv_; const char* xs_ = xb + (st) * 128; \
                  asm volatile("s_cmp_gt_u32 %1, " #lim "\n\ts_cbranch_scc1 1f\n\ts_mov_b32 %0, m0\n\ts_mov_b32 m0, %5\n\ts_nop 0\n\tglobal_load_lds_dwordx4 %2, %4\n\t" \
                               "s_add_u32 m0, %5, 1024\n\ts_nop 0\n\tglobal_load_lds_dwordx4 %3, %4\n\ts_mov_b32 m0, %0\n1:" \
                               : "=&s"(sv_) : "s"(s), "v"(vx[0]), "v"(vx[1]), "s"(xs_), "s"(lx + (sl) * P1_STAGE) : "memory", "scc"); } while (0)
#define P1_DW(lim, sl) do { unsigned sv_; \
                  asm volatile("s_cmp_gt_u32 %3, " #lim "\n\ts_cbranch_scc1 1f\n\ts_mov_b32 %0, m0\n\ts_add_u32 m0, %4, 16384\n\ts_nop 0\n\tglobal_load_lds_dwordx4 %1, off\n\t" \
                               "s_add_u32 m0, %4, 17408\n\tv_lshl_add_u64 %1, %1, 0, %5\n\tglobal_load_lds_dwordx4 %2, off\n\ts_mov_b32 m0, %0\n\tv_lshl_add_u64 %2, %2, 0, %5\n1:" \
                               : "=&s"(sv_), "+v"(pw0), "+v"(pw1) : "s"(s), "s"(lx + (sl) * P1_STAGE), "s"(245760ull) : "memory", "scc"); } while (0)
#define P1_LOOP(NT, G3) do { \
            P1_LOAD(xA, wA_, 0, NT, G3); \
            int slot = 0; \
            _Pragma("nounroll") for (int s = 0; s < 10; ++s) { \
                P1_HALF(xA, wA_, xB, wB_, 1, NT, G3, P1_DX(0, 2, 2), P1_DW(0, 2)); \
                  \
                asm volatile("s_cmp_gt_u32 %0, 7\n\ts_cbranch_scc1 1f\n\ts_waitcnt vmcnt(4) lgkmcnt(0)\n\ts_barrier\n\ts_branch 2f\n" \
                             "1:\n\ts_cmp_eq_u32 %0, 9\n\ts_cbranch_scc1 2f\n\ts_waitcnt vmcnt(0) lgkmcnt(0)\n\ts_barrier\n2:" :: "s"(s) : "memory", "scc"); \
                const int d = slot == 2 ? -2 * P1_STAGE : P1_STAGE; \
                xa0 += d; xa1 += d; wa[0] += d; wa[1] += d; wa[2] += d; wa[3] += G3 ? 128 : d; SBAR(); \
                P1_HALF(xB, wB_, xA, wA_, 0, NT, G3, P1_DX(6, s + 3, slot), P1_DW(6, slot)); \
                slot = slot == 2 ? 0 : slot + 1; \
            } } while (0)
            WAIT_VM(4);
            BAR();
            if (cw < 3) P1_LOOP(4, false); else if (t3) P1_LOOP(4, true); else P1_LOOP(3, false);
            const lds_ptr img = (lds_ptr)lds + L_IMG;
            {
                const int ht = (4 * hj + g) * 64 + rt * 4 + 2 * th;
                float kn = 0.f;
                if (cw < 3) {
                    const bool isv = kd ? cw != 1 : false, toA = kd ? cw == 0 : cw < 2;
                    if (kd == 0 && cw == 0) {
#pragma unroll
                        for (int m = 0; m < 4; ++m)
#pragma unroll
                            for (int i = 0; i < 4; ++i) acc[m][i] *= QSCALE;
                    }
                    const unsigned ts = (kd == 0 && cw == 0) ? QFT : KVT, tb = ((kd == 0 && cw == 0) ? WS_QA : WS_KV + (kd ? 1536 : 0)) + ht * ts;
                    const unsigned o0 = l15 * 16, o1 = 512 + (isv ? l15 ^ 4 : l15) * 16;
#pragma unroll
                    for (int m = 0; m < 4; ++m) {
                        const u32x4 w0 = {cvtpk(acc[m][0][0], acc[m][0][1]), cvtpk(acc[m][0][2], acc[m][0][3]), cvtpk(acc[m][1][0], acc[m][1][1]), cvtpk(acc[m][1][2], acc[m][1][3])};
                        const u32x4 w1 = {cvtpk(acc[m][2][0], acc[m][2][1]), cvtpk(acc[m][2][2], acc[m][2][3]), cvtpk(acc[m][3][0], acc[m][3][1]), cvtpk(acc[m][3][2], acc[m][3][3])};
                        if (toA) { const unsigned mo = tb + (m >> 1) * ts + (m & 1) * 256; ST128(w0, mo + o0); ST128(w1, mo + o1); }
                        else { const lds_ptr d = img + (((isv ? 4 : 0) + 2 * th + (m >> 1)) * 4 + g) * 1536 + (m & 1) * 256; *(AS3(u32x4)*)(d + o0) = w0; *(AS3(u32x4)*)(d + o1) = w1; }
                    }
                    if (kd == 0 && cw == 1) {
#pragma unroll
                        for (int m = 0; m < 4; ++m) {
                            float n2 = 0.f;
#pragma unroll
                            for (int i = 0; i < 4; ++i)
#pragma unroll
                                for (int r = 0; r < 4; ++r) n2 = fmaf(acc[m][i][r], acc[m][i][r], n2);
                            kn = (n2 == n2) ? fmaxf(kn, n2) : __builtin_inff();
                        }
                    }
                } else {
#pragma unroll
                    for (int m = 0; m < 4; ++m) {
                        const unsigned ok = WS_KV + (ht + (m >> 1)) * KVT + 1024, o8 = (m & 1) * 128 + l15 * 8, o16 = (m & 1) * 256 + (l15 ^ 8) * 16;
                        const lds_ptr d = img + ((2 * th + (m >> 1)) * 4 + g) * 1536 + 1024;
                        const u32x2 w1 = {cvtpk(acc[m][1][0], acc[m][1][1]), cvtpk(acc[m][1][2], acc[m][1][3])}, w2 = {cvtpk(acc[m][2][0], acc[m][2][1]), cvtpk(acc[m][2][2], acc[m][2][3])};
                        if (kd == 0) {
                            ST64((u32x2{cvtpk(acc[m][0][0] * QSCALE, acc[m][0][1] * QSCALE), cvtpk(acc[m][0][2] * QSCALE, acc[m][0][3] * QSCALE)}), WS_QA + (ht + (m >> 1)) * QFT + 1024 + o8);
                            ST128((u32x4{w1[0], w1[1], 0x3f803f80u, 0x00003f80u}), ok + (m & 1) * 256 + l15 * 16);
                            *(AS3(u32x2)*)(d + o8) = w2;
                            float n2 = 0.f;
#pragma unroll
                            for (int r = 0; r < 4; ++r) n2 = fmaf(acc[m][1][r], acc[m][1][r], n2);
                            kn = (n2 == n2) ? fmaxf(kn, n2) : __builtin_inff();
                        } else {
                            ST128((u32x4{cvtpk(acc[m][0][0], acc[m][0][1]), cvtpk(acc[m][0][2], acc[m][0][3]), 0x00003f80u, 0u}), ok + 1536 + o16);
                            *(AS3(u32x2)*)(d + o8) = w1;
                            *(AS3(u32x4)*)(d + 16 * 1536 + o16) = u32x4{w2[0], w2[1], 0x00003f80u, 0u};
                        }
                    }
                }
                if (cw == 3 && t3 && g == 0) {
#pragma unroll
                    for (int m = 0; m < 4; ++m) ST32(__float_as_uint(1.0f / (1.0f + __expf(-acc[m][3][0]))), WS_G + (rt * 128 + 64 * th + 16 * m + l15) * 4);
                }
                if (kd == 0 && (cw & 1)) {
                    kn = row_max16(kn);
                    if (l15 == 0) ST32(__float_as_uint(kn), WS_KMAX + ((4 * hj + g) * 64 + (cw == 3 ? 32 : 0) + rt * 2 + th) * 4);
                }
            }
            u32x4 wp0; u32x2 wp1;
            {
                const int fr = (r32 & 3) + 4 * (r32 >> 3);
                const lds_cptr wp = L + L_WPH + cw * 800 + (fr < 10 ? 10 * ((r32 >> 2) & 1) + fr : 0) * 2;
                unsigned wv[12];
#pragma unroll
                for (int j = 0; j < 8; ++j) wv[j] = *(const AS3(uint16_t)*)(wp + hh * 320 + j * 40);
#pragma unroll
                for (int j = 0; j < 4; ++j) wv[8 + j] = *(const AS3(uint16_t)*)(wp + 640 + j * 40);
                const unsigned m0 = fr < 10 ? 0xffffffffu : 0u, m1 = (fr < 10 && hh == 0) ? 0xffffffffu : 0u;
                wp0 = u32x4{(wv[0] | (wv[1] << 16)) & m0, (wv[2] | (wv[3] << 16)) & m0, (wv[4] | (wv[5] << 16)) & m0, (wv[6] | (wv[7] << 16)) & m0};
                wp1 = u32x2{(wv[8] | (wv[9] << 16)) & m1, (wv[10] | (wv[11] << 16)) & m1};
            }
            RAW_BAR();
#pragma unroll
            for (int t2 = 0; t2 < 2; ++t2) {
                const lds_cptr f = L + L_IMG + ((2 * th + t2) * 4 + cw) * 1536;
                unsigned ph[10];
                feature_map(wp0, wp1, *(const AS3(u32x4)*)(f + ka), *(const AS3(u32x2)*)(f + kb), ph);
                const lds_ptr d = (lds_ptr)lds + L_PH + (cw * 4 + 2 * th + t2) * 2560;
                if (kd) {
#pragma unroll
                    for (int u = 0; u < 5; ++u) *(AS3(u32x2)*)(d + r32 * 80 + hh * 40 + u * 8) = u32x2{ph[2 * u], ph[2 * u + 1]};
                } else {
                    *(AS3(u32x4)*)(d + l16) = u32x4{ph[0], ph[1], ph[2], ph[3]}; *(AS3(u32x4)*)(d + 1024 + l16) = u32x4{ph[4], ph[5], ph[6], ph[7]};
                    *(AS3(u32x2)*)(d + 2048 + lane * 8) = u32x2{ph[8], ph[9]};
                }
                if (t2 == 0) {
                    SBAR();
                    WAIT_VM(0);
                    BAR();
                    SBAR();
                }
            }
            if (kd) {
                RAW_BAR();
#pragma unroll
                for (int k2 = 0; k2 < 2; ++k2) {
                    if (k2 == 0 || wid < 4) {
                        const int u = wid + 8 * k2, uh = u / 3, un = u - 3 * uh;
                        f32x4 c0 = {0.f, 0.f, 0.f, 0.f}, c1 = {0.f, 0.f, 0.f, 0.f};
                        const unsigned vo = L_IMG + (16 + uh) * 1536;
                        const unsigned vb = 512 * (p4 >> 1) + 256 * (g >> 1) + 64 * ((2 * (g & 1)) ^ (p4 >> 1)) + 16 * q4 + 8 * (p4 & 1);
                        const unsigned vc = 1024 + 256 * (g >> 1) + 64 * ((2 * (g & 1)) ^ 2) + 16 * q4 + (p4 ? 8 : 0);
                        const unsigned pb = L_PH + (uh * 128 + 8 * g + q4) * 80 + un * 32 + p4 * 8;
#pragma unroll
                        for (int ks = 0; ks < 4; ++ks) {
                            const bf16x8 a0 = cat8(tr_read(L + vo + vb + ks * 6144), tr_read(L + vo + (vb ^ 64) + ks * 6144));
                            const bf16x8 a1 = cat8(tr_read(L + vo + vc + ks * 6144), tr_read(L + vo + (vc ^ 64) + ks * 6144));
                            const bf16x8 b = cat8(tr_read(L + pb + ks * 2560), tr_read(L + pb + ks * 2560 + 320));
                            c0 = MFMA16(a0, b, c0); c1 = MFMA16(a1, b, c1);
                        }
                        const int ph = 16 * un + l15;
                        const unsigned off = ph < 40 ? WS_KVP + ((4 * hj + uh) * 16 + rt) * 3840 + ph * 96 + 16 * g : OOB;
                        ST128(__builtin_bit_cast(u32x4, c0), off); ST128(__builtin_bit_cast(u32x4, c1), g < 2 ? off + 64 : OOB);
                    }
                }
            }
        }
        const int qp = wid & 3, kh = wid >> 2;
        const int own = 2 * qp + kh;
        const int blk = (lane >> 4) & 1, vj = blk ? 2 : p4 >> 1;
        const unsigned kb2 = 1024 + r32 * 16 + hh * 8;
        const unsigned va = 1536 + 512 * vj + 64 * ((2 * hh) ^ vj) + 16 * q4 + (blk ? (p4 ? 8 : 0) : 8 * (p4 & 1)), vb2 = va ^ 64;
        RAW_BAR();
        {
            const char* src = ws + WS_KV + ((size_t)h * 64 + kh * 32) * KVT + qp * 6144; const unsigned dst = lds0 + kh * SLOT + qp * 6144;
            glds_run<4, 0, 0, true>(src, l16, dst); glds_run<2, 4096, 4096, true>(src, l16, dst);
        }
        WAIT_VM(0);
        BAR();
        const lds_cptr lq = L + L_Q + 2 * qp * QFT;
        const u32x4 qa0 = *(const AS3(u32x4)*)(lq + ka), qb0 = *(const AS3(u32x4)*)(lq + QFT + ka);
        u32x2 qa1 = *(const AS3(u32x2)*)(lq + kb), qb1 = *(const AS3(u32x2)*)(lq + QFT + kb);
        float kmx = *(const AS3(float)*)(L + L_Q + 10240 + lane * 4);
        if (hh) { qa1 = u32x2{0u, 0u}; qb1 = u32x2{0u, 0u}; }
        const bf16x8 QA0 = __builtin_bit_cast(bf16x8, qa0), QB0 = __builtin_bit_cast(bf16x8, qb0);
        float nba, nbb;
        {
            kmx = row_max16(kmx);
            auto r = __builtin_amdgcn_permlane16_swap(__float_as_uint(kmx), __float_as_uint(kmx), false, false);
            kmx = fmaxf(__uint_as_float(r[0]), __uint_as_float(r[1]));
            const float kn = -1.01f * __builtin_amdgcn_sqrtf(xhalf_sum(kmx));
            nba = kn * __builtin_amdgcn_sqrtf(qnorm2(qa0, qa1)); nbb = kn * __builtin_amdgcn_sqrtf(qnorm2(qb0, qb1));
        }
        for (int attempt = 0;; ++attempt) {
            f32x16 oa, ob, sA, sB;
#pragma unroll
            for (int r = 0; r < 16; ++r) { oa[r] = 0.f; ob[r] = 0.f; sB[r] = 0.f; }
            const s16x4 QA1 = __builtin_bit_cast(s16x4, hh ? split3(nba) : qa1), QB1 = __builtin_bit_cast(s16x4, hh ? split3(nbb) : qb1);
            lds_cptr pka = L + kh * SLOT + ka, pkb = L + kh * SLOT + kb2, pv = L + kh * SLOT + va, pv2 = L + kh * SLOT + vb2;
            KFrag k0 = ld_k(pka, pkb, 0), k1 = ld_k(pka, pkb, KVT);
            VFrag v0 = ld_v(pv, 0), v1 = v0;
            sA = MFMA32(k0.k0, QA0, f32x16{}); sA = MFMA32K8(k0.k1, QA1, sA);
            for (int c = 0; c < 4; ++c) {
                const int dnext = (c & 1) ? -2 * SLOT : 2 * SLOT;
#define K2_PAIR(t, LOADK0, LOADV0, LOADK1) do { \
                bf16x8 vp = v1.v1; \
                v1 = ld_v(pv, ((t) + 1) * KVT); SBAR(); \
                attn_step(sA, sB, k0, vp, v0.v0, QB0, QB1, ob, oa); \
                LOADK0; SBAR(); \
                attn_step(sB, sA, k1, v0.v1, v0.v0, QA0, QA1, oa, ob); \
                vp = v0.v1; \
                LOADV0; SBAR(); \
                attn_step(sA, sB, k1, vp, v1.v0, QB0, QB1, ob, oa); \
                LOADK1; SBAR(); \
                attn_step(sB, sA, k0, v1.v1, v1.v0, QA0, QA1, oa, ob); } while (0)
#define PRIO(p) do { asm volatile("s_cmp_eq_u32 %0, 0\n\ts_cbranch_scc1 1f\n\ts_setprio " #p "\n1:" :: "s"(kh) : "scc"); SBAR(); } while (0)
                K2_PAIR(0, k0 = ld_k(pka, pkb, 2 * KVT), v0 = ld_v(pv, 2 * KVT), k1 = ld_k(pka, pkb, 3 * KVT));
                K2_PAIR(2, k0 = ld_k(pka, pkb, 4 * KVT), v0 = ld_v(pv, 4 * KVT), k1 = ld_k(pka, pkb, 5 * KVT));
                K2_PAIR(4, k0 = ld_k(pka, pkb, 6 * KVT), v0 = ld_v(pv, 6 * KVT), k1 = ld_k(pka, pkb, 7 * KVT));
#define TURN() do { asm volatile("s_cmp_eq_u32 %0, 3\n\ts_cbranch_scc1 1f\n\ts_waitcnt vmcnt(0) lgkmcnt(0)\n\ts_barrier\n1:" :: "s"(c) : "memory", "scc"); \
                    pka += dnext; pkb += dnext; pv += dnext; pv2 += dnext; k0 = ld_k(pka, pkb, 0);  } while (0)
                K2_PAIR(6, TURN(), v0 = ld_v(pv, 0), k1 = ld_k(pka, pkb, KVT));
            }
            ob = SMF(v1.v1, HI8(sB), ob);
            PRIO(0);
            {
                lds_ptr d = (lds_ptr)lds + L_X + (own ^ 1) * 3072 + l16;
#pragma unroll
                for (int a = 0; a < 3; ++a)
                    *(AS3(f32x4)*)(d + a * 1024) = kh ? f32x4{oa[4 * a], oa[4 * a + 1], oa[4 * a + 2], oa[4 * a + 3]} : f32x4{ob[4 * a], ob[4 * a + 1], ob[4 * a + 2], ob[4 * a + 3]};
            }
            RAW_BAR();
            float o[12];
#pragma unroll
            for (int a = 0; a < 3; ++a) {
                const f32x4 t = *(const AS3(f32x4)*)(L + L_X + own * 3072 + l16 + a * 1024);
#pragma unroll
                for (int r = 0; r < 4; ++r) o[4 * a + r] = t[r] + (kh ? ob[4 * a + r] : oa[4 * a + r]);
            }
            auto rr = __builtin_amdgcn_permlane32_swap(__float_as_uint(o[8]), __float_as_uint(o[8]), false, false);
            const float lsum = __uint_as_float(rr[1]);
            const float sa = *(const AS3(float)*)(L + L_GATE + (own * 32 + r32) * 4) * __builtin_amdgcn_rcpf(lsum);
            const unsigned ocb = WS_OC + ((qt * 8 + own) * 32 + r32) * 16;
            ST128((u32x4{cvtpk(o[0] * sa, o[1] * sa), cvtpk(o[2] * sa, o[3] * sa), cvtpk(o[4] * sa, o[5] * sa), cvtpk(o[6] * sa, o[7] * sa)}), ocb + (2 * h + hh) * (SEQ * 16));
            if (!hh) ST64((u32x2{cvtpk(o[8] * sa, o[9] * sa), cvtpk(o[10] * sa, o[11] * sa)}), ocb + (64 + (h >> 1)) * (SEQ * 16) + (h & 1) * 8);
            if (attempt == 0 && __any(!(lsum >= 5.4210109e-20f)))
                *(AS3(unsigned)*)((lds_ptr)lds + L_FLAG) = 1u;
            WAIT_VM(0);
            RAW_BAR();
            const unsigned redo = *(const AS3(unsigned)*)(L + L_FLAG);
            if (__builtin_amdgcn_readfirstlane(redo) == 0u) break;
            float ma = -__builtin_inff(), mb = -__builtin_inff();
            for (int t = 0; t < 64; ++t) {
                const u32x4 g0 = LD128(WS_KV + (h * 64 + t) * KVT + ka);
                const u32x2 g1 = LD64(WS_KV + (h * 64 + t) * KVT + kb2);
                f32x16 s = MFMA32(__builtin_bit_cast(bf16x8, g0), QA0, f32x16{}); s = MFMA32K8(__builtin_bit_cast(s16x4, g1), __builtin_bit_cast(s16x4, qa1), s);
                f32x16 u = MFMA32(__builtin_bit_cast(bf16x8, g0), QB0, f32x16{}); u = MFMA32K8(__builtin_bit_cast(s16x4, g1), __builtin_bit_cast(s16x4, qb1), u);
#pragma unroll
                for (int r = 0; r < 16; ++r) { ma = fmaxf(ma, s[r]); mb = fmaxf(mb, u[r]); }
            }
            nba = -xhalf_max(ma); nbb = -xhalf_max(mb);
            RAW_BAR();
            BAR();
        }
        {
            const int mt = wid & 3, kp = wid >> 2;
            const unsigned ofo = WS_OC + ((4 * kp + g) * SEQ + tt * 64 + 16 * mt + l15) * 16;
            const lds_cptr wa = L + g * 1280 + (20 * q4 + p4) * 16;
            f32x4 acc[5];
#pragma unroll
            for (int i = 0; i < 5; ++i) acc[i] = f32x4{0.f, 0.f, 0.f, 0.f};
            u32x4 of[10];
#define P3_MMA(br) do { if (kp) { _Pragma("unroll") for (int k = 0; k < 10; ++k) _Pragma("unroll") for (int i = 0; i < 5; ++i) \
                                      acc[i] = MFMA16(LDS_B128(wa + p3_off(br, 2 * k + 1) + i * 64), __builtin_bit_cast(bf16x8, of[k]), acc[i]); } \
                        else { _Pragma("unroll") for (int k = 0; k < 10; ++k) _Pragma("unroll") for (int i = 0; i < 5; ++i) \
                                      acc[i] = MFMA16(LDS_B128(wa + p3_off(br, 2 * k) + i * 64), __builtin_bit_cast(bf16x8, of[k]), acc[i]); } } while (0)
#pragma unroll
            for (int k = 0; k < 10; ++k) of[k] = LD128(ofo + (80 + 8 * k) * (SEQ * 16));
            BAR();
            P3_MMA(1);
            RAW_BAR();
            for (int ks = 7 + wid; ks < 20; ks += 8) P3_WKS(0, ks, ks < 17 ? L_RA + (ks - 7) * 5120 : L_RB + (ks - 17) * 5120);
            BAR();
#pragma unroll
            for (int k = 0; k < 10; ++k) of[k] = LD128(ofo + (8 * k) * (SEQ * 16));
            WAIT_VM(10);
            BAR();
            P3_MMA(0);
            if (kp) {
#pragma unroll
                for (int i = 0; i < 5; ++i) *(AS3(f32x4)*)((lds_ptr)lds + L_CX + (mt * 5 + i) * 1024 + l16) = acc[i];
            }
            RAW_BAR();
            if (!kp) {
                char* dst = (char*)out + (size_t)(tt * 64 + 16 * mt + l15) * 1280 + ct * 160 + g * 40;
#pragma unroll
                for (int i = 0; i < 5; ++i) {
                    const f32x4 v = acc[i] + *(const AS3(f32x4)*)(L + L_CX + (mt * 5 + i) * 1024 + l16);
                    *(u32x2*)(dst + i * 8) = u32x2{cvtpk(v[0], v[1]), cvtpk(v[2], v[3])};
                }
            }
        }
    }
    if (bid == 0 && tid == 512) publish(ws, WS_EPOCH, tag);
}

extern "C" void kernel_launch(void* const* d_in, const int* in_sizes, int n_in, void* d_out, int out_size, void* d_ws, size_t ws_size,
                              hipStream_t stream) {
    fused_kernel<<<256, 768, 0, stream>>>((const uint16_t*)d_in[0], (const uint16_t*)d_in[1], (const uint16_t*)d_in[4], (const uint16_t*)d_in[2],
                                          (const uint16_t*)d_in[6], (const uint16_t*)d_in[5], (const uint16_t*)d_in[7], (char*)d_ws, (uint16_t*)d_out);
}
```

```cpp
#include <hip/hip_runtime.h>
#include <hip/hip_bf16.h>
#include <stdint.h>

typedef __attribute__((ext_vector_type(8))) __bf16 bf16x8;
typedef __attribute__((ext_vector_type(16))) __bf16 bf16x16;
typedef __attribute__((ext_vector_type(4))) short s16x4;
typedef __attribute__((ext_vector_type(8))) short s16x8;
typedef __attribute__((ext_vector_type(4))) float f32x4;
typedef __attribute__((ext_vector_type(16))) float f32x16;
typedef __attribute__((ext_vector_type(4))) unsigned u32x4;
typedef __attribute__((ext_vector_type(2))) unsigned u32x2;
typedef __attribute__((ext_vector_type(2))) __bf16 bf16x2;
typedef __attribute__((ext_vector_type(2))) float f32x2;
typedef __attribute__((address_space(3))) const char* lds_cptr;
typedef __attribute__((address_space(3))) char* lds_ptr;
typedef unsigned long long u64;
#define AS3(T) __attribute__((address_space(3))) T

constexpr unsigned CODE_BYTES = 30000;
constexpr int SEQ = 2048, NH = 32;
constexpr unsigned OOB = 0x7ffffff0u;
constexpr float LOG2E = 1.4426950408889634f;
constexpr float QSCALE = 0.22360679774997896f * 1.4426950408889634f;

constexpr int KVT = 3072;
constexpr int QFT = 1280;
constexpr unsigned WS_KV = 0;
constexpr unsigned WS_QA = WS_KV + NH * 64 * KVT;
constexpr unsigned WS_PHQ = WS_QA + NH * 64 * QFT;
constexpr unsigned WS_WOP = WS_PHQ + NH * 64 * 2560;
constexpr unsigned WS_OC = WS_WOP + 160 * 640 * 16;
constexpr unsigned WS_KMAX = WS_OC + 160 * SEQ * 16;
constexpr unsigned WS_G = WS_KMAX + NH * 64 * 4;
constexpr unsigned WS_KVP = WS_G + SEQ * 4;
constexpr unsigned WS_FT = WS_KVP + NH * 16 * 3840;
constexpr unsigned WS_FH = WS_FT + 2048;
constexpr unsigned WS_FOB = WS_FH + 16384;
constexpr unsigned WS_FOA = WS_FOB + 2048;
constexpr unsigned WS_EPOCH = WS_FOA + 2048;
constexpr unsigned WS_END = WS_EPOCH + 128;

__device__ __forceinline__ unsigned cvtpk(float lo, float hi) {
    return __builtin_bit_cast(unsigned, __builtin_convertvector(f32x2{lo, hi}, bf16x2)); }
__device__ __forceinline__ float bflo(unsigned w) { return __uint_as_float(w << 16); }
__device__ __forceinline__ float bfhi(unsigned w) { return __uint_as_float(w & 0xffff0000u); }
#define GL1(P) "global_load_lds_dwordx4 %1, %2" P "\n\t"
#define GL2(P) GL1(P) "global_load_lds_dwordx4 %1, %2 offset:1024" P "\n\t"
#define GL3(P) GL2(P) "global_load_lds_dwordx4 %1, %2 offset:2048" P "\n\t"
#define GL4(P) GL3(P) "global_load_lds_dwordx4 %1, %2 offset:3072" P "\n\t"
#define GLASM(BODY) asm volatile("s_mov_b32 %0, m0\n\ts_add_u32 m0, %3, %4\n\ts_nop 0\n\t" BODY "s_mov_b32 m0, %0" \
                     : "=&s"(sv) : "v"(voff), "s"(t), "s"(lbase), "i"(LOFF) : "memory", "scc")
template <int N, int GOFF, int LOFF, bool SC1>
__device__ __forceinline__ void glds_run(const void* sbase, unsigned voff, unsigned lbase) {
    static_assert(N >= 1 && N <= 4, "");
    unsigned sv, tl, th;
    lbase = __builtin_amdgcn_readfirstlane(lbase);
    const unsigned lo = __builtin_amdgcn_readfirstlane((unsigned)(uintptr_t)sbase), hi = __builtin_amdgcn_readfirstlane((unsigned)((uintptr_t)sbase >> 32));
    asm volatile("s_add_u32 %0, %2, %4\n\ts_addc_u32 %1, %3, 0" : "=&s"(tl), "=s"(th) : "s"(lo), "s"(hi), "i"(GOFF) : "scc");
    const uint64_t t = ((uint64_t)th << 32) | tl;
    if constexpr (SC1) {
        if constexpr (N == 1) GLASM(GL1(" sc1")); else if constexpr (N == 2) GLASM(GL2(" sc1")); else if constexpr (N == 3) GLASM(GL3(" sc1")); else GLASM(GL4(" sc1"));
    } else {
        if constexpr (N == 1) GLASM(GL1("")); else if constexpr (N == 2) GLASM(GL2("")); else if constexpr (N == 3) GLASM(GL3("")); else GLASM(GL4(""));
    }
}
template <int LOFF>
__device__ __forceinline__ void glds_ptr(const void* p, unsigned lbase) {
    unsigned sv; lbase = __builtin_amdgcn_readfirstlane(lbase);
    asm volatile("s_mov_b32 %0, m0\n\ts_add_u32 m0, %2, %3\n\ts_nop 0\n\tglobal_load_lds_dwordx4 %1, off\n\ts_mov_b32 m0, %0"
                 : "=&s"(sv) : "v"(p), "s"(lbase), "i"(LOFF) : "memory", "scc"); }
__device__ __forceinline__ void touch(const void* p, unsigned lds_scratch) {
    unsigned sv;
    asm volatile("s_mov_b32 %0, m0\n\ts_mov_b32 m0, %2\n\ts_nop 0\n\tglobal_load_lds_dword %1, off sc1\n\ts_mov_b32 m0, %0" : "=&s"(sv) : "v"(p), "s"(lds_scratch) : "memory"); }
__device__ __forceinline__ s16x4 tr_read(lds_cptr p) { return __builtin_amdgcn_ds_read_tr16_b64_v4i16((AS3(s16x4)*)p); }
__device__ __forceinline__ bf16x8 cat8(s16x4 a, s16x4 b) {
    s16x8 r = {a[0], a[1], a[2], a[3], b[0], b[1], b[2], b[3]}; return __builtin_bit_cast(bf16x8, r); }
#define LDS_B128(p) (*(const AS3(bf16x8)*)(p))
#define LDS_B64(p) (*(const AS3(s16x4)*)(p))
#define MFMA16(a, b, c) __builtin_amdgcn_mfma_f32_16x16x32_bf16(a, b, c, 0, 0, 0)
#define MFMA32(a, b, c) __builtin_amdgcn_mfma_f32_32x32x16_bf16(a, b, c, 0, 0, 0)
#define MFMA32K8(a, b, c) __builtin_amdgcn_mfma_f32_32x32x8bf16_1k(a, b, c, 0, 0, 0)
#define WAIT_VM_(n) asm volatile("s_waitcnt vmcnt(" #n ")" ::: "memory")
#define WAIT_VM(n) WAIT_VM_(n)
#define RAW_BAR() asm volatile("s_waitcnt lgkmcnt(0)\n\ts_barrier" ::: "memory")
#define SBAR() __builtin_amdgcn_sched_barrier(0)
#define EXP2(v) __builtin_amdgcn_exp2f(v)
#define SMF(a, lo, acc) __builtin_amdgcn_smfmac_f32_32x32x32_bf16(a, __builtin_bit_cast(bf16x16, lo), acc, 0xDDDD, 0, 0)
#define LO8(s) __builtin_shufflevector(s, s, 0, 1, 2, 3, 4, 5, 6, 7)
#define HI8(s) __builtin_shufflevector(s, s, 8, 9, 10, 11, 12, 13, 14, 15)
#define ST128(v, off) __builtin_amdgcn_raw_buffer_store_b128(v, wsr, off, 0, 16)
#define ST64(v, off) __builtin_amdgcn_raw_buffer_store_b64(v, wsr, off, 0, 16)
#define ST32(v, off) __builtin_amdgcn_raw_buffer_store_b32(v, wsr, off, 0, 16)
__device__ __forceinline__ float xhalf_max(float v) {
    auto r = __builtin_amdgcn_permlane32_swap(__float_as_uint(v), __float_as_uint(v), false, false);
    return fmaxf(__uint_as_float(r[0]), __uint_as_float(r[1])); }
__device__ __forceinline__ float xhalf_min(float v) {
    auto r = __builtin_amdgcn_permlane32_swap(__float_as_uint(v), __float_as_uint(v), false, false);
    return fminf(__uint_as_float(r[0]), __uint_as_float(r[1])); }
__device__ __forceinline__ float xhalf_sum(float v) {
    auto r = __builtin_amdgcn_permlane32_swap(__float_as_uint(v), __float_as_uint(v), false, false);
    return __uint_as_float(r[0]) + __uint_as_float(r[1]); }
__device__ __forceinline__ float row_max16(float v) {
    v = fmaxf(v, __int_as_float(__builtin_amdgcn_update_dpp(0, __float_as_int(v), 0x128, 0xf, 0xf, false)));
    v = fmaxf(v, __int_as_float(__builtin_amdgcn_update_dpp(0, __float_as_int(v), 0x124, 0xf, 0xf, false)));
    v = fmaxf(v, __int_as_float(__builtin_amdgcn_update_dpp(0, __float_as_int(v), 0x122, 0xf, 0xf, false)));
    v = fmaxf(v, __int_as_float(__builtin_amdgcn_update_dpp(0, __float_as_int(v), 0x121, 0xf, 0xf, false)));
    return v; }
__device__ __forceinline__ void publish(char* ws, unsigned off, u64 tag) {
    __hip_atomic_store((u64*)(ws + off), tag, __ATOMIC_RELAXED, __HIP_MEMORY_SCOPE_AGENT); }
__device__ __forceinline__ void wait_flags(const char* ws, unsigned off, u64 tag) {
    for (int spin = 0; spin < (1 << 20); ++spin) {
        const u64 f = __hip_atomic_load((const u64*)(ws + off), __ATOMIC_RELAXED, __HIP_MEMORY_SCOPE_AGENT);
        if (__all(f == tag)) break;
        __builtin_amdgcn_s_sleep(4);
    }
    asm volatile("" ::: "memory");
}

struct KFrag { bf16x8 k0; s16x4 k1; };
struct VFrag { bf16x8 v0, v1; };
__device__ __forceinline__ KFrag ld_k(lds_cptr pa, lds_cptr pb, int off) { KFrag k; k.k0 = LDS_B128(pa + off); k.k1 = LDS_B64(pb + off); return k; }
__device__ __forceinline__ VFrag ld_v2(lds_cptr p, lds_cptr q, int off) {
    VFrag v; v.v0 = cat8(tr_read(p + off), tr_read(q + off)); v.v1 = cat8(tr_read(p + off + 256), tr_read(q + off + 256)); return v; }
#define ld_v(p, off) ld_v2(p, p##2, off)
__device__ __forceinline__ void attn_step(f32x16& sc, f32x16& sn, const KFrag& kn, const bf16x8& vprev1, const bf16x8& vc0,
                                          const bf16x8& qn0, const s16x4& qn1, f32x16& oprev, f32x16& oc) {
    oprev = SMF(vprev1, HI8(sn), oprev); SBAR();
#pragma unroll
    for (int r = 0; r < 4; ++r) sc[r] = EXP2(sc[r]);
    SBAR(); sn = MFMA32(kn.k0, qn0, f32x16{}); SBAR();
#pragma unroll
    for (int r = 4; r < 8; ++r) sc[r] = EXP2(sc[r]);
    SBAR(); sn = MFMA32K8(kn.k1, qn1, sn); SBAR();
#pragma unroll
    for (int r = 8; r < 12; ++r) sc[r] = EXP2(sc[r]);
    SBAR(); oc = SMF(vc0, LO8(sc), oc); SBAR();
#pragma unroll
    for (int r = 12; r < 16; ++r) sc[r] = EXP2(sc[r]);
    SBAR();
}
__device__ __forceinline__ void feature_map(const u32x4 wp0, const u32x2 wp1, const u32x4 f0, const u32x2 f1, unsigned (&ph)[10]) {
    f32x16 pq = MFMA32(__builtin_bit_cast(bf16x8, wp0), __builtin_bit_cast(bf16x8, f0), f32x16{});
    pq = MFMA32K8(__builtin_bit_cast(s16x4, wp1), __builtin_bit_cast(s16x4, f1), pq);
    float mx = fmaxf(pq[0], pq[1]), mn = fminf(pq[0], pq[1]);
#pragma unroll
    for (int u = 2; u < 10; u += 2) { mx = fmaxf(fmaxf(mx, pq[u]), pq[u + 1]); mn = fminf(fminf(mn, pq[u]), pq[u + 1]); }
    mx = xhalf_max(mx); mn = xhalf_min(mn);
    const f32x2 lp = {LOG2E, LOG2E}, cmx = {-mx * LOG2E, -mx * LOG2E}, cmn = {mn * LOG2E, mn * LOG2E};
    f32x2 ep[5], en[5], sp2 = {0.f, 0.f}, sn2 = {0.f, 0.f};
#pragma unroll
    for (int u = 0; u < 5; ++u) {
        const f32x2 p = {pq[2 * u], pq[2 * u + 1]};
        const f32x2 a = __builtin_elementwise_fma(p, lp, cmx), b = __builtin_elementwise_fma(p, -lp, cmn);
        ep[u] = f32x2{EXP2(a[0]), EXP2(a[1])}; en[u] = f32x2{EXP2(b[0]), EXP2(b[1])};
        sp2 += ep[u]; sn2 += en[u];
    }
    const float sp = __builtin_amdgcn_rcpf(xhalf_sum(sp2[0] + sp2[1])), sn = __builtin_amdgcn_rcpf(xhalf_sum(sn2[0] + sn2[1]));
#pragma unroll
    for (int u = 0; u < 5; ++u) {
        const f32x2 a = ep[u] * sp, b = en[u] * sn;
        ph[u] = cvtpk(a[0], a[1]); ph[5 + u] = cvtpk(b[0], b[1]);
    }
}
__device__ __forceinline__ u32x2 split3(float v) {
    const unsigned hi = cvtpk(v, 0.f) & 0xffffu; const float r1 = v - bflo(hi);
    const unsigned mid = cvtpk(r1, 0.f) & 0xffffu; const float r2 = r1 - bflo(mid);
    return u32x2{hi | (mid << 16), cvtpk(r2, 0.f) & 0xffffu};
}
__device__ __forceinline__ float qnorm2(const u32x4& a, const u32x2& b) {
    float n2 = 0.f;
#pragma unroll
    for (int j = 0; j < 4; ++j) { n2 = fmaf(bflo(a[j]), bflo(a[j]), n2); n2 = fmaf(bfhi(a[j]), bfhi(a[j]), n2); }
#pragma unroll
    for (int j = 0; j < 2; ++j) { n2 = fmaf(bflo(b[j]), bflo(b[j]), n2); n2 = fmaf(bfhi(b[j]), bfhi(b[j]), n2); }
    return xhalf_sum(n2);
}

#define LD128(off) __builtin_amdgcn_raw_buffer_load_b128(wsr, off, 0, 16)
#define LD64(off) __builtin_amdgcn_raw_buffer_load_b64(wsr, off, 0, 16)
#define LD32(off) __builtin_amdgcn_raw_buffer_load_b32(wsr, off, 0, 16)
#define BAR() asm volatile("s_barrier" ::: "memory")

__device__ __forceinline__ const char* w_source(int p, int lane, const char* g0, const char* g1, const char* g2) {
    int q = (p & 7) * 64 + lane; const int up = q >= 248; q -= up ? 248 : 0; q = q < 239 ? q : 239;
    const int row = __umul24(q, 2185) >> 16, ch = q - row * 30, sgm = __umul24(ch, 6554) >> 16;
    const uintptr_t b = sgm == 0 ? (uintptr_t)g0 : sgm == 1 ? (uintptr_t)g1 : (uintptr_t)g2;
    return (const char*)(b + (__umul24(16 * (p >> 3) + 8 * up + row, 3840) + (ch - 10 * sgm) * 16));
}

constexpr int P1_STAGE = 49152;
constexpr int L_WPH = 3 * P1_STAGE;
constexpr int L_GW = L_WPH + 3200;
constexpr int L_WO = L_GW + 1280;
constexpr int L_SCR = L_WO + 10240;
constexpr int L_FLAG = L_SCR + 256;
constexpr int LDS_BYTES = L_FLAG + 16;
constexpr int L_IMG = P1_STAGE;
constexpr int L_PH = L_IMG + 32 * 1536;
constexpr int L_Q = L_PH + 40960 + 256;
constexpr int SLOT = 8 * KVT;
constexpr int L_X = 0;
constexpr int L_MISC = L_WO;
constexpr int L_GATE = L_MISC + 3072;
constexpr int L_RA = 4 * SLOT, L_RB = SLOT, L_RC = 2 * SLOT, L_RD = 0, L_RE = L_RC + 6 * 5120;
constexpr int L_CX = L_RC;
static_assert(L_Q + 10240 + 256 <= L_WO && L_RA + 10 * 5120 <= L_WO && L_RE + 3 * 5120 <= L_RA && LDS_BYTES <= 163840, "");
__device__ __forceinline__ constexpr int p3_off(int br, int ks) {
    return br ? (ks < 10 ? L_RA + ks * 5120 : ks < 14 ? L_RB + (ks - 10) * 5120 : L_RC + (ks - 14) * 5120)
              : (ks < 4 ? L_RD + ks * 5120 : ks < 7 ? L_RE + (ks - 4) * 5120 : ks < 17 ? L_RA + (ks - 7) * 5120 : L_RB + (ks - 17) * 5120); }

__global__ __launch_bounds__(768, 3) void fused_kernel(const uint16_t* __restrict__ x, const uint16_t* __restrict__ wA, const uint16_t* __restrict__ wB,
                                                    const uint16_t* __restrict__ woA, const uint16_t* __restrict__ woB,
                                                    const uint16_t* __restrict__ wphi, const uint16_t* __restrict__ gatew,
                                                    char* __restrict__ ws, uint16_t* __restrict__ out) {
    __shared__ __attribute__((aligned(16))) char lds[LDS_BYTES];
    uint64_t pc0; asm volatile("s_getpc_b64 %0" : "=s"(pc0));
    const int bid = blockIdx.x, xcd = bid & 7, bi = bid >> 3;
    const int tid = threadIdx.x, lane = tid & 63;
    const int wid = __builtin_amdgcn_readfirstlane(tid >> 6);
    const unsigned lds0 = (unsigned)(uintptr_t)lds;
    const lds_cptr L = (lds_cptr)lds;
    const unsigned l16 = lane * 16;
    const int g = lane >> 4, l15 = lane & 15, q4 = l15 >> 2, p4 = lane & 3, r32 = lane & 31, hh = lane >> 5;
    const auto wsr = __builtin_amdgcn_make_buffer_rsrc(ws, 0, WS_END, 0x00020000);
    const int rt = (xcd & 1) * 8 + (bi & 7), kd = bi >> 4, hj = (xcd >> 1) * 2 + ((bi >> 3) & 1);
    const int h = xcd * 4 + (bi >> 3), qt = bi & 7;
    const int tt = xcd * 4 + (bi & 3), ct = bi >> 2;
    const char* xb = (const char*)x + (size_t)rt * 128 * 1280;
    const unsigned ka = lane * 16, kb = 1024 + r32 * 8;
    const char* wg0 = (const char*)wA + hj * 160 + (kd ? 2560 : 0);
    const char* wg1 = kd ? (const char*)wB + hj * 160 + 1280 : (const char*)wA + hj * 160 + 1280;
    const char* wg2 = (const char*)wB + hj * 160 + (kd ? 2560 : 0);
#define wsrc(p) w_source(p, lane, wg0, wg1, wg2)
#define P3_WKS(br, ks, dst) do { _Pragma("unroll") for (int j_ = 0; j_ < 5; ++j_) { const int q_ = j_ * 64 + lane, c_ = __umul24(q_, 52429) >> 22, n_ = q_ - c_ * 80; \
        glds_run<1, 0, 0, true>(ws + WS_WOP + ct * 1280 + ((br) * 80 + 4 * (ks)) * 10240, c_ * 10240 + n_ * 16, lds0 + (dst) + j_ * 1024); } } while (0)
    u64 tag = 0;
#define MAKE_TAG() do { u64 z = ((u64)(unsigned)__builtin_amdgcn_readfirstlane((unsigned)(tag >> 32)) << 32) | (unsigned)__builtin_amdgcn_readfirstlane((unsigned)tag); \
        z = z * 0x9E3779B97F4A7C15ull + 0x632BE59BD9B4E019ull; z ^= z >> 29; z *= 0xBF58476D1CE4E5B9ull; z ^= z >> 32; tag = z; } while (0)

    if (wid >= 8) {
        const int hw = wid - 8;
        __builtin_amdgcn_s_setprio(2);
        unsigned hbt = 0;
        if (tid == 512) *(AS3(unsigned)*)((lds_ptr)lds + L_FLAG + 4) = 0u;
#define HBAR() do { hbt += 4; asm volatile("s_waitcnt lgkmcnt(0)" ::: "memory"); \
        if (lane == 0) __hip_atomic_fetch_add((AS3(unsigned)*)((lds_ptr)lds + L_FLAG + 4), 1u, __ATOMIC_RELAXED, __HIP_MEMORY_SCOPE_WORKGROUP); \
        while (__builtin_amdgcn_readfirstlane(__hip_atomic_load((AS3(unsigned)*)((lds_ptr)lds + L_FLAG + 4), __ATOMIC_RELAXED, __HIP_MEMORY_SCOPE_WORKGROUP)) < hbt) __builtin_amdgcn_s_sleep(1); \
        asm volatile("" ::: "memory"); } while (0)
        const char* pw[4];
#pragma unroll
        for (int j = 0; j < 4; ++j) pw[j] = wsrc(16 + 4 * hw + j);
        auto wstage = [&](int slot) {
            const unsigned lw = lds0 + slot * P1_STAGE + 32768 + hw * 4096;
            glds_ptr<0>(pw[0], lw); glds_ptr<1024>(pw[1], lw); glds_ptr<2048>(pw[2], lw); glds_ptr<3072>(pw[3], lw);
#pragma unroll
            for (int j = 0; j < 4; ++j) pw[j] += 245760;
        };
        if (hj == 0 && kd == 0 && hw < 2 && hw * 1024 + l16 < 1280) glds_run<1, 0, L_GW, false>(gatew, hw * 1024 + l16, lds0 + hw * 1024);
        wstage(0);
        RAW_BAR();
        wstage(1);
        WAIT_VM(4);
        BAR();
        wstage(2);
        const char* tp = hw == 0 ? xb + ((bi >> 3) * 32 + r32) * 1280 + 3 * 128
                       : hw == 1 ? ((lane % 6) < 2 ? wg0 : (lane % 6) < 4 ? wg1 : wg2) + ((bi & 7) * 8 + (lane < 48 ? lane / 6 : 0) + 192) * 3840 + (lane & 1) * 156 : xb;
        const unsigned tstep = hw == 0 ? 128u : hw == 1 ? 245760u : 0u;
        touch(tp, lds0 + L_SCR); tp += tstep; touch(tp, lds0 + L_SCR); tp += tstep;
        for (int s = 0; s < 9; ++s) {
            if (s == 0) { WAIT_VM(6); } else if (s == 1) { WAIT_VM(7); } else if (s < 7) { WAIT_VM(6); } else if (s == 7) { WAIT_VM(10); } else { WAIT_VM(0); }
            BAR();
            if (s < 7) wstage(s % 3);
            if (s < 5) { touch(tp, lds0 + L_SCR); tp += tstep; }
            if (s == 5) { const unsigned o = (hw * 64 + lane) * 128; touch((const char*)pc0 + (o < CODE_BYTES ? o : 0u), lds0 + L_SCR); }
            if (s == 6) {
                const int cb = bid < 160 ? bid : 0, ch = cb < 80 ? cb : cb - 80;
                const int r0 = (ch < 64 ? (ch >> 1) * 20 + (ch & 1) * 4 : (ch - 64) * 40 + 16) + hw, dr = (ch < 64 ? 8 : 20) * 1280;
                const char* pr = (const char*)(cb < 80 ? woA : woB) + r0 * 1280; const unsigned lr = lds0 + hw * 1280;
                glds_run<1, 0, L_WO, false>(pr, l16, lr); glds_run<1, 0, L_WO + 5120, false>(pr + dr, l16, lr);
                if (lane < 16) { glds_run<1, 1024, L_WO + 1024, false>(pr, l16, lr); glds_run<1, 1024, L_WO + 6144, false>(pr + dr, l16, lr); }
                if (hw * 1024 + l16 < 3200) glds_run<1, 0, L_WPH, false>((const char*)wphi + hj * 3200, hw * 1024 + l16, lds0 + hw * 1024);
            }
            if (s == 7) tag = __hip_atomic_load((const u64*)(ws + WS_EPOCH), __ATOMIC_RELAXED, __HIP_MEMORY_SCOPE_AGENT);
            if (s == 8 && bid < 160) {
                const lds_cptr r = L + L_WO + q4 * 1280 + g * 32 + p4 * 8 + hw * 128;
#pragma unroll
                for (int j = 0; j < 3; ++j)
                    if (j < 2 || hw < 2) ST128(__builtin_bit_cast(u32x4, cat8(tr_read(r + j * 512), tr_read(r + j * 512 + 5120))), WS_WOP + (bid * 640 + (j * 4 + hw) * 64 + lane) * 16);
            }
        }
        BAR();
        MAKE_TAG();
        BAR();
        const unsigned fhoff = WS_FH + (((4 * hj + (lane & 3)) * 4 + kd) * 16 + rt) * 8;
        if (hw == 0 && lane < 4) publish(ws, fhoff, tag);
        if (kd) BAR();
        const char* kvbase = ws + WS_KV + (size_t)h * 64 * KVT;
        const char* kvw = kvbase + (size_t)(hw >> 1) * 4 * SLOT + (hw & 1) * 12288; const unsigned ldw = lds0 + (hw >> 1) * SLOT + (hw & 1) * 12288;
#define H_CHUNK(j) do { glds_run<4, (j) * SLOT, ((j) & 1) * 2 * SLOT, true>(kvw, l16, ldw); glds_run<4, (j) * SLOT + 4096, ((j) & 1) * 2 * SLOT + 4096, true>(kvw, l16, ldw); \
                        glds_run<4, (j) * SLOT + 8192, ((j) & 1) * 2 * SLOT + 8192, true>(kvw, l16, ldw); } while (0)
        if (hw == 0) wait_flags(ws, WS_FH + (h * 64 + r32) * 8, tag);
        BAR();
        {
            const char* pq = ws + WS_QA + ((size_t)h * 64 + qt * 8) * QFT;
            if (hw < 2) glds_run<3, 0, L_Q, true>(pq + hw * 3072, l16, lds0 + hw * 3072); else glds_run<2, 0, L_Q, true>(pq + 2048 + hw * 2048, l16, lds0 + 2048 + hw * 2048);
            if (hw == 3 && lane < 16) glds_run<1, 0, L_Q + 10240, true>(ws + WS_KMAX + h * 256, l16, lds0);
        }
        WAIT_VM(0);
        BAR();
        if (tid == 512) publish(ws, WS_FT + bid * 8, tag);
        {
            if (kd) {
#pragma unroll
                for (int j = 0; j < 4; ++j) ST128(*(const AS3(u32x4)*)(L + L_IMG + hw * 4096 + j * 1024 + l16), (j < 3 || lane < 48) ? WS_KVP + ((4 * hj + hw) * 16 + rt) * 3840 + j * 1024 + l16 : OOB);
                HBAR();
            } else {
#pragma unroll
                for (int j = 0; j < 10; ++j) ST128(*(const AS3(u32x4)*)(L + L_PH + hw * 10240 + j * 1024 + l16), WS_PHQ + ((4 * hj + hw) * 64 + rt * 4) * 2560 + j * 1024 + l16);
            }
            H_CHUNK(1);
            WAIT_VM(0);
            if (lane == hw) publish(ws, fhoff + 256, tag);
            BAR();
            {
                wait_flags(ws, WS_FH + (lane < 32 ? h * 64 + 32 + lane : 2 * qt + (lane & 1)) * 8, tag);
                H_CHUNK(2);
                const int e = 64 * hw + lane;
                f32x4 ks = {0.f, 0.f, 0.f, 0.f};
#pragma unroll
                for (int j = 0; j < 16; ++j) ks += __builtin_bit_cast(f32x4, LD128(WS_KVP + (h * 16 + j) * 3840 + (e < 240 ? e : 0) * 16));
                u32x4 pf[2][2]; u32x2 pg[2]; float gt[2];
#pragma unroll
                for (int t = 0; t < 2; ++t) {
                    const unsigned o = WS_PHQ + (h * 64 + qt * 8 + 2 * hw + t) * 2560;
                    pf[t][0] = LD128(o + l16); pf[t][1] = LD128(o + 1024 + l16); pg[t] = LD64(o + 2048 + lane * 8);
                    gt[t] = __uint_as_float(LD32(WS_G + (qt * 256 + (2 * hw + t) * 32 + r32) * 4));
                }
                if (e < 240) {
                    const int ph = e / 6, d = 4 * (e - ph * 6), ph_h = ph / 20, u = ph - ph_h * 20;
                    const unsigned w0 = cvtpk(ks[0], ks[1]), w1 = cvtpk(ks[2], ks[3]);
                    lds_ptr dd = (lds_ptr)lds + L_MISC + d * 96 + ph_h * 48 + u * 2;
                    *(AS3(uint16_t)*)(dd) = (uint16_t)(w0 & 0xffffu); *(AS3(uint16_t)*)(dd + 96) = (uint16_t)(w0 >> 16);
                    *(AS3(uint16_t)*)(dd + 192) = (uint16_t)(w1 & 0xffffu); *(AS3(uint16_t)*)(dd + 288) = (uint16_t)(w1 >> 16);
                }
                *(AS3(float)*)((lds_ptr)lds + L_GATE + (64 * hw + lane) * 4) = hh ? gt[1] : gt[0];
                wait_flags(ws, WS_FT + (64 * hw + lane) * 8, tag);
                WAIT_VM(0);
                RAW_BAR();
                H_CHUNK(3);
                const lds_cptr a = L + L_MISC + r32 * 96 + hh * 48;
                const bf16x8 a0 = LDS_B128(a), a1 = LDS_B128(a + 16); const s16x4 a2 = LDS_B64(a + 32);
#pragma unroll
                for (int t = 0; t < 2; ++t) {
                    f32x16 lo = MFMA32(a0, __builtin_bit_cast(bf16x8, pf[t][0]), f32x16{});
                    lo = MFMA32(a1, __builtin_bit_cast(bf16x8, pf[t][1]), lo);
                    lo = MFMA32K8(a2, __builtin_bit_cast(s16x4, pg[t]), lo);
                    auto rb = __builtin_amdgcn_permlane32_swap(__float_as_uint(lo[8]), __float_as_uint(lo[8]), false, false);
                    const float sb = (1.0f - gt[t]) / (__uint_as_float(rb[1]) + 1e-6f);
                    const unsigned oc = WS_OC + ((qt * 8 + 2 * hw + t) * 32 + r32) * 16;
                    ST128((u32x4{cvtpk(lo[0] * sb, lo[1] * sb), cvtpk(lo[2] * sb, lo[3] * sb), cvtpk(lo[4] * sb, lo[5] * sb), cvtpk(lo[6] * sb, lo[7] * sb)}), oc + (80 + 2 * h + hh) * (SEQ * 16));
                    if (!hh) ST64((u32x2{cvtpk(lo[8] * sb, lo[9] * sb), cvtpk(lo[10] * sb, lo[11] * sb)}), oc + (80 + 64 + (h >> 1)) * (SEQ * 16) + (h & 1) * 8);
                }
            }
            WAIT_VM(0);
            HBAR();
            if (tid == 512) { publish(ws, WS_FOB + (qt * 32 + h) * 8, tag); *(AS3(unsigned)*)((lds_ptr)lds + L_FLAG) = 0u; }
            for (int ks = hw; ks < 10; ks += 4) P3_WKS(1, ks, L_RA + ks * 5120);
            RAW_BAR();
            P3_WKS(1, 10 + hw, L_RB + hw * 5120);
            { const int ln = hw * 64 + lane; touch(ws + WS_WOP + bi * 25600 + (ln < 200 ? ln : 0) * 128, lds0 + L_SCR); }
            if (hw == 0) wait_flags(ws, WS_FOB + (xcd * 32 + r32) * 8, tag);
            WAIT_VM(0);
            RAW_BAR();
            BAR();
            const unsigned redo = *(const AS3(unsigned)*)(L + L_FLAG);
            if (__builtin_amdgcn_readfirstlane(redo) != 0u) {
                RAW_BAR();
                H_CHUNK(0); WAIT_VM(0); BAR();
                H_CHUNK(1); WAIT_VM(0); BAR();
                H_CHUNK(2); WAIT_VM(0); BAR();
                H_CHUNK(3);
                if (tid == 512) *(AS3(unsigned)*)((lds_ptr)lds + L_FLAG) = 0u;
                WAIT_VM(0); RAW_BAR();
                P3_WKS(1, 10 + hw, L_RB + hw * 5120); WAIT_VM(0);
                BAR(); BAR();
            }
        }
        if (tid == 512) publish(ws, WS_FOA + (qt * 32 + h) * 8, tag);
        for (int ks = 14 + hw; ks < 20; ks += 4) P3_WKS(1, ks, L_RC + (ks - 14) * 5120);
        WAIT_VM(0);
        BAR();
        for (int ks = hw; ks < 7; ks += 4) P3_WKS(0, ks, ks < 4 ? L_RD + ks * 5120 : L_RE + (ks - 4) * 5120);
        BAR();
        if (hw == 3) wait_flags(ws, WS_FOA + (xcd * 32 + r32) * 8, tag);
        WAIT_VM(0);
        BAR();
        BAR();
        BAR();
    } else {
        const int th = wid >> 2, cw = wid & 3;
        {
            unsigned vx[2];
#pragma unroll
            for (int j = 0; j < 2; ++j) { const int row = 8 * (wid * 2 + j) + (lane >> 3); vx[j] = row * 1280 + (((lane ^ row) & 7) << 4); }
            const unsigned lx = lds0 + wid * 2048;
            const char* pw0 = wsrc(2 * wid); const char* pw1 = wsrc(2 * wid + 1);
#define P1_X(s, slot) do { glds_run<1, 0, 0, false>(xb + (s) * 128, vx[0], lx + (slot) * P1_STAGE); glds_run<1, 0, 1024, false>(xb + (s) * 128, vx[1], lx + (slot) * P1_STAGE); \
                           glds_ptr<16384>(pw0, lx + (slot) * P1_STAGE); glds_ptr<17408>(pw1, lx + (slot) * P1_STAGE); pw0 += 245760; pw1 += 245760; } while (0)
            P1_X(0, 0);
            BAR();
            P1_X(1, 1);
            const bool t3 = cw < 3 || (hj == 0 && kd == 0);
            const int xrow = 64 * th + l15;
            lds_cptr xa0 = L + xrow * 128 + ((g ^ (xrow & 7)) << 4), xa1 = L + ((xrow * 128 + ((g ^ (xrow & 7)) << 4)) ^ 64);
            const unsigned wa0 = 16384 + 8192 * (g >> 1) + 480 * (8 * (g & 1) + q4) + 128 * (g & 1) + 40 * p4 + (cw < 3 ? 160 * cw : 32);
            const unsigned wst = cw < 3 ? 8 : 160;
            lds_cptr wa[4];
#pragma unroll
            for (int i = 0; i < 3; ++i) wa[i] = L + wa0 + i * wst;
            wa[3] = cw < 3 ? L + wa0 + 24 : L + L_GW + g * 16;
            f32x4 acc[4][4];
#pragma unroll
            for (int m = 0; m < 4; ++m)
#pragma unroll
                for (int i = 0; i < 4; ++i) acc[m][i] = f32x4{0.f, 0.f, 0.f, 0.f};
            bf16x8 xA[4], wA_[4], xB[4], wB_[4];
#define P1_LOAD(X, W, ks, NT, G3) do { \
        _Pragma("unroll") for (int i = 0; i < 3; ++i) W[i] = cat8(tr_read(wa[i] + (ks) * 16384), tr_read(wa[i] + (ks) * 16384 + 1920)); \
        if (NT == 4) { if (G3) W[3] = LDS_B128(wa[3] + (ks) * 64); else W[3] = cat8(tr_read(wa[3] + (ks) * 16384), tr_read(wa[3] + (ks) * 16384 + 1920)); } \
        _Pragma("unroll") for (int m = 0; m < 4; ++m) X[m] = LDS_B128(((ks) ? xa1 : xa0) + m * 2048); } while (0)
#define P1_HALF(X, W, XN, WN, ks, NT, G3, MID1, MID2) do { \
        _Pragma("unroll") for (int i = 0; i < NT; ++i) { \
            s16x4 lo_, hi_; \
            acc[0][i] = MFMA16(W[i], X[0], acc[0][i]); SBAR(); \
            if (i == 3 && G3) WN[3] = LDS_B128(wa[3] + (ks) * 64); else lo_ = tr_read(wa[i] + (ks) * 16384); SBAR(); \
            acc[1][i] = MFMA16(W[i], X[1], acc[1][i]); SBAR(); \
            if (!(i == 3 && G3)) { hi_ = tr_read(wa[i] + (ks) * 16384 + 1920); WN[i] = cat8(lo_, hi_); } SBAR(); \
            acc[2][i] = MFMA16(W[i], X[2], acc[2][i]); SBAR(); \
            XN[i] = LDS_B128(((ks) ? xa1 : xa0) + i * 2048); SBAR(); \
            acc[3][i] = MFMA16(W[i], X[3], acc[3][i]); SBAR(); \
            if (NT == 3 && i == 2) XN[3] = LDS_B128(((ks) ? xa1 : xa0) + 3 * 2048); \
            if (i == 0) { MID1; } if (i == 1) { MID2; } SBAR(); \
        } } while (0)
#define P1_DX(lim, st, sl) do { unsigned sv_; const char* xs_ = xb + (st) * 128; \
                  asm volatile("s_cmp_gt_u32 %1, " #lim "\n\ts_cbranch_scc1 1f\n\ts_mov_b32 %0, m0\n\ts_mov_b32 m0, %5\n\ts_nop 0\n\tglobal_load_lds_dwordx4 %2, %4\n\t" \
                               "s_add_u32 m0, %5, 1024\n\ts_nop 0\n\tglobal_load_lds_dwordx4 %3, %4\n\ts_mov_b32 m0, %0\n1:" \
                               : "=&s"(sv_) : "s"(s), "v"(vx[0]), "v"(vx[1]), "s"(xs_), "s"(lx + (sl) * P1_STAGE) : "memory", "scc"); } while (0)
#define P1_DW(lim, sl) do { unsigned sv_; \
                  asm volatile("s_cmp_gt_u32 %3, " #lim "\n\ts_cbranch_scc1 1f\n\ts_mov_b32 %0, m0\n\ts_add_u32 m0, %4, 16384\n\ts_nop 0\n\tglobal_load_lds_dwordx4 %1, off\n\t" \
                               "s_add_u32 m0, %4, 17408\n\tv_lshl_add_u64 %1, %1, 0, %5\n\tglobal_load_lds_dwordx4 %2, off\n\ts_mov_b32 m0, %0\n\tv_lshl_add_u64 %2, %2, 0, %5\n1:" \
                               : "=&s"(sv_), "+v"(pw0), "+v"(pw1) : "s"(s), "s"(lx + (sl) * P1_STAGE), "s"(245760ull) : "memory", "scc"); } while (0)
#define P1_LOOP(NT, G3) do { \
            P1_LOAD(xA, wA_, 0, NT, G3); \
            int slot = 0; \
            _Pragma("nounroll") for (int s = 0; s < 10; ++s) { \
                P1_HALF(xA, wA_, xB, wB_, 1, NT, G3, P1_DX(0, 2, 2), P1_DW(0, 2)); \
                  \
                asm volatile("s_cmp_gt_u32 %0, 7\n\ts_cbranch_scc1 1f\n\ts_waitcnt vmcnt(4) lgkmcnt(0)\n\ts_barrier\n\ts_branch 2f\n" \
                             "1:\n\ts_cmp_eq_u32 %0, 9\n\ts_cbranch_scc1 2f\n\ts_waitcnt vmcnt(0) lgkmcnt(0)\n\ts_barrier\n2:" :: "s"(s) : "memory", "scc"); \
                const int d = slot == 2 ? -2 * P1_STAGE : P1_STAGE; \
                xa0 += d; xa1 += d; wa[0] += d; wa[1] += d; wa[2] += d; wa[3] += G3 ? 128 : d; SBAR(); \
                P1_HALF(xB, wB_, xA, wA_, 0, NT, G3, P1_DX(6, s + 3, slot), P1_DW(6, slot)); \
                slot = slot == 2 ? 0 : slot + 1; \
            } } while (0)
            WAIT_VM(4);
            BAR();
            if (cw < 3) P1_LOOP(4, false); else if (t3) P1_LOOP(4, true); else P1_LOOP(3, false);
            const lds_ptr img = (lds_ptr)lds + L_IMG;
            {
                const int ht = (4 * hj + g) * 64 + rt * 4 + 2 * th;
                float kn = 0.f;
                if (cw < 3) {
                    const bool isv = kd ? cw != 1 : false, toA = kd ? cw == 0 : cw < 2;
                    if (kd == 0 && cw == 0) {
#pragma unroll
                        for (int m = 0; m < 4; ++m)
#pragma unroll
                            for (int i = 0; i < 4; ++i) acc[m][i] *= QSCALE;
                    }
                    const unsigned ts = (kd == 0 && cw == 0) ? QFT : KVT, tb = ((kd == 0 && cw == 0) ? WS_QA : WS_KV + (kd ? 1536 : 0)) + ht * ts;
                    const unsigned o0 = l15 * 16, o1 = 512 + (isv ? l15 ^ 4 : l15) * 16;
#pragma unroll
                    for (int m = 0; m < 4; ++m) {
                        const u32x4 w0 = {cvtpk(acc[m][0][0], acc[m][0][1]), cvtpk(acc[m][0][2], acc[m][0][3]), cvtpk(acc[m][1][0], acc[m][1][1]), cvtpk(acc[m][1][2], acc[m][1][3])};
                        const u32x4 w1 = {cvtpk(acc[m][2][0], acc[m][2][1]), cvtpk(acc[m][2][2], acc[m][2][3]), cvtpk(acc[m][3][0], acc[m][3][1]), cvtpk(acc[m][3][2], acc[m][3][3])};
                        if (toA) { const unsigned mo = tb + (m >> 1) * ts + (m & 1) * 256; ST128(w0, mo + o0); ST128(w1, mo + o1); }
                        else { const lds_ptr d = img + (((isv ? 4 : 0) + 2 * th + (m >> 1)) * 4 + g) * 1536 + (m & 1) * 256; *(AS3(u32x4)*)(d + o0) = w0; *(AS3(u32x4)*)(d + o1) = w1; }
                    }
                    if (kd == 0 && cw == 1) {
#pragma unroll
                        for (int m = 0; m < 4; ++m) {
                            float n2 = 0.f;
#pragma unroll
                            for (int i = 0; i < 4; ++i)
#pragma unroll
                                for (int r = 0; r < 4; ++r) n2 = fmaf(acc[m][i][r], acc[m][i][r], n2);
                            kn = (n2 == n2) ? fmaxf(kn, n2) : __builtin_inff();
                        }
                    }
                } else {
#pragma unroll
                    for (int m = 0; m < 4; ++m) {
                        const unsigned ok = WS_KV + (ht + (m >> 1)) * KVT + 1024, o8 = (m & 1) * 128 + l15 * 8, o16 = (m & 1) * 256 + (l15 ^ 8) * 16;
                        const lds_ptr d = img + ((2 * th + (m >> 1)) * 4 + g) * 1536 + 1024;
                        const u32x2 w1 = {cvtpk(acc[m][1][0], acc[m][1][1]), cvtpk(acc[m][1][2], acc[m][1][3])}, w2 = {cvtpk(acc[m][2][0], acc[m][2][1]), cvtpk(acc[m][2][2], acc[m][2][3])};
                        if (kd == 0) {
                            ST64((u32x2{cvtpk(acc[m][0][0] * QSCALE, acc[m][0][1] * QSCALE), cvtpk(acc[m][0][2] * QSCALE, acc[m][0][3] * QSCALE)}), WS_QA + (ht + (m >> 1)) * QFT + 1024 + o8);
                            ST128((u32x4{w1[0], w1[1], 0x3f803f80u, 0x00003f80u}), ok + (m & 1) * 256 + l15 * 16);
                            *(AS3(u32x2)*)(d + o8) = w2;
                            float n2 = 0.f;
#pragma unroll
                            for (int r = 0; r < 4; ++r) n2 = fmaf(acc[m][1][r], acc[m][1][r], n2);
                            kn = (n2 == n2) ? fmaxf(kn, n2) : __builtin_inff();
                        } else {
                            ST128((u32x4{cvtpk(acc[m][0][0], acc[m][0][1]), cvtpk(acc[m][0][2], acc[m][0][3]), 0x00003f80u, 0u}), ok + 1536 + o16);
                            *(AS3(u32x2)*)(d + o8) = w1;
                            *(AS3(u32x4)*)(d + 16 * 1536 + o16) = u32x4{w2[0], w2[1], 0x00003f80u, 0u};
                        }
                    }
                }
                if (cw == 3 && t3 && g == 0) {
#pragma unroll
                    for (int m = 0; m < 4; ++m) ST32(__float_as_uint(1.0f / (1.0f + __expf(-acc[m][3][0]))), WS_G + (rt * 128 + 64 * th + 16 * m + l15) * 4);
                }
                if (kd == 0 && (cw & 1)) {
                    kn = row_max16(kn);
                    if (l15 == 0) ST32(__float_as_uint(kn), WS_KMAX + ((4 * hj + g) * 64 + (cw == 3 ? 32 : 0) + rt * 2 + th) * 4);
                }
            }
            u32x4 wp0; u32x2 wp1;
            {
                const int fr = (r32 & 3) + 4 * (r32 >> 3);
                const lds_cptr wp = L + L_WPH + cw * 800 + (fr < 10 ? 10 * ((r32 >> 2) & 1) + fr : 0) * 2;
                unsigned wv[12];
#pragma unroll
                for (int j = 0; j < 8; ++j) wv[j] = *(const AS3(uint16_t)*)(wp + hh * 320 + j * 40);
#pragma unroll
                for (int j = 0; j < 4; ++j) wv[8 + j] = *(const AS3(uint16_t)*)(wp + 640 + j * 40);
                const unsigned m0 = fr < 10 ? 0xffffffffu : 0u, m1 = (fr < 10 && hh == 0) ? 0xffffffffu : 0u;
                wp0 = u32x4{(wv[0] | (wv[1] << 16)) & m0, (wv[2] | (wv[3] << 16)) & m0, (wv[4] | (wv[5] << 16)) & m0, (wv[6] | (wv[7] << 16)) & m0};
                wp1 = u32x2{(wv[8] | (wv[9] << 16)) & m1, (wv[10] | (wv[11] << 16)) & m1};
            }
            RAW_BAR();
#pragma unroll
            for (int t2 = 0; t2 < 2; ++t2) {
                const lds_cptr f = L + L_IMG + ((2 * th + t2) * 4 + cw) * 1536;
                unsigned ph[10];
                feature_map(wp0, wp1, *(const AS3(u32x4)*)(f + ka), *(const AS3(u32x2)*)(f + kb), ph);
                const lds_ptr d = (lds_ptr)lds + L_PH + (cw * 4 + 2 * th + t2) * 2560;
                if (kd) {
#pragma unroll
                    for (int u = 0; u < 5; ++u) *(AS3(u32x2)*)(d + r32 * 80 + hh * 40 + u * 8) = u32x2{ph[2 * u], ph[2 * u + 1]};
                } else {
                    *(AS3(u32x4)*)(d + l16) = u32x4{ph[0], ph[1], ph[2], ph[3]}; *(AS3(u32x4)*)(d + 1024 + l16) = u32x4{ph[4], ph[5], ph[6], ph[7]};
                    *(AS3(u32x2)*)(d + 2048 + lane * 8) = u32x2{ph[8], ph[9]};
                }
                if (t2 == 0) {
                    SBAR();
                    WAIT_VM(0);
                    BAR();
                    SBAR();
                }
            }
            if (kd) {
                RAW_BAR();
#pragma unroll
                for (int k2 = 0; k2 < 2; ++k2) {
                    if (k2 == 0 || wid < 4) {
                        const int u = wid + 8 * k2, uh = u / 3, un = u - 3 * uh;
                        f32x4 c0 = {0.f, 0.f, 0.f, 0.f}, c1 = {0.f, 0.f, 0.f, 0.f};
                        const unsigned vo = L_IMG + (16 + uh) * 1536;
                        const unsigned vb = 512 * (p4 >> 1) + 256 * (g >> 1) + 64 * ((2 * (g & 1)) ^ (p4 >> 1)) + 16 * q4 + 8 * (p4 & 1);
                        const unsigned vc = 1024 + 256 * (g >> 1) + 64 * ((2 * (g & 1)) ^ 2) + 16 * q4 + (p4 ? 8 : 0);
                        const unsigned pb = L_PH + (uh * 128 + 8 * g + q4) * 80 + un * 32 + p4 * 8;
#pragma unroll
                        for (int ks = 0; ks < 4; ++ks) {
                            const bf16x8 a0 = cat8(tr_read(L + vo + vb + ks * 6144), tr_read(L + vo + (vb ^ 64) + ks * 6144));
                            const bf16x8 a1 = cat8(tr_read(L + vo + vc + ks * 6144), tr_read(L + vo + (vc ^ 64) + ks * 6144));
                            const bf16x8 b = cat8(tr_read(L + pb + ks * 2560), tr_read(L + pb + ks * 2560 + 320));
                            c0 = MFMA16(a0, b, c0); c1 = MFMA16(a1, b, c1);
                        }
                        const int ph = 16 * un + l15;
                        if (ph < 40) {
                            const lds_ptr d = (lds_ptr)lds + L_IMG + uh * 4096 + ph * 96 + 16 * g;
                            *(AS3(f32x4)*)(d) = c0; if (g < 2) *(AS3(f32x4)*)(d + 64) = c1;
                        }
                    }
                }
            }
        }
        const int qp = wid & 3, kh = wid >> 2;
        const int own = 2 * qp + kh;
        const int blk = (lane >> 4) & 1, vj = blk ? 2 : p4 >> 1;
        const unsigned kb2 = 1024 + r32 * 16 + hh * 8;
        const unsigned va = 1536 + 512 * vj + 64 * ((2 * hh) ^ vj) + 16 * q4 + (blk ? (p4 ? 8 : 0) : 8 * (p4 & 1)), vb2 = va ^ 64;
        RAW_BAR();
        {
            const char* src = ws + WS_KV + ((size_t)h * 64 + kh * 32) * KVT + qp * 6144; const unsigned dst = lds0 + kh * SLOT + qp * 6144;
            glds_run<4, 0, 0, true>(src, l16, dst); glds_run<2, 4096, 4096, true>(src, l16, dst);
        }
        WAIT_VM(0);
        BAR();
        const lds_cptr lq = L + L_Q + 2 * qp * QFT;
        const u32x4 qa0 = *(const AS3(u32x4)*)(lq + ka), qb0 = *(const AS3(u32x4)*)(lq + QFT + ka);
        u32x2 qa1 = *(const AS3(u32x2)*)(lq + kb), qb1 = *(const AS3(u32x2)*)(lq + QFT + kb);
        float kmx = *(const AS3(float)*)(L + L_Q + 10240 + lane * 4);
        if (hh) { qa1 = u32x2{0u, 0u}; qb1 = u32x2{0u, 0u}; }
        const bf16x8 QA0 = __builtin_bit_cast(bf16x8, qa0), QB0 = __builtin_bit_cast(bf16x8, qb0);
        float nba, nbb;
        {
            kmx = row_max16(kmx);
            auto r = __builtin_amdgcn_permlane16_swap(__float_as_uint(kmx), __float_as_uint(kmx), false, false);
            kmx = fmaxf(__uint_as_float(r[0]), __uint_as_float(r[1]));
            const float kn = -1.01f * __builtin_amdgcn_sqrtf(xhalf_sum(kmx));
            nba = kn * __builtin_amdgcn_sqrtf(qnorm2(qa0, qa1)); nbb = kn * __builtin_amdgcn_sqrtf(qnorm2(qb0, qb1));
        }
        for (int attempt = 0;; ++attempt) {
            f32x16 oa, ob, sA, sB;
#pragma unroll
            for (int r = 0; r < 16; ++r) { oa[r] = 0.f; ob[r] = 0.f; sB[r] = 0.f; }
            const s16x4 QA1 = __builtin_bit_cast(s16x4, hh ? split3(nba) : qa1), QB1 = __builtin_bit_cast(s16x4, hh ? split3(nbb) : qb1);
            lds_cptr pka = L + kh * SLOT + ka, pkb = L + kh * SLOT + kb2, pv = L + kh * SLOT + va, pv2 = L + kh * SLOT + vb2;
            KFrag k0 = ld_k(pka, pkb, 0), k1 = ld_k(pka, pkb, KVT);
            VFrag v0 = ld_v(pv, 0), v1 = v0;
            sA = MFMA32(k0.k0, QA0, f32x16{}); sA = MFMA32K8(k0.k1, QA1, sA);
            for (int c = 0; c < 4; ++c) {
                const int dnext = (c & 1) ? -2 * SLOT : 2 * SLOT;
#define K2_PAIR(t, LOADK0, LOADV0, LOADK1) do { \
                bf16x8 vp = v1.v1; \
                v1 = ld_v(pv, ((t) + 1) * KVT); SBAR(); \
                attn_step(sA, sB, k0, vp, v0.v0, QB0, QB1, ob, oa); \
                LOADK0; SBAR(); \
                attn_step(sB, sA, k1, v0.v1, v0.v0, QA0, QA1, oa, ob); \
                vp = v0.v1; \
                LOADV0; SBAR(); \
                attn_step(sA, sB, k1, vp, v1.v0, QB0, QB1, ob, oa); \
                LOADK1; SBAR(); \
                attn_step(sB, sA, k0, v1.v1, v1.v0, QA0, QA1, oa, ob); } while (0)
#define PRIO(p) do { asm volatile("s_cmp_eq_u32 %0, 0\n\ts_cbranch_scc1 1f\n\ts_setprio " #p "\n1:" :: "s"(kh) : "scc"); SBAR(); } while (0)
                K2_PAIR(0, k0 = ld_k(pka, pkb, 2 * KVT), v0 = ld_v(pv, 2 * KVT), k1 = ld_k(pka, pkb, 3 * KVT));
                K2_PAIR(2, k0 = ld_k(pka, pkb, 4 * KVT), v0 = ld_v(pv, 4 * KVT), k1 = ld_k(pka, pkb, 5 * KVT));
                PRIO(0);
                K2_PAIR(4, k0 = ld_k(pka, pkb, 6 * KVT), v0 = ld_v(pv, 6 * KVT), k1 = ld_k(pka, pkb, 7 * KVT));
#define TURN() do { asm volatile("s_cmp_eq_u32 %0, 3\n\ts_cbranch_scc1 1f\n\ts_waitcnt vmcnt(0) lgkmcnt(0)\n\ts_barrier\n1:" :: "s"(c) : "memory", "scc"); \
                    pka += dnext; pkb += dnext; pv += dnext; pv2 += dnext; k0 = ld_k(pka, pkb, 0); PRIO(1); } while (0)
                K2_PAIR(6, TURN(), v0 = ld_v(pv, 0), k1 = ld_k(pka, pkb, KVT));
            }
            ob = SMF(v1.v1, HI8(sB), ob);
            PRIO(0);
            {
                lds_ptr d = (lds_ptr)lds + L_X + (own ^ 1) * 3072 + l16;
#pragma unroll
                for (int a = 0; a < 3; ++a)
                    *(AS3(f32x4)*)(d + a * 1024) = kh ? f32x4{oa[4 * a], oa[4 * a + 1], oa[4 * a + 2], oa[4 * a + 3]} : f32x4{ob[4 * a], ob[4 * a + 1], ob[4 * a + 2], ob[4 * a + 3]};
            }
            RAW_BAR();
            float o[12];
#pragma unroll
            for (int a = 0; a < 3; ++a) {
                const f32x4 t = *(const AS3(f32x4)*)(L + L_X + own * 3072 + l16 + a * 1024);
#pragma unroll
                for (int r = 0; r < 4; ++r) o[4 * a + r] = t[r] + (kh ? ob[4 * a + r] : oa[4 * a + r]);
            }
            auto rr = __builtin_amdgcn_permlane32_swap(__float_as_uint(o[8]), __float_as_uint(o[8]), false, false);
            const float lsum = __uint_as_float(rr[1]);
            const float sa = *(const AS3(float)*)(L + L_GATE + (own * 32 + r32) * 4) * __builtin_amdgcn_rcpf(lsum);
            const unsigned ocb = WS_OC + ((qt * 8 + own) * 32 + r32) * 16;
            ST128((u32x4{cvtpk(o[0] * sa, o[1] * sa), cvtpk(o[2] * sa, o[3] * sa), cvtpk(o[4] * sa, o[5] * sa), cvtpk(o[6] * sa, o[7] * sa)}), ocb + (2 * h + hh) * (SEQ * 16));
            if (!hh) ST64((u32x2{cvtpk(o[8] * sa, o[9] * sa), cvtpk(o[10] * sa, o[11] * sa)}), ocb + (64 + (h >> 1)) * (SEQ * 16) + (h & 1) * 8);
            if (attempt == 0 && __any(!(lsum >= 5.4210109e-20f)))
                *(AS3(unsigned)*)((lds_ptr)lds + L_FLAG) = 1u;
            WAIT_VM(0);
            RAW_BAR();
            const unsigned redo = *(const AS3(unsigned)*)(L + L_FLAG);
            if (__builtin_amdgcn_readfirstlane(redo) == 0u) break;
            float ma = -__builtin_inff(), mb = -__builtin_inff();
            for (int t = 0; t < 64; ++t) {
                const u32x4 g0 = LD128(WS_KV + (h * 64 + t) * KVT + ka);
                const u32x2 g1 = LD64(WS_KV + (h * 64 + t) * KVT + kb2);
                f32x16 s = MFMA32(__builtin_bit_cast(bf16x8, g0), QA0, f32x16{}); s = MFMA32K8(__builtin_bit_cast(s16x4, g1), __builtin_bit_cast(s16x4, qa1), s);
                f32x16 u = MFMA32(__builtin_bit_cast(bf16x8, g0), QB0, f32x16{}); u = MFMA32K8(__builtin_bit_cast(s16x4, g1), __builtin_bit_cast(s16x4, qb1), u);
#pragma unroll
                for (int r = 0; r < 16; ++r) { ma = fmaxf(ma, s[r]); mb = fmaxf(mb, u[r]); }
            }
            nba = -xhalf_max(ma); nbb = -xhalf_max(mb);
            RAW_BAR();
            BAR();
        }
        {
            const int mt = wid & 3, kp = wid >> 2;
            const unsigned ofo = WS_OC + ((4 * kp + g) * SEQ + tt * 64 + 16 * mt + l15) * 16;
            const lds_cptr wa = L + g * 1280 + (20 * q4 + p4) * 16;
            f32x4 acc[5];
#pragma unroll
            for (int i = 0; i < 5; ++i) acc[i] = f32x4{0.f, 0.f, 0.f, 0.f};
            u32x4 of[10];
#define P3_MMA(br) do { if (kp) { _Pragma("unroll") for (int k = 0; k < 10; ++k) _Pragma("unroll") for (int i = 0; i < 5; ++i) \
                                      acc[i] = MFMA16(LDS_B128(wa + p3_off(br, 2 * k + 1) + i * 64), __builtin_bit_cast(bf16x8, of[k]), acc[i]); } \
                        else { _Pragma("unroll") for (int k = 0; k < 10; ++k) _Pragma("unroll") for (int i = 0; i < 5; ++i) \
                                      acc[i] = MFMA16(LDS_B128(wa + p3_off(br, 2 * k) + i * 64), __builtin_bit_cast(bf16x8, of[k]), acc[i]); } } while (0)
#pragma unroll
            for (int k = 0; k < 10; ++k) of[k] = LD128(ofo + (80 + 8 * k) * (SEQ * 16));
            BAR();
            P3_MMA(1);
            RAW_BAR();
            for (int ks = 7 + wid; ks < 20; ks += 8) P3_WKS(0, ks, ks < 17 ? L_RA + (ks - 7) * 5120 : L_RB + (ks - 17) * 5120);
            BAR();
#pragma unroll
            for (int k = 0; k < 10; ++k) of[k] = LD128(ofo + (8 * k) * (SEQ * 16));
            WAIT_VM(10);
            BAR();
            P3_MMA(0);
            if (kp) {
#pragma unroll
                for (int i = 0; i < 5; ++i) *(AS3(f32x4)*)((lds_ptr)lds + L_CX + (mt * 5 + i) * 1024 + l16) = acc[i];
            }
            RAW_BAR();
            if (!kp) {
                char* dst = (char*)out + (size_t)(tt * 64 + 16 * mt + l15) * 1280 + ct * 160 + g * 40;
#pragma unroll
                for (int i = 0; i < 5; ++i) {
                    const f32x4 v = acc[i] + *(const AS3(f32x4)*)(L + L_CX + (mt * 5 + i) * 1024 + l16);
                    *(u32x2*)(dst + i * 8) = u32x2{cvtpk(v[0], v[1]), cvtpk(v[2], v[3])};
                }
            }
        }
    }
    if (bid == 0 && tid == 512) publish(ws, WS_EPOCH, tag);
}

extern "C" void kernel_launch(void* const* d_in, const int* in_sizes, int n_in, void* d_out, int out_size, void* d_ws, size_t ws_size,
                              hipStream_t stream) {
    fused_kernel<<<256, 768, 0, stream>>>((const uint16_t*)d_in[0], (const uint16_t*)d_in[1], (const uint16_t*)d_in[4], (const uint16_t*)d_in[2],
                                          (const uint16_t*)d_in[6], (const uint16_t*)d_in[5], (const uint16_t*)d_in[7], (char*)d_ws, (uint16_t*)d_out);
}
```

```cpp
#include <hip/hip_runtime.h>
#include <hip/hip_bf16.h>
#include <stdint.h>

typedef __attribute__((ext_vector_type(8))) __bf16 bf16x8;
typedef __attribute__((ext_vector_type(16))) __bf16 bf16x16;
typedef __attribute__((ext_vector_type(4))) short s16x4;
typedef __attribute__((ext_vector_type(8))) short s16x8;
typedef __attribute__((ext_vector_type(4))) float f32x4;
typedef __attribute__((ext_vector_type(16))) float f32x16;
typedef __attribute__((ext_vector_type(4))) unsigned u32x4;
typedef __attribute__((ext_vector_type(2))) unsigned u32x2;
typedef __attribute__((ext_vector_type(2))) __bf16 bf16x2;
typedef __attribute__((ext_vector_type(2))) float f32x2;
typedef __attribute__((address_space(3))) const char* lds_cptr;
typedef __attribute__((address_space(3))) char* lds_ptr;
typedef unsigned long long u64;
#define AS3(T) __attribute__((address_space(3))) T

constexpr unsigned CODE_BYTES = 30000;
constexpr int SEQ = 2048, NH = 32;
constexpr unsigned OOB = 0x7ffffff0u;
constexpr float LOG2E = 1.4426950408889634f;
constexpr float QSCALE = 0.22360679774997896f * 1.4426950408889634f;

constexpr int KVT = 3072;
constexpr int QFT = 1280;
constexpr unsigned WS_KV = 0;
constexpr unsigned WS_QA = WS_KV + NH * 64 * KVT;
constexpr unsigned WS_PHQ = WS_QA + NH * 64 * QFT;
constexpr unsigned WS_WOP = WS_PHQ + NH * 64 * 2560;
constexpr unsigned WS_OC = WS_WOP + 160 * 640 * 16;
constexpr unsigned WS_KMAX = WS_OC + 160 * SEQ * 16;
constexpr unsigned WS_G = WS_KMAX + NH * 64 * 4;
constexpr unsigned WS_KVP = WS_G + SEQ * 4;
constexpr unsigned WS_FT = WS_KVP + NH * 16 * 3840;
constexpr unsigned WS_FH = WS_FT + 2048;
constexpr unsigned WS_FOB = WS_FH + 16384;
constexpr unsigned WS_FOA = WS_FOB + 2048;
constexpr unsigned WS_EPOCH = WS_FOA + 2048;
constexpr unsigned WS_END = WS_EPOCH + 128;

__device__ __forceinline__ unsigned cvtpk(float lo, float hi) {
    return __builtin_bit_cast(unsigned, __builtin_convertvector(f32x2{lo, hi}, bf16x2)); }
__device__ __forceinline__ float bflo(unsigned w) { return __uint_as_float(w << 16); }
__device__ __forceinline__ float bfhi(unsigned w) { return __uint_as_float(w & 0xffff0000u); }
#define GL1(P) "global_load_lds_dwordx4 %1, %2" P "\n\t"
#define GL2(P) GL1(P) "global_load_lds_dwordx4 %1, %2 offset:1024" P "\n\t"
#define GL3(P) GL2(P) "global_load_lds_dwordx4 %1, %2 offset:2048" P "\n\t"
#define GL4(P) GL3(P) "global_load_lds_dwordx4 %1, %2 offset:3072" P "\n\t"
#define GLASM(BODY) asm volatile("s_mov_b32 %0, m0\n\ts_add_u32 m0, %3, %4\n\ts_nop 0\n\t" BODY "s_mov_b32 m0, %0" \
                     : "=&s"(sv) : "v"(voff), "s"(t), "s"(lbase), "i"(LOFF) : "memory", "scc")
template <int N, int GOFF, int LOFF, bool SC1>
__device__ __forceinline__ void glds_run(const void* sbase, unsigned voff, unsigned lbase) {
    static_assert(N >= 1 && N <= 4, "");
    unsigned sv, tl, th;
    lbase = __builtin_amdgcn_readfirstlane(lbase);
    const unsigned lo = __builtin_amdgcn_readfirstlane((unsigned)(uintptr_t)sbase), hi = __builtin_amdgcn_readfirstlane((unsigned)((uintptr_t)sbase >> 32));
    asm volatile("s_add_u32 %0, %2, %4\n\ts_addc_u32 %1, %3, 0" : "=&s"(tl), "=s"(th) : "s"(lo), "s"(hi), "i"(GOFF) : "scc");
    const uint64_t t = ((uint64_t)th << 32) | tl;
    if constexpr (SC1) {
        if constexpr (N == 1) GLASM(GL1(" sc1")); else if constexpr (N == 2) GLASM(GL2(" sc1")); else if constexpr (N == 3) GLASM(GL3(" sc1")); else GLASM(GL4(" sc1"));
    } else {
        if constexpr (N == 1) GLASM(GL1("")); else if constexpr (N == 2) GLASM(GL2("")); else if constexpr (N == 3) GLASM(GL3("")); else GLASM(GL4(""));
    }
}
template <int LOFF>
__device__ __forceinline__ void glds_ptr(const void* p, unsigned lbase) {
    unsigned sv; lbase = __builtin_amdgcn_readfirstlane(lbase);
    asm volatile("s_mov_b32 %0, m0\n\ts_add_u32 m0, %2, %3\n\ts_nop 0\n\tglobal_load_lds_dwordx4 %1, off\n\ts_mov_b32 m0, %0"
                 : "=&s"(sv) : "v"(p), "s"(lbase), "i"(LOFF) : "memory", "scc"); }
__device__ __forceinline__ void touch(const void* p, unsigned lds_scratch) {
    unsigned sv;
    asm volatile("s_mov_b32 %0, m0\n\ts_mov_b32 m0, %2\n\ts_nop 0\n\tglobal_load_lds_dword %1, off sc1\n\ts_mov_b32 m0, %0" : "=&s"(sv) : "v"(p), "s"(lds_scratch) : "memory"); }
__device__ __forceinline__ s16x4 tr_read(lds_cptr p) { return __builtin_amdgcn_ds_read_tr16_b64_v4i16((AS3(s16x4)*)p); }
__device__ __forceinline__ bf16x8 cat8(s16x4 a, s16x4 b) {
    s16x8 r = {a[0], a[1], a[2], a[3], b[0], b[1], b[2], b[3]}; return __builtin_bit_cast(bf16x8, r); }
#define LDS_B128(p) (*(const AS3(bf16x8)*)(p))
#define LDS_B64(p) (*(const AS3(s16x4)*)(p))
#define MFMA16(a, b, c) __builtin_amdgcn_mfma_f32_16x16x32_bf16(a, b, c, 0, 0, 0)
#define MFMA32(a, b, c) __builtin_amdgcn_mfma_f32_32x32x16_bf16(a, b, c, 0, 0, 0)
#define MFMA32K8(a, b, c) __builtin_amdgcn_mfma_f32_32x32x8bf16_1k(a, b, c, 0, 0, 0)
#define WAIT_VM_(n) asm volatile("s_waitcnt vmcnt(" #n ")" ::: "memory")
#define WAIT_VM(n) WAIT_VM_(n)
#define RAW_BAR() asm volatile("s_waitcnt lgkmcnt(0)\n\ts_barrier" ::: "memory")
#define SBAR() __builtin_amdgcn_sched_barrier(0)
#define EXP2(v) __builtin_amdgcn_exp2f(v)
#define SMF(a, lo, acc) __builtin_amdgcn_smfmac_f32_32x32x32_bf16(a, __builtin_bit_cast(bf16x16, lo), acc, 0xDDDD, 0, 0)
#define LO8(s) __builtin_shufflevector(s, s, 0, 1, 2, 3, 4, 5, 6, 7)
#define HI8(s) __builtin_shufflevector(s, s, 8, 9, 10, 11, 12, 13, 14, 15)
#define ST128(v, off) __builtin_amdgcn_raw_buffer_store_b128(v, wsr, off, 0, 16)
#define ST64(v, off) __builtin_amdgcn_raw_buffer_store_b64(v, wsr, off, 0, 16)
#define ST32(v, off) __builtin_amdgcn_raw_buffer_store_b32(v, wsr, off, 0, 16)
__device__ __forceinline__ float xhalf_max(float v) {
    auto r = __builtin_amdgcn_permlane32_swap(__float_as_uint(v), __float_as_uint(v), false, false);
    return fmaxf(__uint_as_float(r[0]), __uint_as_float(r[1])); }
__device__ __forceinline__ float xhalf_min(float v) {
    auto r = __builtin_amdgcn_permlane32_swap(__float_as_uint(v), __float_as_uint(v), false, false);
    return fminf(__uint_as_float(r[0]), __uint_as_float(r[1])); }
__device__ __forceinline__ float xhalf_sum(float v) {
    auto r = __builtin_amdgcn_permlane32_swap(__float_as_uint(v), __float_as_uint(v), false, false);
    return __uint_as_float(r[0]) + __uint_as_float(r[1]); }
__device__ __forceinline__ float row_max16(float v) {
    v = fmaxf(v, __int_as_float(__builtin_amdgcn_update_dpp(0, __float_as_int(v), 0x128, 0xf, 0xf, false)));
    v = fmaxf(v, __int_as_float(__builtin_amdgcn_update_dpp(0, __float_as_int(v), 0x124, 0xf, 0xf, false)));
    v = fmaxf(v, __int_as_float(__builtin_amdgcn_update_dpp(0, __float_as_int(v), 0x122, 0xf, 0xf, false)));
    v = fmaxf(v, __int_as_float(__builtin_amdgcn_update_dpp(0, __float_as_int(v), 0x121, 0xf, 0xf, false)));
    return v; }
__device__ __forceinline__ void publish(char* ws, unsigned off, u64 tag) {
    __hip_atomic_store((u64*)(ws + off), tag, __ATOMIC_RELAXED, __HIP_MEMORY_SCOPE_AGENT); }
__device__ __forceinline__ void wait_flags(const char* ws, unsigned off, u64 tag) {
    for (int spin = 0; spin < (1 << 20); ++spin) {
        const u64 f = __hip_atomic_load((const u64*)(ws + off), __ATOMIC_RELAXED, __HIP_MEMORY_SCOPE_AGENT);
        if (__all(f == tag)) break;
        __builtin_amdgcn_s_sleep(4);
    }
    asm volatile("" ::: "memory");
}

struct KFrag { bf16x8 k0; s16x4 k1; };
struct VFrag { bf16x8 v0, v1; };
__device__ __forceinline__ KFrag ld_k(lds_cptr pa, lds_cptr pb, int off) { KFrag k; k.k0 = LDS_B128(pa + off); k.k1 = LDS_B64(pb + off); return k; }
__device__ __forceinline__ VFrag ld_v2(lds_cptr p, lds_cptr q, int off) {
    VFrag v; v.v0 = cat8(tr_read(p + off), tr_read(q + off)); v.v1 = cat8(tr_read(p + off + 256), tr_read(q + off + 256)); return v; }
#define ld_v(p, off) ld_v2(p, p##2, off)
__device__ __forceinline__ void attn_step(f32x16& sc, f32x16& sn, const KFrag& kn, const bf16x8& vprev1, const bf16x8& vc0,
                                          const bf16x8& qn0, const s16x4& qn1, f32x16& oprev, f32x16& oc) {
    oprev = SMF(vprev1, HI8(sn), oprev); SBAR();
#pragma unroll
    for (int r = 0; r < 4; ++r) sc[r] = EXP2(sc[r]);
    SBAR(); sn = MFMA32(kn.k0, qn0, f32x16{}); SBAR();
#pragma unroll
    for (int r = 4; r < 8; ++r) sc[r] = EXP2(sc[r]);
    SBAR(); sn = MFMA32K8(kn.k1, qn1, sn); SBAR();
#pragma unroll
    for (int r = 8; r < 12; ++r) sc[r] = EXP2(sc[r]);
    SBAR(); oc = SMF(vc0, LO8(sc), oc); SBAR();
#pragma unroll
    for (int r = 12; r < 16; ++r) sc[r] = EXP2(sc[r]);
    SBAR();
}
__device__ __forceinline__ void feature_map(const u32x4 wp0, const u32x2 wp1, const u32x4 f0, const u32x2 f1, unsigned (&ph)[10]) {
    f32x16 pq = MFMA32(__builtin_bit_cast(bf16x8, wp0), __builtin_bit_cast(bf16x8, f0), f32x16{});
    pq = MFMA32K8(__builtin_bit_cast(s16x4, wp1), __builtin_bit_cast(s16x4, f1), pq);
    float mx = fmaxf(pq[0], pq[1]), mn = fminf(pq[0], pq[1]);
#pragma unroll
    for (int u = 2; u < 10; u += 2) { mx = fmaxf(fmaxf(mx, pq[u]), pq[u + 1]); mn = fminf(fminf(mn, pq[u]), pq[u + 1]); }
    mx = xhalf_max(mx); mn = xhalf_min(mn);
    const f32x2 lp = {LOG2E, LOG2E}, cmx = {-mx * LOG2E, -mx * LOG2E}, cmn = {mn * LOG2E, mn * LOG2E};
    f32x2 ep[5], en[5], sp2 = {0.f, 0.f}, sn2 = {0.f, 0.f};
#pragma unroll
    for (int u = 0; u < 5; ++u) {
        const f32x2 p = {pq[2 * u], pq[2 * u + 1]};
        const f32x2 a = __builtin_elementwise_fma(p, lp, cmx), b = __builtin_elementwise_fma(p, -lp, cmn);
        ep[u] = f32x2{EXP2(a[0]), EXP2(a[1])}; en[u] = f32x2{EXP2(b[0]), EXP2(b[1])};
        sp2 += ep[u]; sn2 += en[u];
    }
    const float sp = __builtin_amdgcn_rcpf(xhalf_sum(sp2[0] + sp2[1])), sn = __builtin_amdgcn_rcpf(xhalf_sum(sn2[0] + sn2[1]));
#pragma unroll
    for (int u = 0; u < 5; ++u) {
        const f32x2 a = ep[u] * sp, b = en[u] * sn;
        ph[u] = cvtpk(a[0], a[1]); ph[5 + u] = cvtpk(b[0], b[1]);
    }
}
__device__ __forceinline__ u32x2 split3(float v) {
    const unsigned hi = cvtpk(v, 0.f) & 0xffffu; const float r1 = v - bflo(hi);
    const unsigned mid = cvtpk(r1, 0.f) & 0xffffu; const float r2 = r1 - bflo(mid);
    return u32x2{hi | (mid << 16), cvtpk(r2, 0.f) & 0xffffu};
}
__device__ __forceinline__ float qnorm2(const u32x4& a, const u32x2& b) {
    float n2 = 0.f;
#pragma unroll
    for (int j = 0; j < 4; ++j) { n2 = fmaf(bflo(a[j]), bflo(a[j]), n2); n2 = fmaf(bfhi(a[j]), bfhi(a[j]), n2); }
#pragma unroll
    for (int j = 0; j < 2; ++j) { n2 = fmaf(bflo(b[j]), bflo(b[j]), n2); n2 = fmaf(bfhi(b[j]), bfhi(b[j]), n2); }
    return xhalf_sum(n2);
}

#define LD128(off) __builtin_amdgcn_raw_buffer_load_b128(wsr, off, 0, 16)
#define LD64(off) __builtin_amdgcn_raw_buffer_load_b64(wsr, off, 0, 16)
#define LD32(off) __builtin_amdgcn_raw_buffer_load_b32(wsr, off, 0, 16)
#define BAR() asm volatile("s_barrier" ::: "memory")

__device__ __forceinline__ const char* w_source(int p, int lane, const char* g0, const char* g1, const char* g2) {
    int q = (p & 7) * 64 + lane; const int up = q >= 248; q -= up ? 248 : 0; q = q < 239 ? q : 239;
    const int row = __umul24(q, 2185) >> 16, ch = q - row * 30, sgm = __umul24(ch, 6554) >> 16;
    const uintptr_t b = sgm == 0 ? (uintptr_t)g0 : sgm == 1 ? (uintptr_t)g1 : (uintptr_t)g2;
    return (const char*)(b + (__umul24(16 * (p >> 3) + 8 * up + row, 3840) + (ch - 10 * sgm) * 16));
}

constexpr int P1_STAGE = 49152;
constexpr int L_WPH = 3 * P1_STAGE;
constexpr int L_GW = L_WPH + 3200;
constexpr int L_WO = L_GW + 1280;
constexpr int L_SCR = L_WO + 10240;
constexpr int L_EP = L_SCR + 256;
constexpr int L_FLAG = L_EP + 256;
constexpr int LDS_BYTES = L_FLAG + 16;
constexpr int L_IMG = P1_STAGE;
constexpr int L_PH = L_IMG + 32 * 1536;
constexpr int L_Q = L_PH + 40960 + 256;
constexpr int SLOT = 8 * KVT;
constexpr int L_X = 0;
constexpr int L_MISC = L_WO;
constexpr int L_GATE = L_MISC + 3072;
constexpr int L_RA = 4 * SLOT, L_RB = SLOT, L_RC = 2 * SLOT, L_RD = L_RC + 6 * 5120;
constexpr int L_PC = L_RA, L_PHB = L_RA + 20480;
static_assert(L_Q + 10240 + 256 <= L_WO && L_RA + 10 * 5120 <= L_WO && L_RD + 3 * 5120 <= L_RA && LDS_BYTES <= 163840, "");
__device__ __forceinline__ constexpr int p3_off(int br, int ks) {
    return ks < 10 ? L_RA + ks * 5120 : ks < 14 ? L_RB + (ks - 10) * 5120 : br ? L_RC + (ks - 14) * 5120 : ks < 17 ? L_RD + (ks - 14) * 5120 : (ks - 17) * 5120; }

__global__ __launch_bounds__(768, 3) void fused_kernel(const uint16_t* __restrict__ x, const uint16_t* __restrict__ wA, const uint16_t* __restrict__ wB,
                                                    const uint16_t* __restrict__ woA, const uint16_t* __restrict__ woB,
                                                    const uint16_t* __restrict__ wphi, const uint16_t* __restrict__ gatew,
                                                    char* __restrict__ ws, uint16_t* __restrict__ out) {
    __shared__ __attribute__((aligned(16))) char lds[LDS_BYTES];
    uint64_t pc0; asm volatile("s_getpc_b64 %0" : "=s"(pc0));
    const int bid = blockIdx.x, xcd = bid & 7, bi = bid >> 3;
    const int tid = threadIdx.x, lane = tid & 63;
    const int wid = __builtin_amdgcn_readfirstlane(tid >> 6);
    const unsigned lds0 = (unsigned)(uintptr_t)lds;
    const lds_cptr L = (lds_cptr)lds;
    const unsigned l16 = lane * 16;
    const int g = lane >> 4, l15 = lane & 15, q4 = l15 >> 2, p4 = lane & 3, r32 = lane & 31, hh = lane >> 5;
    const auto wsr = __builtin_amdgcn_make_buffer_rsrc(ws, 0, WS_END, 0x00020000);
    const int rt = (xcd & 1) * 8 + (bi & 7), kd = bi >> 4, hj = (xcd >> 1) * 2 + ((bi >> 3) & 1);
    const int h = xcd * 4 + (bi >> 3), qt = bi & 7;
    const int tt = xcd * 4 + (bi & 3), ct = bi >> 2;
    const char* xb = (const char*)x + (size_t)rt * 128 * 1280;
    const unsigned ka = lane * 16, kb = 1024 + r32 * 8;
    const char* wg0 = (const char*)wA + hj * 160 + (kd ? 2560 : 0);
    const char* wg1 = kd ? (const char*)wB + hj * 160 + 1280 : (const char*)wA + hj * 160 + 1280;
    const char* wg2 = (const char*)wB + hj * 160 + (kd ? 2560 : 0);
#define wsrc(p) w_source(p, lane, wg0, wg1, wg2)
#define P3_WKS(br, ks, dst) do { _Pragma("unroll") for (int j_ = 0; j_ < 5; ++j_) { const int q_ = j_ * 64 + lane, c_ = __umul24(q_, 52429) >> 22, n_ = q_ - c_ * 80; \
        glds_run<1, 0, 0, true>(ws + WS_WOP + ct * 1280 + ((br) * 80 + 4 * (ks)) * 10240, c_ * 10240 + n_ * 16, lds0 + (dst) + j_ * 1024); } } while (0)
    u64 tag = 0;
#define MAKE_TAG() do { u64 z = ((u64)(unsigned)__builtin_amdgcn_readfirstlane((unsigned)(tag >> 32)) << 32) | (unsigned)__builtin_amdgcn_readfirstlane((unsigned)tag); \
        z = z * 0x9E3779B97F4A7C15ull + 0x632BE59BD9B4E019ull; z ^= z >> 29; z *= 0xBF58476D1CE4E5B9ull; z ^= z >> 32; tag = z; } while (0)

    if (wid >= 8) {
        const int hw = wid - 8;
        __builtin_amdgcn_s_setprio(2);
        unsigned hbt = 0;
        if (tid == 512) *(AS3(unsigned)*)((lds_ptr)lds + L_FLAG + 4) = 0u;
#define HBAR() do { hbt += 4; asm volatile("s_waitcnt lgkmcnt(0)" ::: "memory"); \
        if (lane == 0) __hip_atomic_fetch_add((AS3(unsigned)*)((lds_ptr)lds + L_FLAG + 4), 1u, __ATOMIC_RELAXED, __HIP_MEMORY_SCOPE_WORKGROUP); \
        while (__builtin_amdgcn_readfirstlane(__hip_atomic_load((AS3(unsigned)*)((lds_ptr)lds + L_FLAG + 4), __ATOMIC_RELAXED, __HIP_MEMORY_SCOPE_WORKGROUP)) < hbt) __builtin_amdgcn_s_sleep(1); \
        asm volatile("" ::: "memory"); } while (0)
        const char* pw[4];
#pragma unroll
        for (int j = 0; j < 4; ++j) pw[j] = wsrc(16 + 4 * hw + j);
        auto wstage = [&](int slot) {
            const unsigned lw = lds0 + slot * P1_STAGE + 32768 + hw * 4096;
            glds_ptr<0>(pw[0], lw); glds_ptr<1024>(pw[1], lw); glds_ptr<2048>(pw[2], lw); glds_ptr<3072>(pw[3], lw);
#pragma unroll
            for (int j = 0; j < 4; ++j) pw[j] += 245760;
        };
        if (hj == 0 && kd == 0 && hw < 2 && hw * 1024 + l16 < 1280) glds_run<1, 0, L_GW, false>(gatew, hw * 1024 + l16, lds0 + hw * 1024);
        wstage(0);
        RAW_BAR();
        wstage(1);
        WAIT_VM(4);
        BAR();
        wstage(2);
        const char* tp = hw == 0 ? xb + ((bi >> 3) * 32 + r32) * 1280 + 3 * 128
                       : hw == 1 ? ((lane % 6) < 2 ? wg0 : (lane % 6) < 4 ? wg1 : wg2) + ((bi & 7) * 8 + (lane < 48 ? lane / 6 : 0) + 192) * 3840 + (lane & 1) * 156 : xb;
        const unsigned tstep = hw == 0 ? 128u : hw == 1 ? 245760u : 0u;
        touch(tp, lds0 + L_SCR); tp += tstep; touch(tp, lds0 + L_SCR); tp += tstep;
        for (int s = 0; s < 9; ++s) {
            if (s == 0) { WAIT_VM(6); } else if (s == 1) { WAIT_VM(7); } else if (s < 4) { WAIT_VM(6); } else if (s == 4) { WAIT_VM(11); } else if (s == 5) { WAIT_VM(12); } else if (s == 6) { WAIT_VM(7); } else if (s == 7) { WAIT_VM(5); } else { WAIT_VM(0); }
            BAR();
            if (s < 7) wstage(s % 3);
            if (s < 5) { touch(tp, lds0 + L_SCR); tp += tstep; }
            if (s == 5) { const unsigned o = (hw * 64 + lane) * 128; touch((const char*)pc0 + (o < CODE_BYTES ? o : 0u), lds0 + L_SCR); }
            if (s == 3) {
                const int cb = bid < 160 ? bid : 0, ch = cb < 80 ? cb : cb - 80;
                const int r0 = (ch < 64 ? (ch >> 1) * 20 + (ch & 1) * 4 : (ch - 64) * 40 + 16) + hw, dr = (ch < 64 ? 8 : 20) * 1280;
                const char* pr = (const char*)(cb < 80 ? woA : woB) + r0 * 1280; const unsigned lr = lds0 + hw * 1280;
                glds_run<1, 0, L_WO, false>(pr, l16, lr); glds_run<1, 0, L_WO + 5120, false>(pr + dr, l16, lr);
                if (lane < 16) { glds_run<1, 1024, L_WO + 1024, false>(pr, l16, lr); glds_run<1, 1024, L_WO + 6144, false>(pr + dr, l16, lr); }
                if (hw * 1024 + l16 < 3200) glds_run<1, 0, L_WPH, false>((const char*)wphi + hj * 3200, hw * 1024 + l16, lds0 + hw * 1024);
            }
            if (s == 4) touch(ws + WS_EPOCH + (lane & 1) * 4, lds0 + L_EP);
            if (s == 8 && bid < 160) {
                const lds_cptr r = L + L_WO + q4 * 1280 + g * 32 + p4 * 8 + hw * 128;
#pragma unroll
                for (int j = 0; j < 3; ++j)
                    if (j < 2 || hw < 2) ST128(__builtin_bit_cast(u32x4, cat8(tr_read(r + j * 512), tr_read(r + j * 512 + 5120))), WS_WOP + (bid * 640 + (j * 4 + hw) * 64 + lane) * 16);
            }
        }
        BAR();
        tag = *(const AS3(u64)*)(L + L_EP);
        MAKE_TAG();
        BAR();
        const unsigned fhoff = WS_FH + (((4 * hj + (lane & 3)) * 4 + kd) * 16 + rt) * 8;
        if (hw == 0 && lane < 4) publish(ws, fhoff, tag);
        if (kd) BAR();
        const char* kvbase = ws + WS_KV + (size_t)h * 64 * KVT;
        const char* kvw = kvbase + (size_t)(hw >> 1) * 4 * SLOT + (hw & 1) * 12288; const unsigned ldw = lds0 + (hw >> 1) * SLOT + (hw & 1) * 12288;
#define H_CHUNK(j) do { glds_run<4, (j) * SLOT, ((j) & 1) * 2 * SLOT, true>(kvw, l16, ldw); glds_run<4, (j) * SLOT + 4096, ((j) & 1) * 2 * SLOT + 4096, true>(kvw, l16, ldw); \
                        glds_run<4, (j) * SLOT + 8192, ((j) & 1) * 2 * SLOT + 8192, true>(kvw, l16, ldw); } while (0)
        if (hw == 0) wait_flags(ws, WS_FH + (h * 64 + r32) * 8, tag);
        BAR();
        {
            const char* pq = ws + WS_QA + ((size_t)h * 64 + qt * 8) * QFT;
            if (hw < 2) glds_run<3, 0, L_Q, true>(pq + hw * 3072, l16, lds0 + hw * 3072); else glds_run<2, 0, L_Q, true>(pq + 2048 + hw * 2048, l16, lds0 + 2048 + hw * 2048);
            if (hw == 3 && lane < 16) glds_run<1, 0, L_Q + 10240, true>(ws + WS_KMAX + h * 256, l16, lds0);
        }
        WAIT_VM(0);
        BAR();
        u32x4 ofb[20]; f32x4 accb[5];
#pragma unroll
        for (int i = 0; i < 5; ++i) accb[i] = f32x4{0.f, 0.f, 0.f, 0.f};
        const lds_cptr wab = L + g * 1280 + (20 * q4 + p4) * 16;
        if (tid == 512) publish(ws, WS_FT + bid * 8, tag);
        {
            if (kd) {
#pragma unroll
                for (int j = 0; j < 4; ++j) ST128(*(const AS3(u32x4)*)(L + L_IMG + hw * 4096 + j * 1024 + l16), (j < 3 || lane < 48) ? WS_KVP + ((4 * hj + hw) * 16 + rt) * 3840 + j * 1024 + l16 : OOB);
                HBAR();
            } else {
#pragma unroll
                for (int j = 0; j < 10; ++j) ST128(*(const AS3(u32x4)*)(L + L_PH + hw * 10240 + j * 1024 + l16), WS_PHQ + ((4 * hj + hw) * 64 + rt * 4) * 2560 + j * 1024 + l16);
            }
            H_CHUNK(1);
            WAIT_VM(0);
            if (lane == hw) publish(ws, fhoff + 256, tag);
            BAR();
            {
                wait_flags(ws, WS_FH + (lane < 32 ? h * 64 + 32 + lane : 2 * qt + (lane & 1)) * 8, tag);
                H_CHUNK(2);
                const int e = 64 * hw + lane;
                f32x4 ks = {0.f, 0.f, 0.f, 0.f};
#pragma unroll
                for (int j = 0; j < 16; ++j) ks += __builtin_bit_cast(f32x4, LD128(WS_KVP + (h * 16 + j) * 3840 + (e < 240 ? e : 0) * 16));
                u32x4 pf[2][2]; u32x2 pg[2]; float gt[2];
#pragma unroll
                for (int t = 0; t < 2; ++t) {
                    const unsigned o = WS_PHQ + (h * 64 + qt * 8 + 2 * hw + t) * 2560;
                    pf[t][0] = LD128(o + l16); pf[t][1] = LD128(o + 1024 + l16); pg[t] = LD64(o + 2048 + lane * 8);
                    gt[t] = __uint_as_float(LD32(WS_G + (qt * 256 + (2 * hw + t) * 32 + r32) * 4));
                }
                if (e < 240) {
                    const int ph = e / 6, d = 4 * (e - ph * 6), ph_h = ph / 20, u = ph - ph_h * 20;
                    const unsigned w0 = cvtpk(ks[0], ks[1]), w1 = cvtpk(ks[2], ks[3]);
                    lds_ptr dd = (lds_ptr)lds + L_MISC + d * 96 + ph_h * 48 + u * 2;
                    *(AS3(uint16_t)*)(dd) = (uint16_t)(w0 & 0xffffu); *(AS3(uint16_t)*)(dd + 96) = (uint16_t)(w0 >> 16);
                    *(AS3(uint16_t)*)(dd + 192) = (uint16_t)(w1 & 0xffffu); *(AS3(uint16_t)*)(dd + 288) = (uint16_t)(w1 >> 16);
                }
                *(AS3(float)*)((lds_ptr)lds + L_GATE + (64 * hw + lane) * 4) = hh ? gt[1] : gt[0];
                wait_flags(ws, WS_FT + (64 * hw + lane) * 8, tag);
                WAIT_VM(0);
                RAW_BAR();
                H_CHUNK(3);
                const lds_cptr a = L + L_MISC + r32 * 96 + hh * 48;
                const bf16x8 a0 = LDS_B128(a), a1 = LDS_B128(a + 16); const s16x4 a2 = LDS_B64(a + 32);
#pragma unroll
                for (int t = 0; t < 2; ++t) {
                    f32x16 lo = MFMA32(a0, __builtin_bit_cast(bf16x8, pf[t][0]), f32x16{});
                    lo = MFMA32(a1, __builtin_bit_cast(bf16x8, pf[t][1]), lo);
                    lo = MFMA32K8(a2, __builtin_bit_cast(s16x4, pg[t]), lo);
                    auto rb = __builtin_amdgcn_permlane32_swap(__float_as_uint(lo[8]), __float_as_uint(lo[8]), false, false);
                    const float sb = (1.0f - gt[t]) / (__uint_as_float(rb[1]) + 1e-6f);
                    const unsigned oc = WS_OC + ((qt * 8 + 2 * hw + t) * 32 + r32) * 16;
                    ST128((u32x4{cvtpk(lo[0] * sb, lo[1] * sb), cvtpk(lo[2] * sb, lo[3] * sb), cvtpk(lo[4] * sb, lo[5] * sb), cvtpk(lo[6] * sb, lo[7] * sb)}), oc + (80 + 2 * h + hh) * (SEQ * 16));
                    if (!hh) ST64((u32x2{cvtpk(lo[8] * sb, lo[9] * sb), cvtpk(lo[10] * sb, lo[11] * sb)}), oc + (80 + 64 + (h >> 1)) * (SEQ * 16) + (h & 1) * 8);
                }
            }
            WAIT_VM(0);
            HBAR();
            if (tid == 512) { publish(ws, WS_FOB + (qt * 32 + h) * 8, tag); *(AS3(unsigned)*)((lds_ptr)lds + L_FLAG) = 0u; }
            for (int ks = hw; ks < 10; ks += 4) P3_WKS(1, ks, L_RA + ks * 5120);
            RAW_BAR();
            P3_WKS(1, 10 + hw, L_RB + hw * 5120);
            wait_flags(ws, WS_FOB + (xcd * 32 + r32) * 8, tag);
            const unsigned ofo = WS_OC + ((80 + g) * SEQ + tt * 64 + 16 * hw + l15) * 16;
#pragma unroll
            for (int k = 0; k < 20; ++k) ofb[k] = LD128(ofo + 4 * k * (SEQ * 16));
            WAIT_VM(0);
            HBAR();
#pragma unroll
            for (int k = 0; k < 14; ++k)
#pragma unroll
                for (int i = 0; i < 5; ++i) accb[i] = MFMA16(LDS_B128(wab + p3_off(1, k) + i * 64), __builtin_bit_cast(bf16x8, ofb[k]), accb[i]);
            HBAR();
            for (int ks = hw; ks < 14; ks += 4) P3_WKS(0, ks, ks < 10 ? L_RA + ks * 5120 : L_RB + (ks - 10) * 5120);
            RAW_BAR();
            BAR();
            const unsigned redo = *(const AS3(unsigned)*)(L + L_FLAG);
            if (__builtin_amdgcn_readfirstlane(redo) != 0u) {
                WAIT_VM(0);
                RAW_BAR();
                H_CHUNK(0); WAIT_VM(0); BAR();
                H_CHUNK(1); WAIT_VM(0); BAR();
                H_CHUNK(2); WAIT_VM(0); BAR();
                H_CHUNK(3);
                if (tid == 512) *(AS3(unsigned)*)((lds_ptr)lds + L_FLAG) = 0u;
                WAIT_VM(0); RAW_BAR();
                P3_WKS(0, 10 + hw, L_RB + hw * 5120);
                BAR(); BAR();
            }
        }
        if (tid == 512) publish(ws, WS_FOA + (qt * 32 + h) * 8, tag);
        if (hw == 3) wait_flags(ws, WS_FOA + (xcd * 32 + r32) * 8, tag);
        else { P3_WKS(1, 14 + hw, L_RC + hw * 5120); P3_WKS(1, 17 + hw, L_RC + (3 + hw) * 5120); }
        WAIT_VM(0);
        BAR();
#pragma unroll
        for (int k = 14; k < 20; ++k)
#pragma unroll
            for (int i = 0; i < 5; ++i) accb[i] = MFMA16(LDS_B128(wab + p3_off(1, k) + i * 64), __builtin_bit_cast(bf16x8, ofb[k]), accb[i]);
        BAR();
#pragma unroll
        for (int i = 0; i < 5; ++i) *(AS3(f32x4)*)((lds_ptr)lds + L_PHB + (hw * 5 + i) * 1024 + l16) = accb[i];
        RAW_BAR();
    } else {
        const int th = wid >> 2, cw = wid & 3;
        {
            unsigned vx[2];
#pragma unroll
            for (int j = 0; j < 2; ++j) { const int row = 8 * (wid * 2 + j) + (lane >> 3); vx[j] = row * 1280 + (((lane ^ row) & 7) << 4); }
            const unsigned lx = lds0 + wid * 2048;
            const char* pw0 = wsrc(2 * wid); const char* pw1 = wsrc(2 * wid + 1);
#define P1_X(s, slot) do { glds_run<1, 0, 0, false>(xb + (s) * 128, vx[0], lx + (slot) * P1_STAGE); glds_run<1, 0, 1024, false>(xb + (s) * 128, vx[1], lx + (slot) * P1_STAGE); \
                           glds_ptr<16384>(pw0, lx + (slot) * P1_STAGE); glds_ptr<17408>(pw1, lx + (slot) * P1_STAGE); pw0 += 245760; pw1 += 245760; } while (0)
            P1_X(0, 0);
            BAR();
            P1_X(1, 1);
            const bool t3 = cw < 3 || (hj == 0 && kd == 0);
            const int xrow = 64 * th + l15;
            lds_cptr xa0 = L + xrow * 128 + ((g ^ (xrow & 7)) << 4), xa1 = L + ((xrow * 128 + ((g ^ (xrow & 7)) << 4)) ^ 64);
            const unsigned wa0 = 16384 + 8192 * (g >> 1) + 480 * (8 * (g & 1) + q4) + 128 * (g & 1) + 40 * p4 + (cw < 3 ? 160 * cw : 32);
            const unsigned wst = cw < 3 ? 8 : 160;
            lds_cptr wa[4];
#pragma unroll
            for (int i = 0; i < 3; ++i) wa[i] = L + wa0 + i * wst;
            wa[3] = cw < 3 ? L + wa0 + 24 : L + L_GW + g * 16;
            f32x4 acc[4][4];
#pragma unroll
            for (int m = 0; m < 4; ++m)
#pragma unroll
                for (int i = 0; i < 4; ++i) acc[m][i] = f32x4{0.f, 0.f, 0.f, 0.f};
            bf16x8 xA[4], wA_[4], xB[4], wB_[4];
#define P1_LOAD(X, W, ks, NT, G3) do { \
        _Pragma("unroll") for (int i = 0; i < 3; ++i) W[i] = cat8(tr_read(wa[i] + (ks) * 16384), tr_read(wa[i] + (ks) * 16384 + 1920)); \
        if (NT == 4) { if (G3) W[3] = LDS_B128(wa[3] + (ks) * 64); else W[3] = cat8(tr_read(wa[3] + (ks) * 16384), tr_read(wa[3] + (ks) * 16384 + 1920)); } \
        _Pragma("unroll") for (int m = 0; m < 4; ++m) X[m] = LDS_B128(((ks) ? xa1 : xa0) + m * 2048); } while (0)
#define P1_HALF(X, W, XN, WN, ks, NT, G3, MID1, MID2) do { \
        _Pragma("unroll") for (int i = 0; i < NT; ++i) { \
            s16x4 lo_, hi_; \
            acc[0][i] = MFMA16(W[i], X[0], acc[0][i]); SBAR(); \
            if (i == 3 && G3) WN[3] = LDS_B128(wa[3] + (ks) * 64); else lo_ = tr_read(wa[i] + (ks) * 16384); SBAR(); \
            acc[1][i] = MFMA16(W[i], X[1], acc[1][i]); SBAR(); \
            if (!(i == 3 && G3)) { hi_ = tr_read(wa[i] + (ks) * 16384 + 1920); WN[i] = cat8(lo_, hi_); } SBAR(); \
            acc[2][i] = MFMA16(W[i], X[2], acc[2][i]); SBAR(); \
            XN[i] = LDS_B128(((ks) ? xa1 : xa0) + i * 2048); SBAR(); \
            acc[3][i] = MFMA16(W[i], X[3], acc[3][i]); SBAR(); \
            if (NT == 3 && i == 2) XN[3] = LDS_B128(((ks) ? xa1 : xa0) + 3 * 2048); \
            if (i == 0) { MID1; } if (i == 1) { MID2; } SBAR(); \
        } } while (0)
#define P1_DX(lim, st, sl) do { unsigned sv_; const char* xs_ = xb + (st) * 128; \
                  asm volatile("s_cmp_gt_u32 %1, " #lim "\n\ts_cbranch_scc1 1f\n\ts_mov_b32 %0, m0\n\ts_mov_b32 m0, %5\n\ts_nop 0\n\tglobal_load_lds_dwordx4 %2, %4\n\t" \
                               "s_add_u32 m0, %5, 1024\n\ts_nop 0\n\tglobal_load_lds_dwordx4 %3, %4\n\ts_mov_b32 m0, %0\n1:" \
                               : "=&s"(sv_) : "s"(s), "v"(vx[0]), "v"(vx[1]), "s"(xs_), "s"(lx + (sl) * P1_STAGE) : "memory", "scc"); } while (0)
#define P1_DW(lim, sl) do { unsigned sv_; \
                  asm volatile("s_cmp_gt_u32 %3, " #lim "\n\ts_cbranch_scc1 1f\n\ts_mov_b32 %0, m0\n\ts_add_u32 m0, %4, 16384\n\ts_nop 0\n\tglobal_load_lds_dwordx4 %1, off\n\t" \
                               "s_add_u32 m0, %4, 17408\n\tv_lshl_add_u64 %1, %1, 0, %5\n\tglobal_load_lds_dwordx4 %2, off\n\ts_mov_b32 m0, %0\n\tv_lshl_add_u64 %2, %2, 0, %5\n1:" \
                               : "=&s"(sv_), "+v"(pw0), "+v"(pw1) : "s"(s), "s"(lx + (sl) * P1_STAGE), "s"(245760ull) : "memory", "scc"); } while (0)
#define P1_LOOP(NT, G3) do { \
            P1_LOAD(xA, wA_, 0, NT, G3); \
            int slot = 0; \
            _Pragma("nounroll") for (int s = 0; s < 10; ++s) { \
                P1_HALF(xA, wA_, xB, wB_, 1, NT, G3, P1_DX(0, 2, 2), P1_DW(0, 2)); \
                  \
                asm volatile("s_cmp_gt_u32 %0, 7\n\ts_cbranch_scc1 1f\n\ts_waitcnt vmcnt(4) lgkmcnt(0)\n\ts_barrier\n\ts_branch 2f\n" \
                             "1:\n\ts_cmp_eq_u32 %0, 9\n\ts_cbranch_scc1 2f\n\ts_waitcnt vmcnt(0) lgkmcnt(0)\n\ts_barrier\n2:" :: "s"(s) : "memory", "scc"); \
                const int d = slot == 2 ? -2 * P1_STAGE : P1_STAGE; \
                xa0 += d; xa1 += d; wa[0] += d; wa[1] += d; wa[2] += d; wa[3] += G3 ? 128 : d; SBAR(); \
                P1_HALF(xB, wB_, xA, wA_, 0, NT, G3, P1_DX(6, s + 3, slot), P1_DW(6, slot)); \
                slot = slot == 2 ? 0 : slot + 1; \
            } } while (0)
            WAIT_VM(4);
            BAR();
            if (cw < 3) P1_LOOP(4, false); else if (t3) P1_LOOP(4, true); else P1_LOOP(3, false);
            const lds_ptr img = (lds_ptr)lds + L_IMG;
            {
                const int ht = (4 * hj + g) * 64 + rt * 4 + 2 * th;
                float kn = 0.f;
                if (cw < 3) {
                    const bool isv = kd ? cw != 1 : false, toA = kd ? cw == 0 : cw < 2;
                    if (kd == 0 && cw == 0) {
#pragma unroll
                        for (int m = 0; m < 4; ++m)
#pragma unroll
                            for (int i = 0; i < 4; ++i) acc[m][i] *= QSCALE;
                    }
                    const unsigned ts = (kd == 0 && cw == 0) ? QFT : KVT, tb = ((kd == 0 && cw == 0) ? WS_QA : WS_KV + (kd ? 1536 : 0)) + ht * ts;
                    const unsigned o0 = l15 * 16, o1 = 512 + (isv ? l15 ^ 4 : l15) * 16;
#pragma unroll
                    for (int m = 0; m < 4; ++m) {
                        const u32x4 w0 = {cvtpk(acc[m][0][0], acc[m][0][1]), cvtpk(acc[m][0][2], acc[m][0][3]), cvtpk(acc[m][1][0], acc[m][1][1]), cvtpk(acc[m][1][2], acc[m][1][3])};
                        const u32x4 w1 = {cvtpk(acc[m][2][0], acc[m][2][1]), cvtpk(acc[m][2][2], acc[m][2][3]), cvtpk(acc[m][3][0], acc[m][3][1]), cvtpk(acc[m][3][2], acc[m][3][3])};
                        if (toA) { const unsigned mo = tb + (m >> 1) * ts + (m & 1) * 256; ST128(w0, mo + o0); ST128(w1, mo + o1); }
                        else { const lds_ptr d = img + (((isv ? 4 : 0) + 2 * th + (m >> 1)) * 4 + g) * 1536 + (m & 1) * 256; *(AS3(u32x4)*)(d + o0) = w0; *(AS3(u32x4)*)(d + o1) = w1; }
                    }
                    if (kd == 0 && cw == 1) {
#pragma unroll
                        for (int m = 0; m < 4; ++m) {
                            float n2 = 0.f;
#pragma unroll
                            for (int i = 0; i < 4; ++i)
#pragma unroll
                                for (int r = 0; r < 4; ++r) n2 = fmaf(acc[m][i][r], acc[m][i][r], n2);
                            kn = (n2 == n2) ? fmaxf(kn, n2) : __builtin_inff();
                        }
                    }
                } else {
#pragma unroll
                    for (int m = 0; m < 4; ++m) {
                        const unsigned ok = WS_KV + (ht + (m >> 1)) * KVT + 1024, o8 = (m & 1) * 128 + l15 * 8, o16 = (m & 1) * 256 + (l15 ^ 8) * 16;
                        const lds_ptr d = img + ((2 * th + (m >> 1)) * 4 + g) * 1536 + 1024;
                        const u32x2 w1 = {cvtpk(acc[m][1][0], acc[m][1][1]), cvtpk(acc[m][1][2], acc[m][1][3])}, w2 = {cvtpk(acc[m][2][0], acc[m][2][1]), cvtpk(acc[m][2][2], acc[m][2][3])};
                        if (kd == 0) {
                            ST64((u32x2{cvtpk(acc[m][0][0] * QSCALE, acc[m][0][1] * QSCALE), cvtpk(acc[m][0][2] * QSCALE, acc[m][0][3] * QSCALE)}), WS_QA + (ht + (m >> 1)) * QFT + 1024 + o8);
                            ST128((u32x4{w1[0], w1[1], 0x3f803f80u, 0x00003f80u}), ok + (m & 1) * 256 + l15 * 16);
                            *(AS3(u32x2)*)(d + o8) = w2;
                            float n2 = 0.f;
#pragma unroll
                            for (int r = 0; r < 4; ++r) n2 = fmaf(acc[m][1][r], acc[m][1][r], n2);
                            kn = (n2 == n2) ? fmaxf(kn, n2) : __builtin_inff();
                        } else {
                            ST128((u32x4{cvtpk(acc[m][0][0], acc[m][0][1]), cvtpk(acc[m][0][2], acc[m][0][3]), 0x00003f80u, 0u}), ok + 1536 + o16);
                            *(AS3(u32x2)*)(d + o8) = w1;
                            *(AS3(u32x4)*)(d + 16 * 1536 + o16) = u32x4{w2[0], w2[1], 0x00003f80u, 0u};
                        }
                    }
                }
                if (cw == 3 && t3 && g == 0) {
#pragma unroll
                    for (int m = 0; m < 4; ++m) ST32(__float_as_uint(1.0f / (1.0f + __expf(-acc[m][3][0]))), WS_G + (rt * 128 + 64 * th + 16 * m + l15) * 4);
                }
                if (kd == 0 && (cw & 1)) {
                    kn = row_max16(kn);
                    if (l15 == 0) ST32(__float_as_uint(kn), WS_KMAX + ((4 * hj + g) * 64 + (cw == 3 ? 32 : 0) + rt * 2 + th) * 4);
                }
            }
            u32x4 wp0; u32x2 wp1;
            {
                const int fr = (r32 & 3) + 4 * (r32 >> 3);
                const lds_cptr wp = L + L_WPH + cw * 800 + (fr < 10 ? 10 * ((r32 >> 2) & 1) + fr : 0) * 2;
                unsigned wv[12];
#pragma unroll
                for (int j = 0; j < 8; ++j) wv[j] = *(const AS3(uint16_t)*)(wp + hh * 320 + j * 40);
#pragma unroll
                for (int j = 0; j < 4; ++j) wv[8 + j] = *(const AS3(uint16_t)*)(wp + 640 + j * 40);
                const unsigned m0 = fr < 10 ? 0xffffffffu : 0u, m1 = (fr < 10 && hh == 0) ? 0xffffffffu : 0u;
                wp0 = u32x4{(wv[0] | (wv[1] << 16)) & m0, (wv[2] | (wv[3] << 16)) & m0, (wv[4] | (wv[5] << 16)) & m0, (wv[6] | (wv[7] << 16)) & m0};
                wp1 = u32x2{(wv[8] | (wv[9] << 16)) & m1, (wv[10] | (wv[11] << 16)) & m1};
            }
            RAW_BAR();
#pragma unroll
            for (int t2 = 0; t2 < 2; ++t2) {
                const lds_cptr f = L + L_IMG + ((2 * th + t2) * 4 + cw) * 1536;
                unsigned ph[10];
                feature_map(wp0, wp1, *(const AS3(u32x4)*)(f + ka), *(const AS3(u32x2)*)(f + kb), ph);
                const lds_ptr d = (lds_ptr)lds + L_PH + (cw * 4 + 2 * th + t2) * 2560;
                if (kd) {
#pragma unroll
                    for (int u = 0; u < 5; ++u) *(AS3(u32x2)*)(d + r32 * 80 + hh * 40 + u * 8) = u32x2{ph[2 * u], ph[2 * u + 1]};
                } else {
                    *(AS3(u32x4)*)(d + l16) = u32x4{ph[0], ph[1], ph[2], ph[3]}; *(AS3(u32x4)*)(d + 1024 + l16) = u32x4{ph[4], ph[5], ph[6], ph[7]};
                    *(AS3(u32x2)*)(d + 2048 + lane * 8) = u32x2{ph[8], ph[9]};
                }
                if (t2 == 0) {
                    SBAR();
                    WAIT_VM(0);
                    BAR();
                    SBAR();
                }
            }
            if (kd) {
                RAW_BAR();
#pragma unroll
                for (int k2 = 0; k2 < 2; ++k2) {
                    if (k2 == 0 || wid < 4) {
                        const int u = wid + 8 * k2, uh = u / 3, un = u - 3 * uh;
                        f32x4 c0 = {0.f, 0.f, 0.f, 0.f}, c1 = {0.f, 0.f, 0.f, 0.f};
                        const unsigned vo = L_IMG + (16 + uh) * 1536;
                        const unsigned vb = 512 * (p4 >> 1) + 256 * (g >> 1) + 64 * ((2 * (g & 1)) ^ (p4 >> 1)) + 16 * q4 + 8 * (p4 & 1);
                        const unsigned vc = 1024 + 256 * (g >> 1) + 64 * ((2 * (g & 1)) ^ 2) + 16 * q4 + (p4 ? 8 : 0);
                        const unsigned pb = L_PH + (uh * 128 + 8 * g + q4) * 80 + un * 32 + p4 * 8;
#pragma unroll
                        for (int ks = 0; ks < 4; ++ks) {
                            const bf16x8 a0 = cat8(tr_read(L + vo + vb + ks * 6144), tr_read(L + vo + (vb ^ 64) + ks * 6144));
                            const bf16x8 a1 = cat8(tr_read(L + vo + vc + ks * 6144), tr_read(L + vo + (vc ^ 64) + ks * 6144));
                            const bf16x8 b = cat8(tr_read(L + pb + ks * 2560), tr_read(L + pb + ks * 2560 + 320));
                            c0 = MFMA16(a0, b, c0); c1 = MFMA16(a1, b, c1);
                        }
                        const int ph = 16 * un + l15;
                        if (ph < 40) {
                            const lds_ptr d = (lds_ptr)lds + L_IMG + uh * 4096 + ph * 96 + 16 * g;
                            *(AS3(f32x4)*)(d) = c0; if (g < 2) *(AS3(f32x4)*)(d + 64) = c1;
                        }
                    }
                }
            }
        }
        const int qp = wid & 3, kh = wid >> 2;
        const int own = 2 * qp + kh;
        const int blk = (lane >> 4) & 1, vj = blk ? 2 : p4 >> 1;
        const unsigned kb2 = 1024 + r32 * 16 + hh * 8;
        const unsigned va = 1536 + 512 * vj + 64 * ((2 * hh) ^ vj) + 16 * q4 + (blk ? (p4 ? 8 : 0) : 8 * (p4 & 1)), vb2 = va ^ 64;
        RAW_BAR();
        {
            const char* src = ws + WS_KV + ((size_t)h * 64 + kh * 32) * KVT + qp * 6144; const unsigned dst = lds0 + kh * SLOT + qp * 6144;
            glds_run<4, 0, 0, true>(src, l16, dst); glds_run<2, 4096, 4096, true>(src, l16, dst);
        }
        WAIT_VM(0);
        BAR();
        const lds_cptr lq = L + L_Q + 2 * qp * QFT;
        const u32x4 qa0 = *(const AS3(u32x4)*)(lq + ka), qb0 = *(const AS3(u32x4)*)(lq + QFT + ka);
        u32x2 qa1 = *(const AS3(u32x2)*)(lq + kb), qb1 = *(const AS3(u32x2)*)(lq + QFT + kb);
        float kmx = *(const AS3(float)*)(L + L_Q + 10240 + lane * 4);
        if (hh) { qa1 = u32x2{0u, 0u}; qb1 = u32x2{0u, 0u}; }
        const bf16x8 QA0 = __builtin_bit_cast(bf16x8, qa0), QB0 = __builtin_bit_cast(bf16x8, qb0);
        float nba, nbb;
        {
            kmx = row_max16(kmx);
            auto r = __builtin_amdgcn_permlane16_swap(__float_as_uint(kmx), __float_as_uint(kmx), false, false);
            kmx = fmaxf(__uint_as_float(r[0]), __uint_as_float(r[1]));
            const float kn = -1.01f * __builtin_amdgcn_sqrtf(xhalf_sum(kmx));
            nba = kn * __builtin_amdgcn_sqrtf(qnorm2(qa0, qa1)); nbb = kn * __builtin_amdgcn_sqrtf(qnorm2(qb0, qb1));
        }
        for (int attempt = 0;; ++attempt) {
            f32x16 oa, ob, sA, sB;
#pragma unroll
            for (int r = 0; r < 16; ++r) { oa[r] = 0.f; ob[r] = 0.f; sB[r] = 0.f; }
            const s16x4 QA1 = __builtin_bit_cast(s16x4, hh ? split3(nba) : qa1), QB1 = __builtin_bit_cast(s16x4, hh ? split3(nbb) : qb1);
            lds_cptr pka = L + kh * SLOT + ka, pkb = L + kh * SLOT + kb2, pv = L + kh * SLOT + va, pv2 = L + kh * SLOT + vb2;
            KFrag k0 = ld_k(pka, pkb, 0), k1 = ld_k(pka, pkb, KVT);
            VFrag v0 = ld_v(pv, 0), v1 = v0;
            sA = MFMA32(k0.k0, QA0, f32x16{}); sA = MFMA32K8(k0.k1, QA1, sA);
            for (int c = 0; c < 4; ++c) {
                const int dnext = (c & 1) ? -2 * SLOT : 2 * SLOT;
#define K2_PAIR(t, LOADK0, LOADV0, LOADK1) do { \
                bf16x8 vp = v1.v1; \
                v1 = ld_v(pv, ((t) + 1) * KVT); SBAR(); \
                attn_step(sA, sB, k0, vp, v0.v0, QB0, QB1, ob, oa); \
                LOADK0; SBAR(); \
                attn_step(sB, sA, k1, v0.v1, v0.v0, QA0, QA1, oa, ob); \
                vp = v0.v1; \
                LOADV0; SBAR(); \
                attn_step(sA, sB, k1, vp, v1.v0, QB0, QB1, ob, oa); \
                LOADK1; SBAR(); \
                attn_step(sB, sA, k0, v1.v1, v1.v0, QA0, QA1, oa, ob); } while (0)
#define PRIO(p) do { asm volatile("s_cmp_eq_u32 %0, 0\n\ts_cbranch_scc1 1f\n\ts_setprio " #p "\n1:" :: "s"(kh) : "scc"); SBAR(); } while (0)
                K2_PAIR(0, k0 = ld_k(pka, pkb, 2 * KVT), v0 = ld_v(pv, 2 * KVT), k1 = ld_k(pka, pkb, 3 * KVT));
                K2_PAIR(2, k0 = ld_k(pka, pkb, 4 * KVT), v0 = ld_v(pv, 4 * KVT), k1 = ld_k(pka, pkb, 5 * KVT));
                PRIO(0);
                K2_PAIR(4, k0 = ld_k(pka, pkb, 6 * KVT), v0 = ld_v(pv, 6 * KVT), k1 = ld_k(pka, pkb, 7 * KVT));
#define TURN() do { asm volatile("s_cmp_eq_u32 %0, 3\n\ts_cbranch_scc1 1f\n\ts_waitcnt vmcnt(0) lgkmcnt(0)\n\ts_barrier\n1:" :: "s"(c) : "memory", "scc"); \
                    pka += dnext; pkb += dnext; pv += dnext; pv2 += dnext; k0 = ld_k(pka, pkb, 0); PRIO(1); } while (0)
                K2_PAIR(6, TURN(), v0 = ld_v(pv, 0), k1 = ld_k(pka, pkb, KVT));
            }
            ob = SMF(v1.v1, HI8(sB), ob);
            PRIO(0);
            {
                lds_ptr d = (lds_ptr)lds + L_X + (own ^ 1) * 3072 + l16;
#pragma unroll
                for (int a = 0; a < 3; ++a)
                    *(AS3(f32x4)*)(d + a * 1024) = kh ? f32x4{oa[4 * a], oa[4 * a + 1], oa[4 * a + 2], oa[4 * a + 3]} : f32x4{ob[4 * a], ob[4 * a + 1], ob[4 * a + 2], ob[4 * a + 3]};
            }
            RAW_BAR();
            float o[12];
#pragma unroll
            for (int a = 0; a < 3; ++a) {
                const f32x4 t = *(const AS3(f32x4)*)(L + L_X + own * 3072 + l16 + a * 1024);
#pragma unroll
                for (int r = 0; r < 4; ++r) o[4 * a + r] = t[r] + (kh ? ob[4 * a + r] : oa[4 * a + r]);
            }
            auto rr = __builtin_amdgcn_permlane32_swap(__float_as_uint(o[8]), __float_as_uint(o[8]), false, false);
            const float lsum = __uint_as_float(rr[1]);
            const float sa = *(const AS3(float)*)(L + L_GATE + (own * 32 + r32) * 4) * __builtin_amdgcn_rcpf(lsum);
            const unsigned ocb = WS_OC + ((qt * 8 + own) * 32 + r32) * 16;
            ST128((u32x4{cvtpk(o[0] * sa, o[1] * sa), cvtpk(o[2] * sa, o[3] * sa), cvtpk(o[4] * sa, o[5] * sa), cvtpk(o[6] * sa, o[7] * sa)}), ocb + (2 * h + hh) * (SEQ * 16));
            if (!hh) ST64((u32x2{cvtpk(o[8] * sa, o[9] * sa), cvtpk(o[10] * sa, o[11] * sa)}), ocb + (64 + (h >> 1)) * (SEQ * 16) + (h & 1) * 8);
            if (attempt == 0 && __any(!(lsum >= 5.4210109e-20f)))
                *(AS3(unsigned)*)((lds_ptr)lds + L_FLAG) = 1u;
            WAIT_VM(0);
            RAW_BAR();
            const unsigned redo = *(const AS3(unsigned)*)(L + L_FLAG);
            if (__builtin_amdgcn_readfirstlane(redo) == 0u) break;
            float ma = -__builtin_inff(), mb = -__builtin_inff();
            for (int t = 0; t < 64; ++t) {
                const u32x4 g0 = LD128(WS_KV + (h * 64 + t) * KVT + ka);
                const u32x2 g1 = LD64(WS_KV + (h * 64 + t) * KVT + kb2);
                f32x16 s = MFMA32(__builtin_bit_cast(bf16x8, g0), QA0, f32x16{}); s = MFMA32K8(__builtin_bit_cast(s16x4, g1), __builtin_bit_cast(s16x4, qa1), s);
                f32x16 u = MFMA32(__builtin_bit_cast(bf16x8, g0), QB0, f32x16{}); u = MFMA32K8(__builtin_bit_cast(s16x4, g1), __builtin_bit_cast(s16x4, qb1), u);
#pragma unroll
                for (int r = 0; r < 16; ++r) { ma = fmaxf(ma, s[r]); mb = fmaxf(mb, u[r]); }
            }
            nba = -xhalf_max(ma); nbb = -xhalf_max(mb);
            RAW_BAR();
            BAR();
        }
        {
            const int mt = wid & 3, kp = wid >> 2;
            if (wid < 6) P3_WKS(0, 14 + wid, wid < 3 ? L_RD + wid * 5120 : (wid - 3) * 5120);
            const unsigned ofo = WS_OC + ((4 * kp + g) * SEQ + tt * 64 + 16 * mt + l15) * 16;
            const lds_cptr wa = L + g * 1280 + (20 * q4 + p4) * 16;
            f32x4 acc[5];
#pragma unroll
            for (int i = 0; i < 5; ++i) acc[i] = f32x4{0.f, 0.f, 0.f, 0.f};
            WAIT_VM(0);
            BAR();
            u32x4 of[10];
#pragma unroll
            for (int k = 0; k < 10; ++k) of[k] = LD128(ofo + (8 * k) * (SEQ * 16));
            if (kp) {
#pragma unroll
                for (int k = 0; k < 10; ++k)
#pragma unroll
                    for (int i = 0; i < 5; ++i) acc[i] = MFMA16(LDS_B128(wa + p3_off(0, 2 * k + 1) + i * 64), __builtin_bit_cast(bf16x8, of[k]), acc[i]);
            } else {
#pragma unroll
                for (int k = 0; k < 10; ++k)
#pragma unroll
                    for (int i = 0; i < 5; ++i) acc[i] = MFMA16(LDS_B128(wa + p3_off(0, 2 * k) + i * 64), __builtin_bit_cast(bf16x8, of[k]), acc[i]);
            }
            RAW_BAR();
            if (kp) {
#pragma unroll
                for (int i = 0; i < 5; ++i) *(AS3(f32x4)*)((lds_ptr)lds + L_PC + (mt * 5 + i) * 1024 + l16) = acc[i];
            }
            RAW_BAR();
            if (!kp) {
                char* dst = (char*)out + (size_t)(tt * 64 + 16 * mt + l15) * 1280 + ct * 160 + g * 40;
#pragma unroll
                for (int i = 0; i < 5; ++i) {
                    const f32x4 v = acc[i] + *(const AS3(f32x4)*)(L + L_PC + (mt * 5 + i) * 1024 + l16) + *(const AS3(f32x4)*)(L + L_PHB + (mt * 5 + i) * 1024 + l16);
                    *(u32x2*)(dst + i * 8) = u32x2{cvtpk(v[0], v[1]), cvtpk(v[2], v[3])};
                }
            }
        }
    }
    if (bid == 0 && tid == 512) publish(ws, WS_EPOCH, tag);
}

extern "C" void kernel_launch(void* const* d_in, const int* in_sizes, int n_in, void* d_out, int out_size, void* d_ws, size_t ws_size,
                              hipStream_t stream) {
    fused_kernel<<<256, 768, 0, stream>>>((const uint16_t*)d_in[0], (const uint16_t*)d_in[1], (const uint16_t*)d_in[4], (const uint16_t*)d_in[2],
                                          (const uint16_t*)d_in[6], (const uint16_t*)d_in[5], (const uint16_t*)d_in[7], (char*)d_ws, (uint16_t*)d_out);
}
```

```cpp
#include <hip/hip_runtime.h>
#include <hip/hip_bf16.h>
#include <stdint.h>

typedef __attribute__((ext_vector_type(8))) __bf16 bf16x8;
typedef __attribute__((ext_vector_type(16))) __bf16 bf16x16;
typedef __attribute__((ext_vector_type(4))) short s16x4;
typedef __attribute__((ext_vector_type(8))) short s16x8;
typedef __attribute__((ext_vector_type(4))) float f32x4;
typedef __attribute__((ext_vector_type(16))) float f32x16;
typedef __attribute__((ext_vector_type(4))) unsigned u32x4;
typedef __attribute__((ext_vector_type(2))) unsigned u32x2;
typedef __attribute__((ext_vector_type(2))) __bf16 bf16x2;
typedef __attribute__((ext_vector_type(2))) float f32x2;
typedef __attribute__((address_space(3))) const char* lds_cptr;
typedef __attribute__((address_space(3))) char* lds_ptr;
typedef unsigned long long u64;
#define AS3(T) __attribute__((address_space(3))) T

constexpr unsigned CODE_BYTES = 30000;
constexpr int SEQ = 2048, NH = 32;
constexpr unsigned OOB = 0x7ffffff0u;
constexpr float LOG2E = 1.4426950408889634f;
constexpr float QSCALE = 0.22360679774997896f * 1.4426950408889634f;

constexpr int KVT = 3072;
constexpr int QFT = 1280;
constexpr unsigned WS_KV = 0;
constexpr unsigned WS_QA = WS_KV + NH * 64 * KVT;
constexpr unsigned WS_PHQ = WS_QA + NH * 64 * QFT;
constexpr unsigned WS_WOP = WS_PHQ + NH * 64 * 2560;
constexpr unsigned WS_OC = WS_WOP + 160 * 640 * 16;
constexpr unsigned WS_KMAX = WS_OC + 160 * SEQ * 16;
constexpr unsigned WS_G = WS_KMAX + NH * 64 * 4;
constexpr unsigned WS_KVP = WS_G + SEQ * 4;
constexpr unsigned WS_FT = WS_KVP + NH * 16 * 3840;
constexpr unsigned WS_FH = WS_FT + 2048;
constexpr unsigned WS_FOB = WS_FH + 16384;
constexpr unsigned WS_FOA = WS_FOB + 2048;
constexpr unsigned WS_EPOCH = WS_FOA + 2048;
constexpr unsigned WS_END = WS_EPOCH + 128;

__device__ __forceinline__ unsigned cvtpk(float lo, float hi) {
    return __builtin_bit_cast(unsigned, __builtin_convertvector(f32x2{lo, hi}, bf16x2)); }
__device__ __forceinline__ float bflo(unsigned w) { return __uint_as_float(w << 16); }
__device__ __forceinline__ float bfhi(unsigned w) { return __uint_as_float(w & 0xffff0000u); }
#define GL1(P) "global_load_lds_dwordx4 %1, %2" P "\n\t"
#define GL2(P) GL1(P) "global_load_lds_dwordx4 %1, %2 offset:1024" P "\n\t"
#define GL3(P) GL2(P) "global_load_lds_dwordx4 %1, %2 offset:2048" P "\n\t"
#define GL4(P) GL3(P) "global_load_lds_dwordx4 %1, %2 offset:3072" P "\n\t"
#define GLASM(BODY) asm volatile("s_mov_b32 %0, m0\n\ts_add_u32 m0, %3, %4\n\ts_nop 0\n\t" BODY "s_mov_b32 m0, %0" \
                     : "=&s"(sv) : "v"(voff), "s"(t), "s"(lbase), "i"(LOFF) : "memory", "scc")
template <int N, int GOFF, int LOFF, bool SC1>
__device__ __forceinline__ void glds_run(const void* sbase, unsigned voff, unsigned lbase) {
    static_assert(N >= 1 && N <= 4, "");
    unsigned sv, tl, th;
    lbase = __builtin_amdgcn_readfirstlane(lbase);
    const unsigned lo = __builtin_amdgcn_readfirstlane((unsigned)(uintptr_t)sbase), hi = __builtin_amdgcn_readfirstlane((unsigned)((uintptr_t)sbase >> 32));
    asm volatile("s_add_u32 %0, %2, %4\n\ts_addc_u32 %1, %3, 0" : "=&s"(tl), "=s"(th) : "s"(lo), "s"(hi), "i"(GOFF) : "scc");
    const uint64_t t = ((uint64_t)th << 32) | tl;
    if constexpr (SC1) {
        if constexpr (N == 1) GLASM(GL1(" sc1")); else if constexpr (N == 2) GLASM(GL2(" sc1")); else if constexpr (N == 3) GLASM(GL3(" sc1")); else GLASM(GL4(" sc1"));
    } else {
        if constexpr (N == 1) GLASM(GL1("")); else if constexpr (N == 2) GLASM(GL2("")); else if constexpr (N == 3) GLASM(GL3("")); else GLASM(GL4(""));
    }
}
template <int LOFF>
__device__ __forceinline__ void glds_ptr(const void* p, unsigned lbase) {
    unsigned sv; lbase = __builtin_amdgcn_readfirstlane(lbase);
    asm volatile("s_mov_b32 %0, m0\n\ts_add_u32 m0, %2, %3\n\ts_nop 0\n\tglobal_load_lds_dwordx4 %1, off\n\ts_mov_b32 m0, %0"
                 : "=&s"(sv) : "v"(p), "s"(lbase), "i"(LOFF) : "memory", "scc"); }
__device__ __forceinline__ void touch(const void* p, unsigned lds_scratch) {
    unsigned sv;
    asm volatile("s_mov_b32 %0, m0\n\ts_mov_b32 m0, %2\n\ts_nop 0\n\tglobal_load_lds_dword %1, off sc1\n\ts_mov_b32 m0, %0" : "=&s"(sv) : "v"(p), "s"(lds_scratch) : "memory"); }
__device__ __forceinline__ s16x4 tr_read(lds_cptr p) { return __builtin_amdgcn_ds_read_tr16_b64_v4i16((AS3(s16x4)*)p); }
__device__ __forceinline__ bf16x8 cat8(s16x4 a, s16x4 b) {
    s16x8 r = {a[0], a[1], a[2], a[3], b[0], b[1], b[2], b[3]}; return __builtin_bit_cast(bf16x8, r); }
#define LDS_B128(p) (*(const AS3(bf16x8)*)(p))
#define LDS_B64(p) (*(const AS3(s16x4)*)(p))
#define MFMA16(a, b, c) __builtin_amdgcn_mfma_f32_16x16x32_bf16(a, b, c, 0, 0, 0)
#define MFMA32(a, b, c) __builtin_amdgcn_mfma_f32_32x32x16_bf16(a, b, c, 0, 0, 0)
#define MFMA32K8(a, b, c) __builtin_amdgcn_mfma_f32_32x32x8bf16_1k(a, b, c, 0, 0, 0)
#define WAIT_VM_(n) asm volatile("s_waitcnt vmcnt(" #n ")" ::: "memory")
#define WAIT_VM(n) WAIT_VM_(n)
#define RAW_BAR() asm volatile("s_waitcnt lgkmcnt(0)\n\ts_barrier" ::: "memory")
#define SBAR() __builtin_amdgcn_sched_barrier(0)
#define EXP2(v) __builtin_amdgcn_exp2f(v)
#define SMF(a, lo, acc) __builtin_amdgcn_smfmac_f32_32x32x32_bf16(a, __builtin_bit_cast(bf16x16, lo), acc, 0xDDDD, 0, 0)
#define LO8(s) __builtin_shufflevector(s, s, 0, 1, 2, 3, 4, 5, 6, 7)
#define HI8(s) __builtin_shufflevector(s, s, 8, 9, 10, 11, 12, 13, 14, 15)
#define ST128(v, off) __builtin_amdgcn_raw_buffer_store_b128(v, wsr, off, 0, 16)
#define ST64(v, off) __builtin_amdgcn_raw_buffer_store_b64(v, wsr, off, 0, 16)
#define ST32(v, off) __builtin_amdgcn_raw_buffer_store_b32(v, wsr, off, 0, 16)
__device__ __forceinline__ float xhalf_max(float v) {
    auto r = __builtin_amdgcn_permlane32_swap(__float_as_uint(v), __float_as_uint(v), false, false);
    return fmaxf(__uint_as_float(r[0]), __uint_as_float(r[1])); }
__device__ __forceinline__ float xhalf_min(float v) {
    auto r = __builtin_amdgcn_permlane32_swap(__float_as_uint(v), __float_as_uint(v), false, false);
    return fminf(__uint_as_float(r[0]), __uint_as_float(r[1])); }
__device__ __forceinline__ float xhalf_sum(float v) {
    auto r = __builtin_amdgcn_permlane32_swap(__float_as_uint(v), __float_as_uint(v), false, false);
    return __uint_as_float(r[0]) + __uint_as_float(r[1]); }
__device__ __forceinline__ float row_max16(float v) {
    v = fmaxf(v, __int_as_float(__builtin_amdgcn_update_dpp(0, __float_as_int(v), 0x128, 0xf, 0xf, false)));
    v = fmaxf(v, __int_as_float(__builtin_amdgcn_update_dpp(0, __float_as_int(v), 0x124, 0xf, 0xf, false)));
    v = fmaxf(v, __int_as_float(__builtin_amdgcn_update_dpp(0, __float_as_int(v), 0x122, 0xf, 0xf, false)));
    v = fmaxf(v, __int_as_float(__builtin_amdgcn_update_dpp(0, __float_as_int(v), 0x121, 0xf, 0xf, false)));
    return v; }
__device__ __forceinline__ void publish(char* ws, unsigned off, u64 tag) {
    __hip_atomic_store((u64*)(ws + off), tag, __ATOMIC_RELAXED, __HIP_MEMORY_SCOPE_AGENT); }
__device__ __forceinline__ void wait_flags(const char* ws, unsigned off, u64 tag) {
    for (int spin = 0; spin < (1 << 20); ++spin) {
        const u64 f = __hip_atomic_load((const u64*)(ws + off), __ATOMIC_RELAXED, __HIP_MEMORY_SCOPE_AGENT);
        if (__all(f == tag)) break;
        __builtin_amdgcn_s_sleep(4);
    }
    asm volatile("" ::: "memory");
}

struct KFrag { bf16x8 k0; s16x4 k1; };
struct VFrag { bf16x8 v0, v1; };
__device__ __forceinline__ KFrag ld_k(lds_cptr pa, lds_cptr pb, int off) { KFrag k; k.k0 = LDS_B128(pa + off); k.k1 = LDS_B64(pb + off); return k; }
__device__ __forceinline__ VFrag ld_v2(lds_cptr p, lds_cptr q, int off) {
    VFrag v; v.v0 = cat8(tr_read(p + off), tr_read(q + off)); v.v1 = cat8(tr_read(p + off + 256), tr_read(q + off + 256)); return v; }
#define ld_v(p, off) ld_v2(p, p##2, off)
__device__ __forceinline__ void attn_step(f32x16& sc, f32x16& sn, const KFrag& kn, const bf16x8& vprev1, const bf16x8& vc0,
                                          const bf16x8& qn0, const s16x4& qn1, f32x16& oprev, f32x16& oc) {
    oprev = SMF(vprev1, HI8(sn), oprev); SBAR();
#pragma unroll
    for (int r = 0; r < 4; ++r) sc[r] = EXP2(sc[r]);
    SBAR(); sn = MFMA32(kn.k0, qn0, f32x16{}); SBAR();
#pragma unroll
    for (int r = 4; r < 8; ++r) sc[r] = EXP2(sc[r]);
    SBAR(); sn = MFMA32K8(kn.k1, qn1, sn); SBAR();
#pragma unroll
    for (int r = 8; r < 12; ++r) sc[r] = EXP2(sc[r]);
    SBAR(); oc = SMF(vc0, LO8(sc), oc); SBAR();
#pragma unroll
    for (int r = 12; r < 16; ++r) sc[r] = EXP2(sc[r]);
    SBAR();
}
__device__ __forceinline__ void feature_map(const u32x4 wp0, const u32x2 wp1, const u32x4 f0, const u32x2 f1, unsigned (&ph)[10]) {
    f32x16 pq = MFMA32(__builtin_bit_cast(bf16x8, wp0), __builtin_bit_cast(bf16x8, f0), f32x16{});
    pq = MFMA32K8(__builtin_bit_cast(s16x4, wp1), __builtin_bit_cast(s16x4, f1), pq);
    float mx = fmaxf(pq[0], pq[1]), mn = fminf(pq[0], pq[1]);
#pragma unroll
    for (int u = 2; u < 10; u += 2) { mx = fmaxf(fmaxf(mx, pq[u]), pq[u + 1]); mn = fminf(fminf(mn, pq[u]), pq[u + 1]); }
    mx = xhalf_max(mx); mn = xhalf_min(mn);
    const f32x2 lp = {LOG2E, LOG2E}, cmx = {-mx * LOG2E, -mx * LOG2E}, cmn = {mn * LOG2E, mn * LOG2E};
    f32x2 ep[5], en[5], sp2 = {0.f, 0.f}, sn2 = {0.f, 0.f};
#pragma unroll
    for (int u = 0; u < 5; ++u) {
        const f32x2 p = {pq[2 * u], pq[2 * u + 1]};
        const f32x2 a = __builtin_elementwise_fma(p, lp, cmx), b = __builtin_elementwise_fma(p, -lp, cmn);
        ep[u] = f32x2{EXP2(a[0]), EXP2(a[1])}; en[u] = f32x2{EXP2(b[0]), EXP2(b[1])};
        sp2 += ep[u]; sn2 += en[u];
    }
    const float sp = __builtin_amdgcn_rcpf(xhalf_sum(sp2[0] + sp2[1])), sn = __builtin_amdgcn_rcpf(xhalf_sum(sn2[0] + sn2[1]));
#pragma unroll
    for (int u = 0; u < 5; ++u) {
        const f32x2 a = ep[u] * sp, b = en[u] * sn;
        ph[u] = cvtpk(a[0], a[1]); ph[5 + u] = cvtpk(b[0], b[1]);
    }
}
__device__ __forceinline__ u32x2 split3(float v) {
    const unsigned hi = cvtpk(v, 0.f) & 0xffffu; const float r1 = v - bflo(hi);
    const unsigned mid = cvtpk(r1, 0.f) & 0xffffu; const float r2 = r1 - bflo(mid);
    return u32x2{hi | (mid << 16), cvtpk(r2, 0.f) & 0xffffu};
}
__device__ __forceinline__ float qnorm2(const u32x4& a, const u32x2& b) {
    float n2 = 0.f;
#pragma unroll
    for (int j = 0; j < 4; ++j) { n2 = fmaf(bflo(a[j]), bflo(a[j]), n2); n2 = fmaf(bfhi(a[j]), bfhi(a[j]), n2); }
#pragma unroll
    for (int j = 0; j < 2; ++j) { n2 = fmaf(bflo(b[j]), bflo(b[j]), n2); n2 = fmaf(bfhi(b[j]), bfhi(b[j]), n2); }
    return xhalf_sum(n2);
}

#define LD128(off) __builtin_amdgcn_raw_buffer_load_b128(wsr, off, 0, 16)
#define LD64(off) __builtin_amdgcn_raw_buffer_load_b64(wsr, off, 0, 16)
#define LD32(off) __builtin_amdgcn_raw_buffer_load_b32(wsr, off, 0, 16)
#define BAR() asm volatile("s_barrier" ::: "memory")

__device__ __forceinline__ const char* w_source(int p, int lane, const char* g0, const char* g1, const char* g2) {
    int q = (p & 7) * 64 + lane; const int up = q >= 248; q -= up ? 248 : 0; q = q < 239 ? q : 239;
    const int row = __umul24(q, 2185) >> 16, ch = q - row * 30, sgm = __umul24(ch, 6554) >> 16;
    const uintptr_t b = sgm == 0 ? (uintptr_t)g0 : sgm == 1 ? (uintptr_t)g1 : (uintptr_t)g2;
    return (const char*)(b + (__umul24(16 * (p >> 3) + 8 * up + row, 3840) + (ch - 10 * sgm) * 16));
}

constexpr int P1_STAGE = 49152;
constexpr int L_WPH = 3 * P1_STAGE;
constexpr int L_GW = L_WPH + 3200;
constexpr int L_WO = L_GW + 1280;
constexpr int L_SCR = L_WO + 10240;
constexpr int L_EP = L_SCR + 256;
constexpr int L_FLAG = L_EP + 256;
constexpr int LDS_BYTES = L_FLAG + 16;
constexpr int L_IMG = P1_STAGE;
constexpr int L_PH = L_IMG + 32 * 1536;
constexpr int L_Q = L_PH + 40960 + 256;
constexpr int SLOT = 8 * KVT;
constexpr int L_X = 0;
constexpr int L_MISC = L_WO;
constexpr int L_GATE = L_MISC + 3072;
constexpr int L_RA = 4 * SLOT, L_RB = SLOT, L_RC = 2 * SLOT, L_RD = 0, L_RE = L_RC + 6 * 5120;
constexpr int L_CX = L_RC;
static_assert(L_Q + 10240 + 256 <= L_WO && L_RA + 10 * 5120 <= L_WO && L_RE + 3 * 5120 <= L_RA && LDS_BYTES <= 163840, "");
__device__ __forceinline__ constexpr int p3_off(int br, int ks) {
    return br ? (ks < 10 ? L_RA + ks * 5120 : ks < 14 ? L_RB + (ks - 10) * 5120 : L_RC + (ks - 14) * 5120)
              : (ks < 4 ? L_RD + ks * 5120 : ks < 7 ? L_RE + (ks - 4) * 5120 : ks < 17 ? L_RA + (ks - 7) * 5120 : L_RB + (ks - 17) * 5120); }

__global__ __launch_bounds__(768, 3) void fused_kernel(const uint16_t* __restrict__ x, const uint16_t* __restrict__ wA, const uint16_t* __restrict__ wB,
                                                    const uint16_t* __restrict__ woA, const uint16_t* __restrict__ woB,
                                                    const uint16_t* __restrict__ wphi, const uint16_t* __restrict__ gatew,
                                                    char* __restrict__ ws, uint16_t* __restrict__ out) {
    __shared__ __attribute__((aligned(16))) char lds[LDS_BYTES];
    uint64_t pc0; asm volatile("s_getpc_b64 %0" : "=s"(pc0));
    const int bid = blockIdx.x, xcd = bid & 7, bi = bid >> 3;
    const int tid = threadIdx.x, lane = tid & 63;
    const int wid = __builtin_amdgcn_readfirstlane(tid >> 6);
    const unsigned lds0 = (unsigned)(uintptr_t)lds;
    const lds_cptr L = (lds_cptr)lds;
    const unsigned l16 = lane * 16;
    const int g = lane >> 4, l15 = lane & 15, q4 = l15 >> 2, p4 = lane & 3, r32 = lane & 31, hh = lane >> 5;
    const auto wsr = __builtin_amdgcn_make_buffer_rsrc(ws, 0, WS_END, 0x00020000);
    const int rt = (xcd & 1) * 8 + (bi & 7), kd = bi >> 4, hj = (xcd >> 1) * 2 + ((bi >> 3) & 1);
    const int h = xcd * 4 + (bi >> 3), qt = bi & 7;
    const int tt = xcd * 4 + (bi & 3), ct = bi >> 2;
    const char* xb = (const char*)x + (size_t)rt * 128 * 1280;
    const unsigned ka = lane * 16, kb = 1024 + r32 * 8;
    const char* wg0 = (const char*)wA + hj * 160 + (kd ? 2560 : 0);
    const char* wg1 = kd ? (const char*)wB + hj * 160 + 1280 : (const char*)wA + hj * 160 + 1280;
    const char* wg2 = (const char*)wB + hj * 160 + (kd ? 2560 : 0);
#define wsrc(p) w_source(p, lane, wg0, wg1, wg2)
#define P3_WKS(br, ks, dst) do { _Pragma("unroll") for (int j_ = 0; j_ < 5; ++j_) { const int q_ = j_ * 64 + lane, c_ = __umul24(q_, 52429) >> 22, n_ = q_ - c_ * 80; \
        glds_run<1, 0, 0, true>(ws + WS_WOP + ct * 1280 + ((br) * 80 + 4 * (ks)) * 10240, c_ * 10240 + n_ * 16, lds0 + (dst) + j_ * 1024); } } while (0)
    u64 tag = 0;
#define MAKE_TAG() do { u64 z = ((u64)(unsigned)__builtin_amdgcn_readfirstlane((unsigned)(tag >> 32)) << 32) | (unsigned)__builtin_amdgcn_readfirstlane((unsigned)tag); \
        z = z * 0x9E3779B97F4A7C15ull + 0x632BE59BD9B4E019ull; z ^= z >> 29; z *= 0xBF58476D1CE4E5B9ull; z ^= z >> 32; tag = z; } while (0)

    if (wid >= 8) {
        const int hw = wid - 8;
        __builtin_amdgcn_s_setprio(2);
        unsigned hbt = 0;
        if (tid == 512) *(AS3(unsigned)*)((lds_ptr)lds + L_FLAG + 4) = 0u;
#define HBAR() do { hbt += 4; asm volatile("s_waitcnt lgkmcnt(0)" ::: "memory"); \
        if (lane == 0) __hip_atomic_fetch_add((AS3(unsigned)*)((lds_ptr)lds + L_FLAG + 4), 1u, __ATOMIC_RELAXED, __HIP_MEMORY_SCOPE_WORKGROUP); \
        while (__builtin_amdgcn_readfirstlane(__hip_atomic_load((AS3(unsigned)*)((lds_ptr)lds + L_FLAG + 4), __ATOMIC_RELAXED, __HIP_MEMORY_SCOPE_WORKGROUP)) < hbt) __builtin_amdgcn_s_sleep(1); \
        asm volatile("" ::: "memory"); } while (0)
        const char* pw[4];
#pragma unroll
        for (int j = 0; j < 4; ++j) pw[j] = wsrc(16 + 4 * hw + j);
        auto wstage = [&](int slot) {
            const unsigned lw = lds0 + slot * P1_STAGE + 32768 + hw * 4096;
            glds_ptr<0>(pw[0], lw); glds_ptr<1024>(pw[1], lw); glds_ptr<2048>(pw[2], lw); glds_ptr<3072>(pw[3], lw);
#pragma unroll
            for (int j = 0; j < 4; ++j) pw[j] += 245760;
        };
        if (hj == 0 && kd == 0 && hw < 2 && hw * 1024 + l16 < 1280) glds_run<1, 0, L_GW, false>(gatew, hw * 1024 + l16, lds0 + hw * 1024);
        wstage(0);
        RAW_BAR();
        wstage(1);
        WAIT_VM(4);
        BAR();
        wstage(2);
        const char* tp = hw == 0 ? xb + ((bi >> 3) * 32 + r32) * 1280 + 3 * 128
                       : hw == 1 ? ((lane % 6) < 2 ? wg0 : (lane % 6) < 4 ? wg1 : wg2) + ((bi & 7) * 8 + (lane < 48 ? lane / 6 : 0) + 192) * 3840 + (lane & 1) * 156 : xb;
        const unsigned tstep = hw == 0 ? 128u : hw == 1 ? 245760u : 0u;
        touch(tp, lds0 + L_SCR); tp += tstep; touch(tp, lds0 + L_SCR); tp += tstep;
        for (int s = 0; s < 9; ++s) {
            if (s == 0) { WAIT_VM(6); } else if (s == 1) { WAIT_VM(7); } else if (s < 4) { WAIT_VM(6); } else if (s == 4) { WAIT_VM(11); } else if (s == 5) { WAIT_VM(12); } else if (s == 6) { WAIT_VM(7); } else if (s == 7) { WAIT_VM(5); } else { WAIT_VM(0); }
            BAR();
            if (s < 7) wstage(s % 3);
            if (s < 5) { touch(tp, lds0 + L_SCR); tp += tstep; }
            if (s == 5) { const unsigned o = (hw * 64 + lane) * 128; touch((const char*)pc0 + (o < CODE_BYTES ? o : 0u), lds0 + L_SCR); }
            if (s == 3) {
                const int cb = bid < 160 ? bid : 0, ch = cb < 80 ? cb : cb - 80;
                const int r0 = (ch < 64 ? (ch >> 1) * 20 + (ch & 1) * 4 : (ch - 64) * 40 + 16) + hw, dr = (ch < 64 ? 8 : 20) * 1280;
                const char* pr = (const char*)(cb < 80 ? woA : woB) + r0 * 1280; const unsigned lr = lds0 + hw * 1280;
                glds_run<1, 0, L_WO, false>(pr, l16, lr); glds_run<1, 0, L_WO + 5120, false>(pr + dr, l16, lr);
                if (lane < 16) { glds_run<1, 1024, L_WO + 1024, false>(pr, l16, lr); glds_run<1, 1024, L_WO + 6144, false>(pr + dr, l16, lr); }
                if (hw * 1024 + l16 < 3200) glds_run<1, 0, L_WPH, false>((const char*)wphi + hj * 3200, hw * 1024 + l16, lds0 + hw * 1024);
            }
            if (s == 4) touch(ws + WS_EPOCH + (lane & 1) * 4, lds0 + L_EP);
            if (s == 8 && bid < 160) {
                const lds_cptr r = L + L_WO + q4 * 1280 + g * 32 + p4 * 8 + hw * 128;
#pragma unroll
                for (int j = 0; j < 3; ++j)
                    if (j < 2 || hw < 2) ST128(__builtin_bit_cast(u32x4, cat8(tr_read(r + j * 512), tr_read(r + j * 512 + 5120))), WS_WOP + (bid * 640 + (j * 4 + hw) * 64 + lane) * 16);
            }
        }
        BAR();
        tag = *(const AS3(u64)*)(L + L_EP);
        MAKE_TAG();
        BAR();
        const unsigned fhoff = WS_FH + (((4 * hj + (lane & 3)) * 4 + kd) * 16 + rt) * 8;
        if (hw == 0 && lane < 4) publish(ws, fhoff, tag);
        if (kd) BAR();
        const char* kvbase = ws + WS_KV + (size_t)h * 64 * KVT;
        const char* kvw = kvbase + (size_t)(hw >> 1) * 4 * SLOT + (hw & 1) * 12288; const unsigned ldw = lds0 + (hw >> 1) * SLOT + (hw & 1) * 12288;
#define H_CHUNK(j) do { glds_run<4, (j) * SLOT, ((j) & 1) * 2 * SLOT, true>(kvw, l16, ldw); glds_run<4, (j) * SLOT + 4096, ((j) & 1) * 2 * SLOT + 4096, true>(kvw, l16, ldw); \
                        glds_run<4, (j) * SLOT + 8192, ((j) & 1) * 2 * SLOT + 8192, true>(kvw, l16, ldw); } while (0)
        if (hw == 0) wait_flags(ws, WS_FH + (h * 64 + r32) * 8, tag);
        BAR();
        {
            const char* pq = ws + WS_QA + ((size_t)h * 64 + qt * 8) * QFT;
            if (hw < 2) glds_run<3, 0, L_Q, true>(pq + hw * 3072, l16, lds0 + hw * 3072); else glds_run<2, 0, L_Q, true>(pq + 2048 + hw * 2048, l16, lds0 + 2048 + hw * 2048);
            if (hw == 3 && lane < 16) glds_run<1, 0, L_Q + 10240, true>(ws + WS_KMAX + h * 256, l16, lds0);
        }
        WAIT_VM(0);
        BAR();
        if (tid == 512) publish(ws, WS_FT + bid * 8, tag);
        {
            if (kd) {
#pragma unroll
                for (int j = 0; j < 4; ++j) ST128(*(const AS3(u32x4)*)(L + L_IMG + hw * 4096 + j * 1024 + l16), (j < 3 || lane < 48) ? WS_KVP + ((4 * hj + hw) * 16 + rt) * 3840 + j * 1024 + l16 : OOB);
                HBAR();
            } else {
#pragma unroll
                for (int j = 0; j < 10; ++j) ST128(*(const AS3(u32x4)*)(L + L_PH + hw * 10240 + j * 1024 + l16), WS_PHQ + ((4 * hj + hw) * 64 + rt * 4) * 2560 + j * 1024 + l16);
            }
            H_CHUNK(1);
            WAIT_VM(0);
            if (lane == hw) publish(ws, fhoff + 256, tag);
            BAR();
            {
                wait_flags(ws, WS_FH + (lane < 32 ? h * 64 + 32 + lane : 2 * qt + (lane & 1)) * 8, tag);
                H_CHUNK(2);
                const int e = 64 * hw + lane;
                f32x4 ks = {0.f, 0.f, 0.f, 0.f};
#pragma unroll
                for (int j = 0; j < 16; ++j) ks += __builtin_bit_cast(f32x4, LD128(WS_KVP + (h * 16 + j) * 3840 + (e < 240 ? e : 0) * 16));
                u32x4 pf[2][2]; u32x2 pg[2]; float gt[2];
#pragma unroll
                for (int t = 0; t < 2; ++t) {
                    const unsigned o = WS_PHQ + (h * 64 + qt * 8 + 2 * hw + t) * 2560;
                    pf[t][0] = LD128(o + l16); pf[t][1] = LD128(o + 1024 + l16); pg[t] = LD64(o + 2048 + lane * 8);
                    gt[t] = __uint_as_float(LD32(WS_G + (qt * 256 + (2 * hw + t) * 32 + r32) * 4));
                }
                if (e < 240) {
                    const int ph = e / 6, d = 4 * (e - ph * 6), ph_h = ph / 20, u = ph - ph_h * 20;
                    const unsigned w0 = cvtpk(ks[0], ks[1]), w1 = cvtpk(ks[2], ks[3]);
                    lds_ptr dd = (lds_ptr)lds + L_MISC + d * 96 + ph_h * 48 + u * 2;
                    *(AS3(uint16_t)*)(dd) = (uint16_t)(w0 & 0xffffu); *(AS3(uint16_t)*)(dd + 96) = (uint16_t)(w0 >> 16);
                    *(AS3(uint16_t)*)(dd + 192) = (uint16_t)(w1 & 0xffffu); *(AS3(uint16_t)*)(dd + 288) = (uint16_t)(w1 >> 16);
                }
                *(AS3(float)*)((lds_ptr)lds + L_GATE + (64 * hw + lane) * 4) = hh ? gt[1] : gt[0];
                wait_flags(ws, WS_FT + (64 * hw + lane) * 8, tag);
                WAIT_VM(0);
                RAW_BAR();
                H_CHUNK(3);
                const lds_cptr a = L + L_MISC + r32 * 96 + hh * 48;
                const bf16x8 a0 = LDS_B128(a), a1 = LDS_B128(a + 16); const s16x4 a2 = LDS_B64(a + 32);
#pragma unroll
                for (int t = 0; t < 2; ++t) {
                    f32x16 lo = MFMA32(a0, __builtin_bit_cast(bf16x8, pf[t][0]), f32x16{});
                    lo = MFMA32(a1, __builtin_bit_cast(bf16x8, pf[t][1]), lo);
                    lo = MFMA32K8(a2, __builtin_bit_cast(s16x4, pg[t]), lo);
                    auto rb = __builtin_amdgcn_permlane32_swap(__float_as_uint(lo[8]), __float_as_uint(lo[8]), false, false);
                    const float sb = (1.0f - gt[t]) / (__uint_as_float(rb[1]) + 1e-6f);
                    const unsigned oc = WS_OC + ((qt * 8 + 2 * hw + t) * 32 + r32) * 16;
                    ST128((u32x4{cvtpk(lo[0] * sb, lo[1] * sb), cvtpk(lo[2] * sb, lo[3] * sb), cvtpk(lo[4] * sb, lo[5] * sb), cvtpk(lo[6] * sb, lo[7] * sb)}), oc + (80 + 2 * h + hh) * (SEQ * 16));
                    if (!hh) ST64((u32x2{cvtpk(lo[8] * sb, lo[9] * sb), cvtpk(lo[10] * sb, lo[11] * sb)}), oc + (80 + 64 + (h >> 1)) * (SEQ * 16) + (h & 1) * 8);
                }
            }
            WAIT_VM(0);
            HBAR();
            if (tid == 512) { publish(ws, WS_FOB + (qt * 32 + h) * 8, tag); *(AS3(unsigned)*)((lds_ptr)lds + L_FLAG) = 0u; }
            for (int ks = hw; ks < 10; ks += 4) P3_WKS(1, ks, L_RA + ks * 5120);
            RAW_BAR();
            P3_WKS(1, 10 + hw, L_RB + hw * 5120);
            { const int ln = hw * 64 + lane; touch(ws + WS_WOP + bi * 25600 + (ln < 200 ? ln : 0) * 128, lds0 + L_SCR); }
            if (hw == 0) wait_flags(ws, WS_FOB + (xcd * 32 + r32) * 8, tag);
            WAIT_VM(0);
            RAW_BAR();
            BAR();
            const unsigned redo = *(const AS3(unsigned)*)(L + L_FLAG);
            if (__builtin_amdgcn_readfirstlane(redo) != 0u) {
                RAW_BAR();
                H_CHUNK(0); WAIT_VM(0); BAR();
                H_CHUNK(1); WAIT_VM(0); BAR();
                H_CHUNK(2); WAIT_VM(0); BAR();
                H_CHUNK(3);
                if (tid == 512) *(AS3(unsigned)*)((lds_ptr)lds + L_FLAG) = 0u;
                WAIT_VM(0); RAW_BAR();
                P3_WKS(1, 10 + hw, L_RB + hw * 5120); WAIT_VM(0);
                BAR(); BAR();
            }
        }
        if (tid == 512) publish(ws, WS_FOA + (qt * 32 + h) * 8, tag);
        for (int ks = 14 + hw; ks < 20; ks += 4) P3_WKS(1, ks, L_RC + (ks - 14) * 5120);
        WAIT_VM(0);
        BAR();
        for (int ks = hw; ks < 7; ks += 4) P3_WKS(0, ks, ks < 4 ? L_RD + ks * 5120 : L_RE + (ks - 4) * 5120);
        BAR();
        if (hw == 3) wait_flags(ws, WS_FOA + (xcd * 32 + r32) * 8, tag);
        WAIT_VM(0);
        BAR();
        BAR();
        BAR();
    } else {
        const int th = wid >> 2, cw = wid & 3;
        {
            unsigned vx[2];
#pragma unroll
            for (int j = 0; j < 2; ++j) { const int row = 8 * (wid * 2 + j) + (lane >> 3); vx[j] = row * 1280 + (((lane ^ row) & 7) << 4); }
            const unsigned lx = lds0 + wid * 2048;
            const char* pw0 = wsrc(2 * wid); const char* pw1 = wsrc(2 * wid + 1);
#define P1_X(s, slot) do { glds_run<1, 0, 0, false>(xb + (s) * 128, vx[0], lx + (slot) * P1_STAGE); glds_run<1, 0, 1024, false>(xb + (s) * 128, vx[1], lx + (slot) * P1_STAGE); \
                           glds_ptr<16384>(pw0, lx + (slot) * P1_STAGE); glds_ptr<17408>(pw1, lx + (slot) * P1_STAGE); pw0 += 245760; pw1 += 245760; } while (0)
            P1_X(0, 0);
            BAR();
            P1_X(1, 1);
            const bool t3 = cw < 3 || (hj == 0 && kd == 0);
            const int xrow = 64 * th + l15;
            lds_cptr xa0 = L + xrow * 128 + ((g ^ (xrow & 7)) << 4), xa1 = L + ((xrow * 128 + ((g ^ (xrow & 7)) << 4)) ^ 64);
            const unsigned wa0 = 16384 + 8192 * (g >> 1) + 480 * (8 * (g & 1) + q4) + 128 * (g & 1) + 40 * p4 + (cw < 3 ? 160 * cw : 32);
            const unsigned wst = cw < 3 ? 8 : 160;
            lds_cptr wa[4];
#pragma unroll
            for (int i = 0; i < 3; ++i) wa[i] = L + wa0 + i * wst;
            wa[3] = cw < 3 ? L + wa0 + 24 : L + L_GW + g * 16;
            f32x4 acc[4][4];
#pragma unroll
            for (int m = 0; m < 4; ++m)
#pragma unroll
                for (int i = 0; i < 4; ++i) acc[m][i] = f32x4{0.f, 0.f, 0.f, 0.f};
            bf16x8 xA[4], wA_[4], xB[4], wB_[4];
#define P1_LOAD(X, W, ks, NT, G3) do { \
        _Pragma("unroll") for (int i = 0; i < 3; ++i) W[i] = cat8(tr_read(wa[i] + (ks) * 16384), tr_read(wa[i] + (ks) * 16384 + 1920)); \
        if (NT == 4) { if (G3) W[3] = LDS_B128(wa[3] + (ks) * 64); else W[3] = cat8(tr_read(wa[3] + (ks) * 16384), tr_read(wa[3] + (ks) * 16384 + 1920)); } \
        _Pragma("unroll") for (int m = 0; m < 4; ++m) X[m] = LDS_B128(((ks) ? xa1 : xa0) + m * 2048); } while (0)
#define P1_HALF(X, W, XN, WN, ks, NT, G3, MID1, MID2) do { \
        _Pragma("unroll") for (int i = 0; i < NT; ++i) { \
            s16x4 lo_, hi_; \
            acc[0][i] = MFMA16(W[i], X[0], acc[0][i]); SBAR(); \
            if (i == 3 && G3) WN[3] = LDS_B128(wa[3] + (ks) * 64); else lo_ = tr_read(wa[i] + (ks) * 16384); SBAR(); \
            acc[1][i] = MFMA16(W[i], X[1], acc[1][i]); SBAR(); \
            if (!(i == 3 && G3)) { hi_ = tr_read(wa[i] + (ks) * 16384 + 1920); WN[i] = cat8(lo_, hi_); } SBAR(); \
            acc[2][i] = MFMA16(W[i], X[2], acc[2][i]); SBAR(); \
            XN[i] = LDS_B128(((ks) ? xa1 : xa0) + i * 2048); SBAR(); \
            acc[3][i] = MFMA16(W[i], X[3], acc[3][i]); SBAR(); \
            if (NT == 3 && i == 2) XN[3] = LDS_B128(((ks) ? xa1 : xa0) + 3 * 2048); \
            if (i == 0) { MID1; } if (i == 1) { MID2; } SBAR(); \
        } } while (0)
#define P1_DX(lim, st, sl) do { unsigned sv_; const char* xs_ = xb + (st) * 128; \
                  asm volatile("s_cmp_gt_u32 %1, " #lim "\n\ts_cbranch_scc1 1f\n\ts_mov_b32 %0, m0\n\ts_mov_b32 m0, %5\n\ts_nop 0\n\tglobal_load_lds_dwordx4 %2, %4\n\t" \
                               "s_add_u32 m0, %5, 1024\n\ts_nop 0\n\tglobal_load_lds_dwordx4 %3, %4\n\ts_mov_b32 m0, %0\n1:" \
                               : "=&s"(sv_) : "s"(s), "v"(vx[0]), "v"(vx[1]), "s"(xs_), "s"(lx + (sl) * P1_STAGE) : "memory", "scc"); } while (0)
#define P1_DW(lim, sl) do { unsigned sv_; \
                  asm volatile("s_cmp_gt_u32 %3, " #lim "\n\ts_cbranch_scc1 1f\n\ts_mov_b32 %0, m0\n\ts_add_u32 m0, %4, 16384\n\ts_nop 0\n\tglobal_load_lds_dwordx4 %1, off\n\t" \
                               "s_add_u32 m0, %4, 17408\n\tv_lshl_add_u64 %1, %1, 0, %5\n\tglobal_load_lds_dwordx4 %2, off\n\ts_mov_b32 m0, %0\n\tv_lshl_add_u64 %2, %2, 0, %5\n1:" \
                               : "=&s"(sv_), "+v"(pw0), "+v"(pw1) : "s"(s), "s"(lx + (sl) * P1_STAGE), "s"(245760ull) : "memory", "scc"); } while (0)
#define P1_LOOP(NT, G3) do { \
            P1_LOAD(xA, wA_, 0, NT, G3); \
            int slot = 0; \
            _Pragma("nounroll") for (int s = 0; s < 10; ++s) { \
                P1_HALF(xA, wA_, xB, wB_, 1, NT, G3, P1_DX(0, 2, 2), P1_DW(0, 2)); \
                  \
                asm volatile("s_cmp_gt_u32 %0, 7\n\ts_cbranch_scc1 1f\n\ts_waitcnt vmcnt(4) lgkmcnt(0)\n\ts_barrier\n\ts_branch 2f\n" \
                             "1:\n\ts_cmp_eq_u32 %0, 9\n\ts_cbranch_scc1 2f\n\ts_waitcnt vmcnt(0) lgkmcnt(0)\n\ts_barrier\n2:" :: "s"(s) : "memory", "scc"); \
                const int d = slot == 2 ? -2 * P1_STAGE : P1_STAGE; \
                xa0 += d; xa1 += d; wa[0] += d; wa[1] += d; wa[2] += d; wa[3] += G3 ? 128 : d; SBAR(); \
                P1_HALF(xB, wB_, xA, wA_, 0, NT, G3, P1_DX(6, s + 3, slot), P1_DW(6, slot)); \
                slot = slot == 2 ? 0 : slot + 1; \
            } } while (0)
            WAIT_VM(4);
            BAR();
            if (cw < 3) P1_LOOP(4, false); else if (t3) P1_LOOP(4, true); else P1_LOOP(3, false);
            const lds_ptr img = (lds_ptr)lds + L_IMG;
            {
                const int ht = (4 * hj + g) * 64 + rt * 4 + 2 * th;
                float kn = 0.f;
                if (cw < 3) {
                    const bool isv = kd ? cw != 1 : false, toA = kd ? cw == 0 : cw < 2;
                    if (kd == 0 && cw == 0) {
#pragma unroll
                        for (int m = 0; m < 4; ++m)
#pragma unroll
                            for (int i = 0; i < 4; ++i) acc[m][i] *= QSCALE;
                    }
                    const unsigned ts = (kd == 0 && cw == 0) ? QFT : KVT, tb = ((kd == 0 && cw == 0) ? WS_QA : WS_KV + (kd ? 1536 : 0)) + ht * ts;
                    const unsigned o0 = l15 * 16, o1 = 512 + (isv ? l15 ^ 4 : l15) * 16;
#pragma unroll
                    for (int m = 0; m < 4; ++m) {
                        const u32x4 w0 = {cvtpk(acc[m][0][0], acc[m][0][1]), cvtpk(acc[m][0][2], acc[m][0][3]), cvtpk(acc[m][1][0], acc[m][1][1]), cvtpk(acc[m][1][2], acc[m][1][3])};
                        const u32x4 w1 = {cvtpk(acc[m][2][0], acc[m][2][1]), cvtpk(acc[m][2][2], acc[m][2][3]), cvtpk(acc[m][3][0], acc[m][3][1]), cvtpk(acc[m][3][2], acc[m][3][3])};
                        if (toA) { const unsigned mo = tb + (m >> 1) * ts + (m & 1) * 256; ST128(w0, mo + o0); ST128(w1, mo + o1); }
                        else { const lds_ptr d = img + (((isv ? 4 : 0) + 2 * th + (m >> 1)) * 4 + g) * 1536 + (m & 1) * 256; *(AS3(u32x4)*)(d + o0) = w0; *(AS3(u32x4)*)(d + o1) = w1; }
                    }
                    if (kd == 0 && cw == 1) {
#pragma unroll
                        for (int m = 0; m < 4; ++m) {
                            float n2 = 0.f;
#pragma unroll
                            for (int i = 0; i < 4; ++i)
#pragma unroll
                                for (int r = 0; r < 4; ++r) n2 = fmaf(acc[m][i][r], acc[m][i][r], n2);
                            kn = (n2 == n2) ? fmaxf(kn, n2) : __builtin_inff();
                        }
                    }
                } else {
#pragma unroll
                    for (int m = 0; m < 4; ++m) {
                        const unsigned ok = WS_KV + (ht + (m >> 1)) * KVT + 1024, o8 = (m & 1) * 128 + l15 * 8, o16 = (m & 1) * 256 + (l15 ^ 8) * 16;
                        const lds_ptr d = img + ((2 * th + (m >> 1)) * 4 + g) * 1536 + 1024;
                        const u32x2 w1 = {cvtpk(acc[m][1][0], acc[m][1][1]), cvtpk(acc[m][1][2], acc[m][1][3])}, w2 = {cvtpk(acc[m][2][0], acc[m][2][1]), cvtpk(acc[m][2][2], acc[m][2][3])};
                        if (kd == 0) {
                            ST64((u32x2{cvtpk(acc[m][0][0] * QSCALE, acc[m][0][1] * QSCALE), cvtpk(acc[m][0][2] * QSCALE, acc[m][0][3] * QSCALE)}), WS_QA + (ht + (m >> 1)) * QFT + 1024 + o8);
                            ST128((u32x4{w1[0], w1[1], 0x3f803f80u, 0x00003f80u}), ok + (m & 1) * 256 + l15 * 16);
                            *(AS3(u32x2)*)(d + o8) = w2;
                            float n2 = 0.f;
#pragma unroll
                            for (int r = 0; r < 4; ++r) n2 = fmaf(acc[m][1][r], acc[m][1][r], n2);
                            kn = (n2 == n2) ? fmaxf(kn, n2) : __builtin_inff();
                        } else {
                            ST128((u32x4{cvtpk(acc[m][0][0], acc[m][0][1]), cvtpk(acc[m][0][2], acc[m][0][3]), 0x00003f80u, 0u}), ok + 1536 + o16);
                            *(AS3(u32x2)*)(d + o8) = w1;
                            *(AS3(u32x4)*)(d + 16 * 1536 + o16) = u32x4{w2[0], w2[1], 0x00003f80u, 0u};
                        }
                    }
                }
                if (cw == 3 && t3 && g == 0) {
#pragma unroll
                    for (int m = 0; m < 4; ++m) ST32(__float_as_uint(1.0f / (1.0f + __expf(-acc[m][3][0]))), WS_G + (rt * 128 + 64 * th + 16 * m + l15) * 4);
                }
                if (kd == 0 && (cw & 1)) {
                    kn = row_max16(kn);
                    if (l15 == 0) ST32(__float_as_uint(kn), WS_KMAX + ((4 * hj + g) * 64 + (cw == 3 ? 32 : 0) + rt * 2 + th) * 4);
                }
            }
            u32x4 wp0; u32x2 wp1;
            {
                const int fr = (r32 & 3) + 4 * (r32 >> 3);
                const lds_cptr wp = L + L_WPH + cw * 800 + (fr < 10 ? 10 * ((r32 >> 2) & 1) + fr : 0) * 2;
                unsigned wv[12];
#pragma unroll
                for (int j = 0; j < 8; ++j) wv[j] = *(const AS3(uint16_t)*)(wp + hh * 320 + j * 40);
#pragma unroll
                for (int j = 0; j < 4; ++j) wv[8 + j] = *(const AS3(uint16_t)*)(wp + 640 + j * 40);
                const unsigned m0 = fr < 10 ? 0xffffffffu : 0u, m1 = (fr < 10 && hh == 0) ? 0xffffffffu : 0u;
                wp0 = u32x4{(wv[0] | (wv[1] << 16)) & m0, (wv[2] | (wv[3] << 16)) & m0, (wv[4] | (wv[5] << 16)) & m0, (wv[6] | (wv[7] << 16)) & m0};
                wp1 = u32x2{(wv[8] | (wv[9] << 16)) & m1, (wv[10] | (wv[11] << 16)) & m1};
            }
            RAW_BAR();
#pragma unroll
            for (int t2 = 0; t2 < 2; ++t2) {
                const lds_cptr f = L + L_IMG + ((2 * th + t2) * 4 + cw) * 1536;
                unsigned ph[10];
                feature_map(wp0, wp1, *(const AS3(u32x4)*)(f + ka), *(const AS3(u32x2)*)(f + kb), ph);
                const lds_ptr d = (lds_ptr)lds + L_PH + (cw * 4 + 2 * th + t2) * 2560;
                if (kd) {
#pragma unroll
                    for (int u = 0; u < 5; ++u) *(AS3(u32x2)*)(d + r32 * 80 + hh * 40 + u * 8) = u32x2{ph[2 * u], ph[2 * u + 1]};
                } else {
                    *(AS3(u32x4)*)(d + l16) = u32x4{ph[0], ph[1], ph[2], ph[3]}; *(AS3(u32x4)*)(d + 1024 + l16) = u32x4{ph[4], ph[5], ph[6], ph[7]};
                    *(AS3(u32x2)*)(d + 2048 + lane * 8) = u32x2{ph[8], ph[9]};
                }
                if (t2 == 0) {
                    SBAR();
                    WAIT_VM(0);
                    BAR();
                    SBAR();
                }
            }
            if (kd) {
                RAW_BAR();
#pragma unroll
                for (int k2 = 0; k2 < 2; ++k2) {
                    if (k2 == 0 || wid < 4) {
                        const int u = wid + 8 * k2, uh = u / 3, un = u - 3 * uh;
                        f32x4 c0 = {0.f, 0.f, 0.f, 0.f}, c1 = {0.f, 0.f, 0.f, 0.f};
                        const unsigned vo = L_IMG + (16 + uh) * 1536;
                        const unsigned vb = 512 * (p4 >> 1) + 256 * (g >> 1) + 64 * ((2 * (g & 1)) ^ (p4 >> 1)) + 16 * q4 + 8 * (p4 & 1);
                        const unsigned vc = 1024 + 256 * (g >> 1) + 64 * ((2 * (g & 1)) ^ 2) + 16 * q4 + (p4 ? 8 : 0);
                        const unsigned pb = L_PH + (uh * 128 + 8 * g + q4) * 80 + un * 32 + p4 * 8;
#pragma unroll
                        for (int ks = 0; ks < 4; ++ks) {
                            const bf16x8 a0 = cat8(tr_read(L + vo + vb + ks * 6144), tr_read(L + vo + (vb ^ 64) + ks * 6144));
                            const bf16x8 a1 = cat8(tr_read(L + vo + vc + ks * 6144), tr_read(L + vo + (vc ^ 64) + ks * 6144));
                            const bf16x8 b = cat8(tr_read(L + pb + ks * 2560), tr_read(L + pb + ks * 2560 + 320));
                            c0 = MFMA16(a0, b, c0); c1 = MFMA16(a1, b, c1);
                        }
                        const int ph = 16 * un + l15;
                        if (ph < 40) {
                            const lds_ptr d = (lds_ptr)lds + L_IMG + uh * 4096 + ph * 96 + 16 * g;
                            *(AS3(f32x4)*)(d) = c0; if (g < 2) *(AS3(f32x4)*)(d + 64) = c1;
                        }
                    }
                }
            }
        }
        const int qp = wid & 3, kh = wid >> 2;
        const int own = 2 * qp + kh;
        const int blk = (lane >> 4) & 1, vj = blk ? 2 : p4 >> 1;
        const unsigned kb2 = 1024 + r32 * 16 + hh * 8;
        const unsigned va = 1536 + 512 * vj + 64 * ((2 * hh) ^ vj) + 16 * q4 + (blk ? (p4 ? 8 : 0) : 8 * (p4 & 1)), vb2 = va ^ 64;
        RAW_BAR();
        {
            const char* src = ws + WS_KV + ((size_t)h * 64 + kh * 32) * KVT + qp * 6144; const unsigned dst = lds0 + kh * SLOT + qp * 6144;
            glds_run<4, 0, 0, true>(src, l16, dst); glds_run<2, 4096, 4096, true>(src, l16, dst);
        }
        WAIT_VM(0);
        BAR();
        const lds_cptr lq = L + L_Q + 2 * qp * QFT;
        const u32x4 qa0 = *(const AS3(u32x4)*)(lq + ka), qb0 = *(const AS3(u32x4)*)(lq + QFT + ka);
        u32x2 qa1 = *(const AS3(u32x2)*)(lq + kb), qb1 = *(const AS3(u32x2)*)(lq + QFT + kb);
        float kmx = *(const AS3(float)*)(L + L_Q + 10240 + lane * 4);
        if (hh) { qa1 = u32x2{0u, 0u}; qb1 = u32x2{0u, 0u}; }
        const bf16x8 QA0 = __builtin_bit_cast(bf16x8, qa0), QB0 = __builtin_bit_cast(bf16x8, qb0);
        float nba, nbb;
        {
            kmx = row_max16(kmx);
            auto r = __builtin_amdgcn_permlane16_swap(__float_as_uint(kmx), __float_as_uint(kmx), false, false);
            kmx = fmaxf(__uint_as_float(r[0]), __uint_as_float(r[1]));
            const float kn = -1.01f * __builtin_amdgcn_sqrtf(xhalf_sum(kmx));
            nba = kn * __builtin_amdgcn_sqrtf(qnorm2(qa0, qa1)); nbb = kn * __builtin_amdgcn_sqrtf(qnorm2(qb0, qb1));
        }
        for (int attempt = 0;; ++attempt) {
            f32x16 oa, ob, sA, sB;
#pragma unroll
            for (int r = 0; r < 16; ++r) { oa[r] = 0.f; ob[r] = 0.f; sB[r] = 0.f; }
            const s16x4 QA1 = __builtin_bit_cast(s16x4, hh ? split3(nba) : qa1), QB1 = __builtin_bit_cast(s16x4, hh ? split3(nbb) : qb1);
            lds_cptr pka = L + kh * SLOT + ka, pkb = L + kh * SLOT + kb2, pv = L + kh * SLOT + va, pv2 = L + kh * SLOT + vb2;
            KFrag k0 = ld_k(pka, pkb, 0), k1 = ld_k(pka, pkb, KVT);
            VFrag v0 = ld_v(pv, 0), v1 = v0;
            sA = MFMA32(k0.k0, QA0, f32x16{}); sA = MFMA32K8(k0.k1, QA1, sA);
            for (int c = 0; c < 4; ++c) {
                const int dnext = (c & 1) ? -2 * SLOT : 2 * SLOT;
#define K2_PAIR(t, LOADK0, LOADV0, LOADK1) do { \
                bf16x8 vp = v1.v1; \
                v1 = ld_v(pv, ((t) + 1) * KVT); SBAR(); \
                attn_step(sA, sB, k0, vp, v0.v0, QB0, QB1, ob, oa); \
                LOADK0; SBAR(); \
                attn_step(sB, sA, k1, v0.v1, v0.v0, QA0, QA1, oa, ob); \
                vp = v0.v1; \
                LOADV0; SBAR(); \
                attn_step(sA, sB, k1, vp, v1.v0, QB0, QB1, ob, oa); \
                LOADK1; SBAR(); \
                attn_step(sB, sA, k0, v1.v1, v1.v0, QA0, QA1, oa, ob); } while (0)
#define PRIO(p) do { asm volatile("s_cmp_eq_u32 %0, 0\n\ts_cbranch_scc1 1f\n\ts_setprio " #p "\n1:" :: "s"(kh) : "scc"); SBAR(); } while (0)
                K2_PAIR(0, k0 = ld_k(pka, pkb, 2 * KVT), v0 = ld_v(pv, 2 * KVT), k1 = ld_k(pka, pkb, 3 * KVT));
                K2_PAIR(2, k0 = ld_k(pka, pkb, 4 * KVT), v0 = ld_v(pv, 4 * KVT), k1 = ld_k(pka, pkb, 5 * KVT));
                PRIO(0);
                K2_PAIR(4, k0 = ld_k(pka, pkb, 6 * KVT), v0 = ld_v(pv, 6 * KVT), k1 = ld_k(pka, pkb, 7 * KVT));
#define TURN() do { asm volatile("s_cmp_eq_u32 %0, 3\n\ts_cbranch_scc1 1f\n\ts_waitcnt vmcnt(0) lgkmcnt(0)\n\ts_barrier\n1:" :: "s"(c) : "memory", "scc"); \
                    pka += dnext; pkb += dnext; pv += dnext; pv2 += dnext; k0 = ld_k(pka, pkb, 0); PRIO(1); } while (0)
                K2_PAIR(6, TURN(), v0 = ld_v(pv, 0), k1 = ld_k(pka, pkb, KVT));
            }
            ob = SMF(v1.v1, HI8(sB), ob);
            PRIO(0);
            {
                lds_ptr d = (lds_ptr)lds + L_X + (own ^ 1) * 3072 + l16;
#pragma unroll
                for (int a = 0; a < 3; ++a)
                    *(AS3(f32x4)*)(d + a * 1024) = kh ? f32x4{oa[4 * a], oa[4 * a + 1], oa[4 * a + 2], oa[4 * a + 3]} : f32x4{ob[4 * a], ob[4 * a + 1], ob[4 * a + 2], ob[4 * a + 3]};
            }
            RAW_BAR();
            float o[12];
#pragma unroll
            for (int a = 0; a < 3; ++a) {
                const f32x4 t = *(const AS3(f32x4)*)(L + L_X + own * 3072 + l16 + a * 1024);
#pragma unroll
                for (int r = 0; r < 4; ++r) o[4 * a + r] = t[r] + (kh ? ob[4 * a + r] : oa[4 * a + r]);
            }
            auto rr = __builtin_amdgcn_permlane32_swap(__float_as_uint(o[8]), __float_as_uint(o[8]), false, false);
            const float lsum = __uint_as_float(rr[1]);
            const float sa = *(const AS3(float)*)(L + L_GATE + (own * 32 + r32) * 4) * __builtin_amdgcn_rcpf(lsum);
            const unsigned ocb = WS_OC + ((qt * 8 + own) * 32 + r32) * 16;
            ST128((u32x4{cvtpk(o[0] * sa, o[1] * sa), cvtpk(o[2] * sa, o[3] * sa), cvtpk(o[4] * sa, o[5] * sa), cvtpk(o[6] * sa, o[7] * sa)}), ocb + (2 * h + hh) * (SEQ * 16));
            if (!hh) ST64((u32x2{cvtpk(o[8] * sa, o[9] * sa), cvtpk(o[10] * sa, o[11] * sa)}), ocb + (64 + (h >> 1)) * (SEQ * 16) + (h & 1) * 8);
            if (attempt == 0 && __any(!(lsum >= 5.4210109e-20f)))
                *(AS3(unsigned)*)((lds_ptr)lds + L_FLAG) = 1u;
            WAIT_VM(0);
            RAW_BAR();
            const unsigned redo = *(const AS3(unsigned)*)(L + L_FLAG);
            if (__builtin_amdgcn_readfirstlane(redo) == 0u) break;
            float ma = -__builtin_inff(), mb = -__builtin_inff();
            for (int t = 0; t < 64; ++t) {
                const u32x4 g0 = LD128(WS_KV + (h * 64 + t) * KVT + ka);
                const u32x2 g1 = LD64(WS_KV + (h * 64 + t) * KVT + kb2);
                f32x16 s = MFMA32(__builtin_bit_cast(bf16x8, g0), QA0, f32x16{}); s = MFMA32K8(__builtin_bit_cast(s16x4, g1), __builtin_bit_cast(s16x4, qa1), s);
                f32x16 u = MFMA32(__builtin_bit_cast(bf16x8, g0), QB0, f32x16{}); u = MFMA32K8(__builtin_bit_cast(s16x4, g1), __builtin_bit_cast(s16x4, qb1), u);
#pragma unroll
                for (int r = 0; r < 16; ++r) { ma = fmaxf(ma, s[r]); mb = fmaxf(mb, u[r]); }
            }
            nba = -xhalf_max(ma); nbb = -xhalf_max(mb);
            RAW_BAR();
            BAR();
        }
        {
            const int mt = wid & 3, kp = wid >> 2;
            const unsigned ofo = WS_OC + ((4 * kp + g) * SEQ + tt * 64 + 16 * mt + l15) * 16;
            const lds_cptr wa = L + g * 1280 + (20 * q4 + p4) * 16;
            f32x4 acc[5];
#pragma unroll
            for (int i = 0; i < 5; ++i) acc[i] = f32x4{0.f, 0.f, 0.f, 0.f};
            u32x4 of[10];
#define P3_MMA(br) do { if (kp) { _Pragma("unroll") for (int k = 0; k < 10; ++k) _Pragma("unroll") for (int i = 0; i < 5; ++i) \
                                      acc[i] = MFMA16(LDS_B128(wa + p3_off(br, 2 * k + 1) + i * 64), __builtin_bit_cast(bf16x8, of[k]), acc[i]); } \
                        else { _Pragma("unroll") for (int k = 0; k < 10; ++k) _Pragma("unroll") for (int i = 0; i < 5; ++i) \
                                      acc[i] = MFMA16(LDS_B128(wa + p3_off(br, 2 * k) + i * 64), __builtin_bit_cast(bf16x8, of[k]), acc[i]); } } while (0)
#pragma unroll
            for (int k = 0; k < 10; ++k) of[k] = LD128(ofo + (80 + 8 * k) * (SEQ * 16));
            BAR();
            P3_MMA(1);
            RAW_BAR();
            for (int ks = 7 + wid; ks < 20; ks += 8) P3_WKS(0, ks, ks < 17 ? L_RA + (ks - 7) * 5120 : L_RB + (ks - 17) * 5120);
            BAR();
#pragma unroll
            for (int k = 0; k < 10; ++k) of[k] = LD128(ofo + (8 * k) * (SEQ * 16));
            WAIT_VM(10);
            BAR();
            P3_MMA(0);
            if (kp) {
#pragma unroll
                for (int i = 0; i < 5; ++i) *(AS3(f32x4)*)((lds_ptr)lds + L_CX + (mt * 5 + i) * 1024 + l16) = acc[i];
            }
            RAW_BAR();
            if (!kp) {
                char* dst = (char*)out + (size_t)(tt * 64 + 16 * mt + l15) * 1280 + ct * 160 + g * 40;
#pragma unroll
                for (int i = 0; i < 5; ++i) {
                    const f32x4 v = acc[i] + *(const AS3(f32x4)*)(L + L_CX + (mt * 5 + i) * 1024 + l16);
                    *(u32x2*)(dst + i * 8) = u32x2{cvtpk(v[0], v[1]), cvtpk(v[2], v[3])};
                }
            }
        }
    }
    if (bid == 0 && tid == 512) publish(ws, WS_EPOCH, tag);
}

extern "C" void kernel_launch(void* const* d_in, const int* in_sizes, int n_in, void* d_out, int out_size, void* d_ws, size_t ws_size,
                              hipStream_t stream) {
    fused_kernel<<<256, 768, 0, stream>>>((const uint16_t*)d_in[0], (const uint16_t*)d_in[1], (const uint16_t*)d_in[4], (const uint16_t*)d_in[2],
                                          (const uint16_t*)d_in[6], (const uint16_t*)d_in[5], (const uint16_t*)d_in[7], (char*)d_ws, (uint16_t*)d_out);
}
```
